```python
import torch

def _softmax(x, axis=-1):
    return torch.softmax(x, dim=axis)

def _gelu(x):
    return torch.nn.functional.gelu(x, approximate='tanh')

def _silu(x):
    return torch.nn.functional.silu(x)

def _sigmoid(x):
    return torch.sigmoid(x)

def _layernorm(x, axis=-1, eps=1e-05):
    m = x.mean(dim=axis, keepdim=True)
    v = x.var(dim=axis, keepdim=True, unbiased=False)
    return (x - m) / torch.sqrt(v + eps)

def _scatter_add(tgt, idx, src, axis=0):
    return tgt.clone().index_add_(axis, idx, src)

def _segment_sum(src, seg, n):
    out = torch.zeros((n,) + src.shape[1:], dtype=src.dtype)
    return out.index_add_(0, seg, src)

def _pad(x, pw):
    flat = tuple((v for pair in reversed(pw) for v in pair))
    return torch.nn.functional.pad(x, flat)
kernel_name = 'interleave_layers__spectral_fnet__mixer_gmlp__prompt'

def setup_inputs():

    _w = lambda a: (lambda t: t.to(torch.bfloat16) if t.is_floating_point() else t)(torch.as_tensor(a))

    def _A_setup():
        B, S, D = (1, 2048, 1024)
        ns = torch.arange(S)[:, None] * torch.arange(S)[None, :]
        nd = torch.arange(D)[:, None] * torch.arange(D)[None, :]
        Ws_re = torch.cos(-2.0 * torch.pi * ns / S).to(float)
        Ws_im = torch.sin(-2.0 * torch.pi * ns / S).to(float)
        Wd_re = torch.cos(-2.0 * torch.pi * nd / D).to(float)
        Wd_im = torch.sin(-2.0 * torch.pi * nd / D).to(float)
        return {'x': _w((lambda *s: torch.randn(*s, dtype=torch.bfloat16) * 0.05)(B, S, D)), 'dft_seq_re': _w(Ws_re), 'dft_seq_im': _w(Ws_im), 'dft_hid_re': _w(Wd_re), 'dft_hid_im': _w(Wd_im)}
    _A_in = _A_setup()

    def _B_setup():
        B, S, D, F = (1, 2048, 1024, 3584)
        Fh = F // 2
        return {'x': _w((lambda *s: torch.randn(*s, dtype=torch.bfloat16) * 0.05)(B, S, D)), 'w_in': _w((lambda *s: torch.randn(*s, dtype=torch.bfloat16) * 1.6)(D, 2 * Fh)), 'w_sp': _w(0.01 * (lambda *s: torch.randn(*s, dtype=torch.bfloat16) * 1.6)(S, S)), 'b_sp': _w(1.0 + 0.01 * (lambda *s: torch.randn(*s, dtype=torch.bfloat16) * 1.6)(S)), 'w_out': _w((lambda *s: torch.randn(*s, dtype=torch.bfloat16) * 1.6)(Fh, D))}
    _B_in = _B_setup()
    return {'A_x': _A_in['x'], 'A_dft_seq_re': _A_in['dft_seq_re'], 'A_dft_seq_im': _A_in['dft_seq_im'], 'A_dft_hid_re': _A_in['dft_hid_re'], 'A_dft_hid_im': _A_in['dft_hid_im'], 'B_x': _B_in['x'], 'B_w_in': _B_in['w_in'], 'B_w_sp': _B_in['w_sp'], 'B_b_sp': _B_in['b_sp'], 'B_w_out': _B_in['w_out']}

def reference(A_x, A_dft_seq_re, A_dft_seq_im, A_dft_hid_re, A_dft_hid_im, B_x, B_w_in, B_w_sp, B_b_sp, B_w_out):

    def _A_ref(x, dft_seq_re, dft_seq_im, dft_hid_re, dft_hid_im):
        a = torch.einsum('bsd,ed->bse', x, dft_hid_re)
        b = torch.einsum('bsd,ed->bse', x, dft_hid_im)
        y = torch.einsum('ts,bse->bte', dft_seq_re, a) - torch.einsum('ts,bse->bte', dft_seq_im, b)
        return y

    def _B_ref(x, w_in, w_sp, b_sp, w_out):
        B, S, D = x.shape
        F = w_in.shape[-1]
        Fh = F // 2
        z = _gelu(torch.einsum('bsd,df->bsf', x, w_in))
        z1 = z[..., :Fh].clone()
        z2 = _layernorm(z[..., Fh:], axis=-1)
        g = torch.einsum('ts,bsf->btf', w_sp, z2) + b_sp[None, :, None]
        return torch.einsum('bsf,fd->bsd', z1 * g, w_out)
    x = A_x
    x = x + _A_ref(_layernorm(x, axis=-1), A_dft_seq_re, A_dft_seq_im, A_dft_hid_re, A_dft_hid_im)
    x = x + _B_ref(_layernorm(x, axis=-1), B_w_in, B_w_sp, B_b_sp, B_w_out)
    x = x + _A_ref(_layernorm(x, axis=-1), A_dft_seq_re, A_dft_seq_im, A_dft_hid_re, A_dft_hid_im)
    x = x + _B_ref(_layernorm(x, axis=-1), B_w_in, B_w_sp, B_b_sp, B_w_out)
    return x
```

```cpp
#include <hip/hip_runtime.h>
#include <hip/hip_bf16.h>
#include <stdint.h>

#define LAS __attribute__((address_space(3)))
typedef unsigned short u16;
typedef short s16x4 __attribute__((ext_vector_type(4)));
typedef short s16x8 __attribute__((ext_vector_type(8)));
typedef __bf16 bf16x8 __attribute__((ext_vector_type(8)));
typedef __bf16 bf16x2 __attribute__((ext_vector_type(2)));
typedef float f32x2 __attribute__((ext_vector_type(2)));
typedef float f32x4 __attribute__((ext_vector_type(4)));
typedef unsigned u32x2 __attribute__((ext_vector_type(2)));
typedef unsigned u32x4 __attribute__((ext_vector_type(4)));

constexpr int S_ = 2048, D_ = 1024, F_ = 3584, FH = 1792;
constexpr float LN_EPS = 1e-5f;

__device__ __forceinline__ unsigned pack_bf16(float lo, float hi) {
  f32x2 v = {lo, hi};
  return __builtin_bit_cast(unsigned, __builtin_convertvector(v, bf16x2));
}
__device__ __forceinline__ float bf_lo(unsigned u) { return __builtin_bit_cast(float, u << 16); }
__device__ __forceinline__ float bf_hi(unsigned u) { return __builtin_bit_cast(float, u & 0xffff0000u); }
__device__ __forceinline__ float xor16(float v) { return __shfl_xor(v, 16); }
__device__ __forceinline__ float xor32(float v) { return __shfl_xor(v, 32); }
__device__ __forceinline__ float gelu_tanh(float x) {
  const float c1 = -2.0f * 0.7978845608028654f * 1.4426950408889634f, c2 = c1 * 0.044715f;
  float t = x * __builtin_fmaf(x * x, c2, c1);
  float e = __builtin_amdgcn_exp2f(t);
  return x * __builtin_amdgcn_rcpf(1.0f + e);
}
#define WAIT_V(n) asm volatile("s_waitcnt vmcnt(%0)" ::"n"(n) : "memory")
#define WAIT_L0() asm volatile("s_waitcnt lgkmcnt(0)" ::: "memory")
#define BARRIER() do { asm volatile("" ::: "memory"); __builtin_amdgcn_s_barrier(); asm volatile("" ::: "memory"); } while (0)

enum { EPI_G1 = 0, EPI_G2 = 1, EPI_K3 = 2, EPI_K4 = 3, EPI_K5 = 4 };

struct Args {
  const char* A;
  const char* A2;
  const char* B;
  const char* B2;
  char* O;
  char* O2;
  const char* R;
  const float* stat_in;
  float* stat_out;
  const float* csum;
  const u16* bias;
  unsigned long long* xch;
};

template <int EPI_, int BM_, int BN_, int WM_, int WN_, int AM_, int BMODE_, int K_, int NS_, int GM_, int GN_, int XM_, int XN_, int PA_, int PB_, int FLAG_>
struct Cfg {
  static constexpr int EPI = EPI_, BM = BM_, BN = BN_, WM = WM_, WN = WN_, AM = AM_, BMODE = BMODE_, K = K_, NS = NS_;
  static constexpr int GM = GM_, GN = GN_, XM = XM_, XN = XN_, PA = PA_, PB = PB_, FLAG = FLAG_;
  static constexpr int MT = BM / WM / 16, NT = BN / WN / 16;
  static constexpr int NPA = BM / 8, NPB = BN / 8, NP = NPA + NPB;
  static constexpr int ABYTES = BM * 128, STAGE = (BM + BN) * 128, T = K / 64;
  static constexpr int LDS = NS * STAGE;
};

template <class C>
__global__ __launch_bounds__(512, 2) void gemm_k(Args a) {
  extern __shared__ __attribute__((aligned(16))) char smem_[];
  LAS char* smem = (LAS char*)smem_;
  constexpr int BM = C::BM, BN = C::BN, MT = C::MT, NT = C::NT, NS = C::NS, T = C::T, STAGE = C::STAGE;
  const int tid = threadIdx.x, lane = tid & 63;
  const int wid = __builtin_amdgcn_readfirstlane(tid >> 6);
  const int xcd = blockIdx.x & 7, idx = blockIdx.x >> 3;
  constexpr int RN = C::GN / C::XN;
  const int lm = idx / C::XN, ln = idx % C::XN;
  const int bm = (xcd / RN) * C::XM + lm, bn = (xcd % RN) * C::XN + ln;
  const int cls = (lm + ln) & 3;

  if (wid >= 4) {
    const int w = wid - 4;
    constexpr int PPW = (C::NP + 3) / 4;
    const char* src[PPW];
    unsigned kmul[PPW];
#pragma unroll
    for (int i = 0; i < PPW; ++i) {
      int p = w + 4 * i;
      if (p >= C::NP) p = C::NP - 1;
      if (4 * i < C::NPA) {
        if (C::AM == 0) {
          int row = p * 8 + (lane >> 3);
          src[i] = a.A + (size_t)(bm * BM + row) * C::PA + (((lane & 7) ^ ((row >> 1) & 7)) << 4);
          kmul[i] = 128;
        } else {
          src[i] = a.A + ((size_t)(bm * (BM / 16) + (p >> 1)) * C::PA + (p & 1)) * 1024 + lane * 16;
          kmul[i] = 2048;
        }
      } else {
        int q = p - C::NPA;
        if (C::BMODE == 0) {
          int row = q * 8 + (lane >> 3);
          const char* base = a.B; int n0 = bn * BN;
          if (C::EPI == EPI_G1 && n0 >= D_) { base = a.B2; n0 -= D_; }
          src[i] = base + (size_t)(n0 + row) * C::PB + (((lane & 7) ^ ((row >> 1) & 7)) << 4);
          kmul[i] = 128;
        } else if (C::BMODE == 1) {
          int cg = q >> 3, k = (q & 7) * 8 + (lane >> 3);
          int f = (((k >> 1) & 1) << 1) | ((k >> 3) & 1);
          int col = bn * BN + cg * 64;
          if (C::EPI == EPI_K3) col = bn * 128 + (cg & 1) * 64 + (cg >> 1) * FH;
          src[i] = a.B + (size_t)k * C::PB + col * 2 + (((lane & 7) ^ (f << 1)) << 4);
          kmul[i] = 64 * C::PB;
        } else {
          int kk = q / (BN / 16), c = q % (BN / 16);
          src[i] = a.B + ((size_t)kk * C::PB + bn * (BN / 16) + c) * 1024 + lane * 16;
          kmul[i] = 2 * C::PB * 1024;
        }
      }
    }
    const bool full = (C::NP % 4 == 0) || (w < C::NP % 4);
    auto issue = [&](int s) {
      int ch = s;
      if ((s & 3) == cls) { ch = s + 4; if (ch >= T) ch -= T; }
      const unsigned slot = (unsigned)(s % NS) * STAGE;
#pragma unroll
      for (int i = 0; i < PPW; ++i) {
        if (i == PPW - 1 && !full) break;
        const char* g = src[i] + (size_t)ch * kmul[i];
        if (C::EPI == EPI_G2 && 4 * i < C::NPA && ch >= T / 2) g = src[i] + (a.A2 - a.A) + (size_t)(ch - T / 2) * kmul[i];
        __builtin_amdgcn_global_load_lds((const unsigned*)g, (LAS unsigned*)(smem + slot + (w + 4 * i) * 1024), 16, 0, 0);
      }
    };
#pragma unroll
    for (int s = 0; s < NS - 1; ++s) issue(s);
    if (full) WAIT_V((NS - 2) * PPW); else WAIT_V((NS - 2) * (PPW - 1));
    for (int s = 0; s < T; ++s) {
      BARRIER();
      if (s + NS - 1 < T) {
        issue(s + NS - 1);
        if (full) WAIT_V((NS - 2) * PPW); else WAIT_V((NS - 2) * (PPW - 1));
      } else {
        WAIT_V(0);
      }
    }
    return;
  }

  const int wm = wid / C::WN, wn = wid % C::WN;
  const int r = lane & 15, j = lane >> 4;
  const int m0 = bm * BM + wm * (MT * 16);

  float mean[MT], rstd[MT];
  if (C::EPI == EPI_G1 || C::EPI == EPI_K3) {
#pragma unroll
    for (int mt = 0; mt < MT; ++mt) {
      const f32x4* p = (const f32x4*)(a.stat_in + (size_t)(m0 + mt * 16 + r) * 32 + j * 8);
      f32x4 u = p[0], v = p[1];
      float s = (u[0] + u[2]) + (v[0] + v[2]);
      s += xor16(s); s += xor32(s);
      float mu = s * (1.0f / 16.0f);
      float d0 = u[0] - mu, d1 = u[2] - mu, d2 = v[0] - mu, d3 = v[2] - mu;
      float q = (u[1] + u[3]) + (v[1] + v[3]) + 64.0f * ((d0 * d0 + d1 * d1) + (d2 * d2 + d3 * d3));
      q += xor16(q); q += xor32(q);
      mean[mt] = mu; rstd[mt] = 1.0f / sqrtf(q * (1.0f / D_) + LN_EPS);
    }
  }

  unsigned aoff[2], boff[4];
  if (C::AM == 0) { aoff[0] = (wm * MT * 16 + r) * 128 + ((j ^ (r >> 1)) << 4); aoff[1] = aoff[0] ^ 64; }
  else { aoff[0] = wm * MT * 2048 + lane * 16; aoff[1] = aoff[0] + 1024; }
  if (C::BMODE == 0) { boff[0] = C::ABYTES + (wn * NT * 16 + r) * 128 + ((j ^ (r >> 1)) << 4); boff[1] = boff[0] ^ 64; boff[2] = boff[3] = 0; }
  else if (C::BMODE == 1) {
    const int q = (lane >> 2) & 3, p = lane & 3, f = ((q >> 1) << 1) | (j & 1);
#pragma unroll
    for (int c = 0; c < 4; ++c) boff[c] = C::ABYTES + (j * 8 + q) * 128 + ((c ^ f) << 5) + p * 8;
  } else { boff[0] = C::ABYTES + wn * NT * 1024 + lane * 8; boff[1] = boff[2] = boff[3] = 0; }

  f32x4 acc[MT][NT];
#pragma unroll
  for (int mt = 0; mt < MT; ++mt)
#pragma unroll
    for (int nt = 0; nt < NT; ++nt) acc[mt][nt] = (f32x4){0.f, 0.f, 0.f, 0.f};

  auto rdA = [&](s16x8 (&fa)[MT], unsigned slot, int kk) {
#pragma unroll
    for (int mt = 0; mt < MT; ++mt) fa[mt] = *(const LAS s16x8*)(smem + slot + aoff[kk] + mt * 2048);
  };
  auto rdB = [&](s16x8 (&fb)[NT], unsigned slot, int kk) {
#pragma unroll
    for (int nt = 0; nt < NT; ++nt) {
      if (C::BMODE == 0) fb[nt] = *(const LAS s16x8*)(smem + slot + boff[kk] + nt * 2048);
      else {
        unsigned o;
        if (C::BMODE == 1) { const int c = wn * NT + nt; o = slot + boff[(NT % 4 == 0) ? (nt & 3) : 0] + kk * 4096 + (c >> 2) * 8192; }
        else o = slot + boff[0] + (kk * (BN / 16) + nt) * 1024;
        s16x4 lo = __builtin_amdgcn_ds_read_tr16_b64_v4i16((LAS s16x4*)(smem + o));
        s16x4 hi = __builtin_amdgcn_ds_read_tr16_b64_v4i16((LAS s16x4*)(smem + o + 512));
        fb[nt] = __builtin_shufflevector(lo, hi, 0, 1, 2, 3, 4, 5, 6, 7);
      }
    }
  };
  static_assert(C::BMODE != 1 || NT % 4 == 0, "");
  auto mma = [&](const s16x8 (&fa)[MT], const s16x8 (&fb)[NT]) {
#pragma unroll
    for (int nt = 0; nt < NT; ++nt)
#pragma unroll
      for (int mt = 0; mt < MT; ++mt)
        acc[mt][nt] = __builtin_amdgcn_mfma_f32_16x16x32_bf16(__builtin_bit_cast(bf16x8, fb[nt]), __builtin_bit_cast(bf16x8, fa[mt]), acc[mt][nt], 0, 0, 0);
  };

  s16x8 fa0[MT], fb0[NT], fa1[MT], fb1[NT];
  BARRIER();
  rdA(fa0, 0, 0); rdB(fb0, 0, 0);
  rdA(fa1, 0, 1); rdB(fb1, 0, 1);
  mma(fa0, fb0);
  WAIT_L0();
  unsigned slot = 0;
  for (int s = 1; s < T; ++s) {
    slot += STAGE; if (slot == NS * STAGE) slot = 0;
    BARRIER();
    rdA(fa0, slot, 0); rdB(fb0, slot, 0);
    mma(fa1, fb1);
    rdA(fa1, slot, 1); rdB(fb1, slot, 1);
    mma(fa0, fb0);
    WAIT_L0();
  }
  mma(fa1, fb1);

  const int n0 = bn * BN + wn * (NT * 16);
  if (C::EPI == EPI_G1) {
    const int half = n0 >= D_, nn = n0 - half * D_;
    const float sg = half ? -1.0f : 1.0f;
#pragma unroll
    for (int nt = 0; nt < NT; ++nt) {
      const f32x4 cs = *(const f32x4*)(a.csum + n0 + nt * 16 + j * 4);
#pragma unroll
      for (int mt = 0; mt < MT; ++mt) {
        const float rs = rstd[mt] * sg; f32x4 v = (acc[mt][nt] - mean[mt] * cs) * rs;
        const int k = half * S_ + m0 + mt * 16;
        char* dst = a.O + ((size_t)(k >> 5) * (D_ / 16) + ((nn >> 4) + nt)) * 1024 + ((r >> 2) & 1) * 512 + (((k >> 3) & 2) + (r >> 3)) * 128 + (r & 3) * 32 + j * 8;
        *(u32x2*)dst = (u32x2){pack_bf16(v[0], v[1]), pack_bf16(v[2], v[3])};
      }
    }
  } else if (C::EPI == EPI_G2 || C::EPI == EPI_K5) {
    static_assert(C::EPI != EPI_G2 && C::EPI != EPI_K5 || (C::WN == 1 && C::BN == 64), "");
#pragma unroll
    for (int mt = 0; mt < MT; ++mt) {
      const int m = m0 + mt * 16;
      float o[NT][4]; float s = 0.f;
#pragma unroll
      for (int nt = 0; nt < NT; ++nt) {
        const int n = n0 + nt * 16;
        const size_t img = ((size_t)(m >> 4) * (D_ / 32) + (n >> 5)) * 1024 + (((n >> 3) & 2) + (j >> 1)) * 256 + r * 16 + (j & 1) * 8;
        const size_t rm = ((size_t)(m + r) * D_ + n + j * 4) * 2;
        const u32x2 x = *(const u32x2*)(a.R + ((C::FLAG & 1) ? rm : img));
        const u32x2 y = {pack_bf16(acc[mt][nt][0] + bf_lo(x[0]), acc[mt][nt][1] + bf_hi(x[0])), pack_bf16(acc[mt][nt][2] + bf_lo(x[1]), acc[mt][nt][3] + bf_hi(x[1]))};
        *(u32x2*)(a.O + ((C::FLAG & 2) ? rm : img)) = y;
        o[nt][0] = bf_lo(y[0]); o[nt][1] = bf_hi(y[0]); o[nt][2] = bf_lo(y[1]); o[nt][3] = bf_hi(y[1]);
        s += (o[nt][0] + o[nt][1]) + (o[nt][2] + o[nt][3]);
      }
      if (!(C::FLAG & 2)) {
        s += xor16(s); s += xor32(s);
        const float mu = s * (1.0f / 64.0f); float q = 0.f;
#pragma unroll
        for (int nt = 0; nt < NT; ++nt)
#pragma unroll
          for (int e = 0; e < 4; ++e) { const float d = o[nt][e] - mu; q = __builtin_fmaf(d, d, q); }
        q += xor16(q); q += xor32(q);
        if (j == 0) *(f32x2*)(a.stat_out + (size_t)(m + r) * 32 + bn * 2) = (f32x2){mu, q};
      }
    }
  } else if (C::EPI == EPI_K3) {
    const int f0 = bn * 128;
    unsigned z[MT][NT][2];
#pragma unroll
    for (int nt = 0; nt < NT; ++nt) {
      const f32x4 cs = *(const f32x4*)(a.csum + wn * FH + f0 + nt * 16 + j * 4);
#pragma unroll
      for (int mt = 0; mt < MT; ++mt) {
        f32x4 v = (acc[mt][nt] - mean[mt] * cs) * rstd[mt];
        z[mt][nt][0] = pack_bf16(gelu_tanh(v[0]), gelu_tanh(v[1]));
        z[mt][nt][1] = pack_bf16(gelu_tanh(v[2]), gelu_tanh(v[3]));
      }
    }
    if (wn == 0) {
#pragma unroll
      for (int mt = 0; mt < MT; ++mt)
#pragma unroll
        for (int nt = 0; nt < NT; ++nt) {
          const int m = m0 + mt * 16, n = f0 + nt * 16;
          const size_t img = ((size_t)(m >> 4) * (FH / 32) + (n >> 5)) * 1024 + (((n >> 3) & 2) + (j >> 1)) * 256 + r * 16 + (j & 1) * 8;
          *(u32x2*)(a.O + img) = (u32x2){z[mt][nt][0], z[mt][nt][1]};
        }
    } else {
#pragma unroll
      for (int mt = 0; mt < MT; ++mt) {
        float s = 0.f;
#pragma unroll
        for (int nt = 0; nt < NT; ++nt) s += (bf_lo(z[mt][nt][0]) + bf_hi(z[mt][nt][0])) + (bf_lo(z[mt][nt][1]) + bf_hi(z[mt][nt][1]));
        s += xor16(s); s += xor32(s);
        const float mu = s * (1.0f / 128.0f); float q = 0.f;
#pragma unroll
        for (int nt = 0; nt < NT; ++nt) {
          float d;
          d = bf_lo(z[mt][nt][0]) - mu; q = __builtin_fmaf(d, d, q); d = bf_hi(z[mt][nt][0]) - mu; q = __builtin_fmaf(d, d, q);
          d = bf_lo(z[mt][nt][1]) - mu; q = __builtin_fmaf(d, d, q); d = bf_hi(z[mt][nt][1]) - mu; q = __builtin_fmaf(d, d, q);
        }
        q += xor16(q); q += xor32(q);
        unsigned qb = __builtin_bit_cast(unsigned, q);
        if (qb == 0xffffffffu) qb = 0x7fc00000u;
        if (j == 0) __hip_atomic_store(a.xch + (size_t)(m0 + mt * 16 + r) * 16 + bn, ((unsigned long long)qb << 32) | __builtin_bit_cast(unsigned, mu), __ATOMIC_RELAXED, __HIP_MEMORY_SCOPE_AGENT);
      }
      unsigned long long g[MT][4];
      unsigned long long* xrow = a.xch + (size_t)(m0 + r) * 16 + j * 4;
      for (unsigned spin = 0;; ++spin) {
        bool ok = true;
#pragma unroll
        for (int mt = 0; mt < MT; ++mt)
#pragma unroll
          for (int i = 0; i < 4; ++i) {
            g[mt][i] = __hip_atomic_load(xrow + mt * 256 + i, __ATOMIC_RELAXED, __HIP_MEMORY_SCOPE_AGENT);
            ok &= (unsigned)(g[mt][i] >> 32) != 0xffffffffu || j * 4 + i >= 14;
          }
        if (__all(ok) || spin > (1u << 22)) break;
        __builtin_amdgcn_s_sleep(1);
      }
#pragma unroll
      for (int mt = 0; mt < MT; ++mt) {
        float mu_i[4], q_i[4], s = 0.f;
#pragma unroll
        for (int i = 0; i < 4; ++i) {
          const bool valid = j * 4 + i < 14;
          mu_i[i] = __builtin_bit_cast(float, (unsigned)g[mt][i]); q_i[i] = valid ? __builtin_bit_cast(float, (unsigned)(g[mt][i] >> 32)) : 0.f;
          s += valid ? mu_i[i] : 0.f;
        }
        s += xor16(s); s += xor32(s);
        const float mu = s * (1.0f / 14.0f); float q = 0.f;
#pragma unroll
        for (int i = 0; i < 4; ++i) { const float d = (j * 4 + i < 14) ? mu_i[i] - mu : 0.f; q += q_i[i] + 128.0f * d * d; }
        q += xor16(q); q += xor32(q);
        const float rs = 1.0f / sqrtf(q * (1.0f / FH) + LN_EPS);
        const int k = m0 + mt * 16;
#pragma unroll
        for (int nt = 0; nt < NT; ++nt) {
          char* dst = a.O2 + ((size_t)(k >> 5) * (FH / 16) + ((f0 >> 4) + nt)) * 1024 + ((r >> 2) & 1) * 512 + (((k >> 3) & 2) + (r >> 3)) * 128 + (r & 3) * 32 + j * 8;
          *(u32x2*)dst = (u32x2){pack_bf16((bf_lo(z[mt][nt][0]) - mu) * rs, (bf_hi(z[mt][nt][0]) - mu) * rs), pack_bf16((bf_lo(z[mt][nt][1]) - mu) * rs, (bf_hi(z[mt][nt][1]) - mu) * rs)};
        }
      }
    }
  } else if (C::EPI == EPI_K4) {
#pragma unroll
    for (int mt = 0; mt < MT; ++mt) {
      const int m = m0 + mt * 16;
      const float b = __builtin_bit_cast(float, (unsigned)a.bias[m + r] << 16);
#pragma unroll
      for (int nt = 0; nt < NT; ++nt) {
        const int n = n0 + nt * 16;
        const size_t img = ((size_t)(m >> 4) * (FH / 32) + (n >> 5)) * 1024 + (((n >> 3) & 2) + (j >> 1)) * 256 + r * 16 + (j & 1) * 8;
        const u32x2 x = *(const u32x2*)(a.R + img);
        *(u32x2*)(a.O + img) = (u32x2){pack_bf16((acc[mt][nt][0] + b) * bf_lo(x[0]), (acc[mt][nt][1] + b) * bf_hi(x[0])), pack_bf16((acc[mt][nt][2] + b) * bf_lo(x[1]), (acc[mt][nt][3] + b) * bf_hi(x[1]))};
      }
    }
  }
}

__global__ __launch_bounds__(256) void prologue_k(const u16* x, const u16* wdre, const u16* wdim, const u16* win, float* stat, float* wsum, float* csum, u32x4* xch) {
  __shared__ float red[32][65];
  const int tid = threadIdx.x, lane = tid & 63, wid = tid >> 6, b = blockIdx.x;
  if (b < 56) {
    const int ch = tid & 7, rg = tid >> 3;
    float s[8] = {0.f, 0.f, 0.f, 0.f, 0.f, 0.f, 0.f, 0.f};
#pragma unroll 8
    for (int k = rg; k < D_; k += 32) {
      const u32x4 v = *(const u32x4*)(win + (size_t)k * F_ + b * 64 + ch * 8);
#pragma unroll
      for (int e = 0; e < 4; ++e) { s[2 * e] += bf_lo(v[e]); s[2 * e + 1] += bf_hi(v[e]); }
    }
#pragma unroll
    for (int e = 0; e < 8; ++e) red[rg][ch * 8 + e] = s[e];
    __syncthreads();
    if (tid < 64) { float t = 0.f; for (int i = 0; i < 32; ++i) t += red[i][tid]; csum[b * 64 + tid] = t; }
    return;
  }
  const int gw = (b - 56) * 4 + wid;
  if (gw < 4096) {
    const u16* row = gw < 2048 ? x + (size_t)gw * D_ : (gw < 3072 ? wdre + (size_t)(gw - 2048) * D_ : wdim + (size_t)(gw - 3072) * D_);
    const u32x4 v0 = *(const u32x4*)(row + lane * 16), v1 = *(const u32x4*)(row + lane * 16 + 8);
    float e[16];
#pragma unroll
    for (int i = 0; i < 4; ++i) { e[2 * i] = bf_lo(v0[i]); e[2 * i + 1] = bf_hi(v0[i]); e[8 + 2 * i] = bf_lo(v1[i]); e[9 + 2 * i] = bf_hi(v1[i]); }
    float s = 0.f;
#pragma unroll
    for (int i = 0; i < 16; ++i) s += e[i];
    s += __shfl_xor(s, 1); s += __shfl_xor(s, 2);
    if (gw < 2048) {
      const float mu = s * (1.0f / 64.0f); float q = 0.f;
#pragma unroll
      for (int i = 0; i < 16; ++i) { const float d = e[i] - mu; q = __builtin_fmaf(d, d, q); }
      q += __shfl_xor(q, 1); q += __shfl_xor(q, 2);
      if ((lane & 3) == 0) *(f32x2*)(stat + (size_t)gw * 32 + (lane >> 2) * 2) = (f32x2){mu, q};
      if (lane < 16) xch[gw * 16 + lane] = (u32x4){~0u, ~0u, ~0u, ~0u};
    } else {
      s += __shfl_xor(s, 4); s += __shfl_xor(s, 8); s += __shfl_xor(s, 16); s += __shfl_xor(s, 32);
      if (lane == 0) wsum[gw - 2048] = s;
    }
  }
}

using CG1a = Cfg<EPI_G1, 128, 128, 2, 2, 0, 0, 1024, 4, 16, 16, 4, 8, 2048,     2048,     0>;
using CG1b = Cfg<EPI_G1, 128, 128, 2, 2, 1, 0, 1024, 4, 16, 16, 4, 8, D_ / 32,  2048,     0>;
using CG2a = Cfg<EPI_G2, 128, 64,  4, 1, 0, 2, 4096, 5, 16, 16, 4, 8, 4096,     D_ / 16,  1>;
using CG2b = Cfg<EPI_G2, 128, 64,  4, 1, 0, 2, 4096, 5, 16, 16, 4, 8, 4096,     D_ / 16,  0>;
using CK3  = Cfg<EPI_K3, 128, 256, 2, 2, 1, 1, 1024, 3, 16, 14, 4, 7, D_ / 32,  F_ * 2,   0>;
using CK4  = Cfg<EPI_K4, 128, 112, 4, 1, 0, 2, 2048, 5, 16, 16, 4, 8, 4096,     FH / 16,  0>;
using CK5a = Cfg<EPI_K5, 128, 64,  4, 1, 1, 1, 1792, 5, 16, 16, 4, 8, FH / 32,  D_ * 2,   0>;
using CK5b = Cfg<EPI_K5, 128, 64,  4, 1, 1, 1, 1792, 5, 16, 16, 4, 8, FH / 32,  D_ * 2,   2>;

template <class C>
static void launch(const Args& a, hipStream_t st) {
  (void)hipFuncSetAttribute((const void*)gemm_k<C>, hipFuncAttributeMaxDynamicSharedMemorySize, C::LDS);
  hipLaunchKernelGGL(gemm_k<C>, dim3(C::GM * C::GN), dim3(512), C::LDS, st, a);
}

extern "C" void kernel_launch(void* const* d_in, const int* in_sizes, int n_in, void* d_out, int out_size, void* d_ws, size_t ws_size, hipStream_t stream) {
  const char* x = (const char*)d_in[0];
  const char* wsre = (const char*)d_in[1];
  const char* wsim = (const char*)d_in[2];
  const char* wdre = (const char*)d_in[3];
  const char* wdim = (const char*)d_in[4];
  const char* win = (const char*)d_in[6];
  const char* wsp = (const char*)d_in[7];
  const u16* bsp = (const u16*)d_in[8];
  const char* wout = (const char*)d_in[9];
  char* ws = (char*)d_ws;
  constexpr size_t MB = 1 << 20;
  char* ab = ws;
  char* x1 = ws + 8 * MB;
  char* x2 = ws + 12 * MB;
  char* z1 = ws + 16 * MB;
  char* z2 = ws + 24 * MB;
  char* h = ws + 32 * MB;
  float* st0 = (float*)(ws + 40 * MB);
  float* st1 = (float*)(ws + 40 * MB + 256 * 1024);
  float* st2 = (float*)(ws + 40 * MB + 512 * 1024);
  unsigned long long* xch = (unsigned long long*)(ws + 41 * MB);
  float* wsum = (float*)(ws + 42 * MB);
  float* csum = wsum + 2048;

  hipLaunchKernelGGL(prologue_k, dim3(56 + 1024), dim3(256), 0, stream, (const u16*)x, (const u16*)wdre, (const u16*)wdim, (const u16*)win, st0, wsum, csum, (u32x4*)xch);
  for (int l = 0; l < 2; ++l) {
    const char* xin = l == 0 ? x : x2;
    Args g1{xin, nullptr, wdre, wdim, ab, nullptr, nullptr, l == 0 ? st0 : st2, nullptr, wsum, nullptr, nullptr};
    if (l == 0) launch<CG1a>(g1, stream); else launch<CG1b>(g1, stream);
    Args g2{wsre, wsim, ab, nullptr, x1, nullptr, xin, nullptr, st1, nullptr, nullptr, nullptr};
    if (l == 0) launch<CG2a>(g2, stream); else launch<CG2b>(g2, stream);
    Args k3{x1, nullptr, win, nullptr, z1, z2, nullptr, st1, nullptr, csum, nullptr, xch + (size_t)l * S_ * 16};
    launch<CK3>(k3, stream);
    Args k4{wsp, nullptr, z2, nullptr, h, nullptr, z1, nullptr, nullptr, nullptr, bsp, nullptr};
    launch<CK4>(k4, stream);
    Args k5{h, nullptr, wout, nullptr, l == 0 ? x2 : (char*)d_out, nullptr, x1, nullptr, st2, nullptr, nullptr, nullptr};
    if (l == 0) launch<CK5a>(k5, stream); else launch<CK5b>(k5, stream);
  }
}
```

```cpp
#include <hip/hip_runtime.h>
#include <hip/hip_bf16.h>
#include <stdint.h>

#define LAS __attribute__((address_space(3)))
typedef unsigned short u16;
typedef short s16x4 __attribute__((ext_vector_type(4)));
typedef short s16x8 __attribute__((ext_vector_type(8)));
typedef __bf16 bf16x8 __attribute__((ext_vector_type(8)));
typedef __bf16 bf16x2 __attribute__((ext_vector_type(2)));
typedef float f32x2 __attribute__((ext_vector_type(2)));
typedef float f32x4 __attribute__((ext_vector_type(4)));
typedef unsigned u32x2 __attribute__((ext_vector_type(2)));
typedef unsigned u32x4 __attribute__((ext_vector_type(4)));

constexpr int S_ = 2048, D_ = 1024, F_ = 3584, FH = 1792;
constexpr float LN_EPS = 1e-5f;

__device__ __forceinline__ unsigned pack_bf16(float lo, float hi) {
  f32x2 v = {lo, hi};
  return __builtin_bit_cast(unsigned, __builtin_convertvector(v, bf16x2));
}
__device__ __forceinline__ float bf_lo(unsigned u) { return __builtin_bit_cast(float, u << 16); }
__device__ __forceinline__ float bf_hi(unsigned u) { return __builtin_bit_cast(float, u & 0xffff0000u); }
__device__ __forceinline__ void swap16(unsigned& a, unsigned& b) { asm volatile("s_nop 1\n\tv_permlane16_swap_b32 %0, %1" : "+v"(a), "+v"(b)); }
__device__ __forceinline__ void swap32(unsigned& a, unsigned& b) { asm volatile("s_nop 1\n\tv_permlane32_swap_b32 %0, %1" : "+v"(a), "+v"(b)); }
__device__ __forceinline__ float sum32(float v) {
  unsigned a = __builtin_bit_cast(unsigned, v), b = a; swap32(a, b);
  return __builtin_bit_cast(float, a) + __builtin_bit_cast(float, b);
}
__device__ __forceinline__ float sum_groups(float v) {
  unsigned a = __builtin_bit_cast(unsigned, v), b = a; swap16(a, b);
  return sum32(__builtin_bit_cast(float, a) + __builtin_bit_cast(float, b));
}
__device__ __forceinline__ u32x4 pair_rows(u32x2 a, u32x2 b) {
  unsigned a0 = a[0], a1 = a[1], b0 = b[0], b1 = b[1]; swap16(a0, b0); swap16(a1, b1);
  return (u32x4){a0, a1, b0, b1};
}
__device__ __forceinline__ void unpair_rows(u32x4 v, u32x2& a, u32x2& b) {
  unsigned a0 = v[0], a1 = v[1], b0 = v[2], b1 = v[3]; swap16(a0, b0); swap16(a1, b1);
  a = (u32x2){a0, a1}; b = (u32x2){b0, b1};
}
__device__ __forceinline__ u32x4 pair_cols(u32x2 a, u32x2 b) {
  unsigned a0 = a[0], a1 = a[1], b0 = b[0], b1 = b[1]; swap32(a0, b0); swap32(a1, b1); swap16(a0, b0); swap16(a1, b1);
  return (u32x4){a0, a1, b0, b1};
}
__device__ __forceinline__ void unpair_cols(u32x4 v, u32x2& a, u32x2& b) {
  unsigned a0 = v[0], a1 = v[1], b0 = v[2], b1 = v[3]; swap16(a0, b0); swap16(a1, b1); swap32(a0, b0); swap32(a1, b1);
  a = (u32x2){a0, a1}; b = (u32x2){b0, b1};
}
__device__ __forceinline__ u32x2 pack4(f32x4 v) { return (u32x2){pack_bf16(v[0], v[1]), pack_bf16(v[2], v[3])}; }
__device__ __forceinline__ f32x4 unpack4(u32x2 y) { return (f32x4){bf_lo(y[0]), bf_hi(y[0]), bf_lo(y[1]), bf_hi(y[1])}; }
__device__ __forceinline__ f32x4 gelu_tanh4(f32x4 x) {
  const float c1 = -2.0f * 0.7978845608028654f * 1.4426950408889634f, c2 = c1 * 0.044715f;
  const f32x4 t = x * (x * x * c2 + c1);
  f32x4 e;
#pragma unroll
  for (int i = 0; i < 4; ++i) e[i] = __builtin_amdgcn_exp2f(t[i]);
  e = e + 1.0f;
#pragma unroll
  for (int i = 0; i < 4; ++i) e[i] = __builtin_amdgcn_rcpf(e[i]);
  return x * e;
}
#define STAUX 16
typedef __amdgpu_buffer_rsrc_t rsrc_t;
__device__ __forceinline__ rsrc_t mk_rsrc(const void* p, unsigned bytes) { return __builtin_amdgcn_make_buffer_rsrc((void*)p, 0, bytes, 0x00020000); }
__device__ __forceinline__ void st16(rsrc_t r, unsigned off, u32x4 v) { __builtin_amdgcn_raw_buffer_store_b128(v, r, off, 0, STAUX); }
template <int AUX> __device__ __forceinline__ u32x4 ld16(rsrc_t r, unsigned off) { return __builtin_amdgcn_raw_buffer_load_b128(r, off, 0, AUX); }
#define WAIT_V(n) asm volatile("s_waitcnt vmcnt(%0)" ::"n"(n) : "memory")
#define WAIT_L0() asm volatile("s_waitcnt lgkmcnt(0)" ::: "memory")
#define BARRIER() do { asm volatile("" ::: "memory"); __builtin_amdgcn_s_barrier(); asm volatile("" ::: "memory"); } while (0)
#define PIN_S(x) asm volatile("" : "+s"(x))

enum { EPI_G1 = 0, EPI_G2 = 1, EPI_K3 = 2, EPI_K4 = 3, EPI_K5 = 4 };

struct Args {
  const char* A;
  const char* A2;
  const char* B;
  const char* B2;
  char* O;
  char* O2;
  const char* R;
  const float* stat_in;
  float* stat_out;
  const float* csum;
  const u16* bias;
  unsigned long long* xch;
};
#define TS(i)
#define TSR(i)

template <int EPI_, int BM_, int BN_, int WM_, int WN_, int AM_, int BMODE_, int K_, int NS_, int GM_, int GN_, int XM_, int XN_, int PA_, int PB_, int FLAG_>
struct Cfg {
  static constexpr int EPI = EPI_, BM = BM_, BN = BN_, WM = WM_, WN = WN_, AM = AM_, BMODE = BMODE_, K = K_, NS = NS_;
  static constexpr int GM = GM_, GN = GN_, XM = XM_, XN = XN_, PA = PA_, PB = PB_, FLAG = FLAG_;
  static constexpr int MT = BM / WM / 16, NT = BN / WN / 16;
  static constexpr int NPA = BM / 8, NPB = BN / 8, NP = NPA + NPB;
  static constexpr int ABYTES = BM * 128, STAGE = (BM + BN) * 128, T = K / 64;
  static constexpr int LDS = NS * STAGE + 2048;
};

template <class C>
__global__ __launch_bounds__(512, 2) void gemm_k(Args a) {
  extern __shared__ __attribute__((aligned(16))) char smem_[];
  LAS char* smem = (LAS char*)smem_;
  constexpr int BM = C::BM, BN = C::BN, MT = C::MT, NT = C::NT, NS = C::NS, T = C::T, STAGE = C::STAGE;
  constexpr bool FOLD = C::EPI == EPI_G1 || C::EPI == EPI_K3;
  constexpr bool SPLIT = C::EPI == EPI_G2;
  constexpr int HN0 = C::EPI == EPI_K3 ? 4 : 0;
  constexpr unsigned CS_OFF = NS * STAGE, ST_OFF = CS_OFF + 1024;
  constexpr unsigned HO_OFF = ((T - 1) % NS == 0) ? STAGE : 0;
  static_assert(!FOLD || (((T - 1) % NS == 0 || (T - 1) % NS == NS - 1) && MT == 4), "");
  TSR(4); TS(0);
  const int tid = threadIdx.x;
  int lane = tid & 63;
  const int wid = __builtin_amdgcn_readfirstlane(tid >> 6);
  const int xcd = blockIdx.x & 7, idx = blockIdx.x >> 3;
  constexpr int RN = C::GN / C::XN;
  const int lm = idx / C::XN, ln = idx % C::XN;
  const int bm = (xcd / RN) * C::XM + lm, bn = SPLIT ? ln : (xcd % RN) * C::XN + ln, kh = SPLIT ? xcd & 1 : 0;
  const int cw = wid & 3, wm = cw / C::WN, wn = cw % C::WN;
  const int n0 = bn * BN + wn * (NT * 16);
  constexpr int OW = C::EPI == EPI_K4 || C::EPI == EPI_K3 ? FH : D_;
  f32x4 acc[MT][NT];
  u32x4 rv[FOLD ? 1 : NT];
  float bias[2];
  char* pO = a.O; char* pO2 = a.O2; float* pSt = a.stat_out; unsigned long long* pX = a.xch;
  PIN_S(pO);
  if (C::EPI == EPI_K3 || SPLIT) { PIN_S(pO2); PIN_S(pX); }
  if (C::EPI == EPI_G2 || C::EPI == EPI_K5) PIN_S(pSt);

  if (wid >= 4) {
    const int w = cw, cls = (lm + ln) & 3;
    constexpr int PPW = (C::NP + 3) / 4, PWA = C::NPA / 4;
    static_assert(C::NPA % 4 == 0 && T % 4 == 0, "");
    const char* ab; const char* bb; unsigned av, bv;
    constexpr unsigned AI = C::AM == 0 ? 32 * C::PA : 2 * C::PA * 1024, AK = C::AM == 0 ? 128 : 2048;
    if (C::AM == 0) { ab = (kh ? a.A2 : a.A) + (size_t)(bm * BM + w * 8) * C::PA; av = (lane >> 3) * C::PA + (((lane & 7) ^ (((w & 1) << 2) | (lane >> 4))) << 4); }
    else { ab = a.A + ((size_t)(bm * (BM / 16) + (w >> 1)) * C::PA + (w & 1)) * 1024; av = lane * 16; }
    constexpr unsigned BK_ = C::BMODE == 0 ? 128 : (C::BMODE == 1 ? 64 * C::PB : 2 * C::PB * 1024);
    if (C::BMODE == 0) {
      const bool hi = C::EPI == EPI_G1 && bn * BN >= D_;
      bb = (hi ? a.B2 : a.B) + (size_t)(bn * BN - (hi ? D_ : 0) + w * 8) * C::PB; bv = (lane >> 3) * C::PB + (((lane & 7) ^ (((w & 1) << 2) | (lane >> 4))) << 4);
    } else if (C::BMODE == 1) {
      bb = a.B + (size_t)(w * 8) * C::PB + (C::EPI == EPI_K3 ? bn * 128 : bn * BN) * 2;
      bv = (lane >> 3) * C::PB + (((lane & 7) ^ ((((lane >> 4) & 1) << 2) | ((w & 1) << 1))) << 4);
    } else { bb = a.B + (size_t)(bn * (BN / 16)) * 1024 + (size_t)kh * T * BK_; bv = lane * 16; }
    auto boffs = [&](int i) -> unsigned {
      if (C::BMODE == 0) return i * 32 * C::PB;
      if (C::BMODE == 1) { const int cg = i >> 1; return (i & 1) * 32 * C::PB + (C::EPI == EPI_K3 ? (cg >> 1) * 128 + (cg & 1) * FH * 2 : cg * 128); }
      const int q = w + 4 * i, kk = q >= BN / 16; return (kk * C::PB + q - kk * (BN / 16)) * 1024;
    };
    const bool full = (C::NP % 4 == 0) || (w < C::NP % 4);
    auto issue = [&](int s, unsigned slot) {
      int ch = s;
      if ((s & 3) == cls) { ch = s + 4; if (ch >= T) ch -= T; }
      const char* ga = ab + (size_t)ch * AK;
      const char* gb = bb + (size_t)ch * BK_;
#pragma unroll
      for (int i = 0; i < PPW; ++i) {
        if (i == PPW - 1 && !full) break;
        const char* g = i < PWA ? ga + i * AI + av : gb + boffs(i - PWA) + bv;
        __builtin_amdgcn_global_load_lds((const unsigned*)g, (LAS unsigned*)(smem + slot + (w + 4 * i) * 1024), 16, 0, 0);
      }
    };
#define WAIT_STAGES(n) do { if (full) WAIT_V((n) * PPW); else WAIT_V((n) * (PPW - 1)); } while (0)
#pragma unroll
    for (int s = 0; s < NS - 1; ++s) issue(s, s * STAGE);
    if (FOLD && w == 3) {
      const float* cs = C::EPI == EPI_G1 ? a.csum + bn * BN + (lane & 31) * 4 : a.csum + bn * 128 + (lane >> 5) * 64 + ((lane >> 4) & 1) * FH + (lane & 15) * 4;
      __builtin_amdgcn_global_load_lds((const unsigned*)cs, (LAS unsigned*)(smem + CS_OFF), 16, 0, 0);
    }
    WAIT_STAGES(NS - 2);
    unsigned slot = (NS - 1) * STAGE;
#pragma unroll 1
    for (int s = 0; s < T - NS + 1; ++s) {
      BARRIER();
      issue(s + NS - 1, slot);
      slot += STAGE; if (slot == NS * STAGE) slot = 0;
      WAIT_STAGES(NS - 2);
    }
    if (NS >= 6) { BARRIER(); WAIT_STAGES(3); }
    if (NS >= 5) { BARRIER(); WAIT_STAGES(2); }
    if (NS >= 4) { BARRIER(); WAIT_STAGES(1); }
    BARRIER(); WAIT_V(0);
    BARRIER();
    if (!FOLD) return;
    BARRIER();
#pragma unroll
    for (int mt = 0; mt < 2; ++mt)
#pragma unroll
      for (int nt = 0; nt < 4; ++nt) acc[mt][HN0 + nt] = *(const LAS f32x4*)(smem + HO_OFF + ((w * 2 + mt) * 4 + nt) * 1024 + lane * 16);
  } else {
    const int r = lane & 15, j = lane >> 4;
    if (FOLD) {
      const f32x4* p = (const f32x4*)(a.stat_in + (size_t)(bm * BM + cw * 32 + (lane & 31)) * 32 + (lane >> 5) * 16);
      const f32x4 u0 = p[0], u1 = p[1], u2 = p[2], u3 = p[3];
      const float mu = sum32(((u0[0] + u0[2]) + (u1[0] + u1[2])) + ((u2[0] + u2[2]) + (u3[0] + u3[2]))) * (1.0f / 16.0f);
      float q = ((u0[1] + u0[3]) + (u1[1] + u1[3])) + ((u2[1] + u2[3]) + (u3[1] + u3[3])), d, q2 = 0.f;
      d = u0[0] - mu; q2 = __builtin_fmaf(d, d, q2); d = u0[2] - mu; q2 = __builtin_fmaf(d, d, q2);
      d = u1[0] - mu; q2 = __builtin_fmaf(d, d, q2); d = u1[2] - mu; q2 = __builtin_fmaf(d, d, q2);
      d = u2[0] - mu; q2 = __builtin_fmaf(d, d, q2); d = u2[2] - mu; q2 = __builtin_fmaf(d, d, q2);
      d = u3[0] - mu; q2 = __builtin_fmaf(d, d, q2); d = u3[2] - mu; q2 = __builtin_fmaf(d, d, q2);
      q = sum32(__builtin_fmaf(q2, 64.0f, q));
      const float rs = 1.0f / sqrtf(q * (1.0f / D_) + LN_EPS);
      if (lane < 32) *(LAS f32x2*)(smem + ST_OFF + (cw * 32 + lane) * 8) = (f32x2){rs, -mu * rs};
    } else {
      const int m0 = bm * BM + wm * (MT * 16) + kh * 32;
      const rsrc_t rr = mk_rsrc(a.R, S_ * OW * 2);
      if (C::FLAG & 1) {
#pragma unroll
        for (int i = 0; i < NT; ++i) rv[i] = ld16<0>(rr, ((m0 + (i >> 1) * 16 + r) * D_ + n0 + (i & 1) * 32 + j * 8) * 2);
      } else {
        const unsigned img = (unsigned)(((m0 >> 4) + (j & 1)) * (OW / 32)) * 1024u + (n0 >> 4) * 512 + (j >> 1) * 256 + r * 16;
#pragma unroll
        for (int nt = 0; nt < NT; ++nt) rv[nt] = ld16<0>(rr, img + nt * 512);
      }
      if (C::EPI == EPI_K4) {
        bias[0] = __builtin_bit_cast(float, (unsigned)a.bias[m0 + r] << 16); bias[1] = __builtin_bit_cast(float, (unsigned)a.bias[m0 + 16 + r] << 16);
#pragma unroll
        for (int nt = 0; nt < NT; ++nt) {
          const u32x2 lo = pack4(gelu_tanh4(unpack4((u32x2){rv[nt][0], rv[nt][1]}))), hi = pack4(gelu_tanh4(unpack4((u32x2){rv[nt][2], rv[nt][3]})));
          rv[nt] = (u32x4){lo[0], lo[1], hi[0], hi[1]};
          asm volatile("" : "+v"(rv[nt]));
        }
        asm volatile("" : "+v"(bias[0]), "+v"(bias[1]));
      }
    }

    unsigned aoff[2], aoff2[2], boff[4];
    if (C::AM == 0) { aoff[0] = (wm * MT * 16 + r) * 128 + ((j ^ (r >> 1)) << 4); aoff[1] = aoff[0] ^ 64; }
    else { aoff[0] = wm * MT * 2048 + lane * 16; aoff[1] = aoff[0] + 1024; }
    aoff2[0] = aoff[0] + 4096 - kh * 4096; aoff2[1] = aoff[1] + 4096 - kh * 4096; aoff[0] += kh * 4096; aoff[1] += kh * 4096;
    if (C::BMODE == 0) { boff[0] = C::ABYTES + (wn * NT * 16 + r) * 128 + ((j ^ (r >> 1)) << 4); boff[1] = boff[0] ^ 64; boff[2] = boff[3] = 0; }
    else if (C::BMODE == 1) {
      const int q = (lane >> 2) & 3, p = lane & 3, f = ((q >> 1) << 1) | (j & 1);
#pragma unroll
      for (int c = 0; c < 4; ++c) boff[c] = C::ABYTES + wn * (NT / 4) * 8192 + (j * 8 + q) * 128 + ((c ^ f) << 5) + p * 8;
    } else { boff[0] = C::ABYTES + wn * NT * 1024 + lane * 8; boff[1] = boff[2] = boff[3] = 0; }

#pragma unroll
    for (int mt = 0; mt < MT; ++mt)
#pragma unroll
      for (int nt = 0; nt < NT; ++nt) acc[mt][nt] = (f32x4){0.f, 0.f, 0.f, 0.f};

    constexpr int BR = C::BMODE == 0 ? 1 : 2, NR = MT + NT * BR, NM = MT * NT;
    auto rd = [&](s16x8 (&fa)[MT], s16x8 (&fb)[NT], unsigned slot, int kk, int i) {
      if (i >= BR && i < BR + MT) { const int mt = i - BR; fa[mt] = *(const LAS s16x8*)(smem + slot + (mt < 2 ? aoff[kk] : aoff2[kk]) + (mt & 1) * 2048); return; }
      const int ib = i < BR ? i : i - MT, nt = ib / BR, t = ib % BR;
      if (C::BMODE == 0) { fb[nt] = *(const LAS s16x8*)(smem + slot + boff[kk] + nt * 2048); return; }
      unsigned o;
      if (C::BMODE == 1) o = slot + boff[nt & 3] + kk * 4096 + (nt >> 2) * 8192;
      else o = slot + boff[0] + (kk * (BN / 16) + nt) * 1024;
      const s16x4 v = __builtin_amdgcn_ds_read_tr16_b64_v4i16((LAS s16x4*)(smem + o + t * 512));
      if (t == 0) fb[nt] = __builtin_shufflevector(v, v, 0, 1, 2, 3, -1, -1, -1, -1);
      else { const s16x4 lo = __builtin_shufflevector(fb[nt], fb[nt], 0, 1, 2, 3); fb[nt] = __builtin_shufflevector(lo, v, 0, 1, 2, 3, 4, 5, 6, 7); }
    };
    static_assert(C::BMODE != 1 || NT % 4 == 0, "");
    constexpr int RPM = (NR + NM - 3) / (NM - 2);
    auto phase = [&](const s16x8 (&ca)[MT], const s16x8 (&cb)[NT], s16x8 (&na)[MT], s16x8 (&nb)[NT], unsigned slot, int kk, bool rdnext) {
#pragma unroll
      for (int i = 0; i < NM; ++i) {
        const int nt = i / MT, mt = i % MT;
        acc[mt][nt] = __builtin_amdgcn_mfma_f32_16x16x32_bf16(__builtin_bit_cast(bf16x8, cb[nt]), __builtin_bit_cast(bf16x8, ca[mt]), acc[mt][nt], 0, 0, 0);
        if (rdnext) {
#pragma unroll
          for (int u = 0; u < RPM; ++u) if (i * RPM + u < NR) rd(na, nb, slot, kk, i * RPM + u);
        }
        __builtin_amdgcn_sched_barrier(0);
      }
    };

    s16x8 fa0[MT], fb0[NT], fa1[MT], fb1[NT];
    WAIT_L0();
    BARRIER();
    TS(1);
#pragma unroll
    for (int i = 0; i < NR; ++i) rd(fa0, fb0, 0, 0, i);
    __builtin_amdgcn_sched_barrier(0);
    phase(fa0, fb0, fa1, fb1, 0, 1, true);
    WAIT_L0();
    unsigned slot = 0;
#pragma unroll 1
    for (int s = 1; s < T; ++s) {
      slot += STAGE; if (slot == NS * STAGE) slot = 0;
      BARRIER();
      __builtin_amdgcn_sched_barrier(0);
      phase(fa1, fb1, fa0, fb0, slot, 0, true);
      phase(fa0, fb0, fa1, fb1, slot, 1, true);
      WAIT_L0();
    }
    __builtin_amdgcn_sched_barrier(0);
    phase(fa1, fb1, fa0, fb0, 0, 0, false);
    TS(2);
    if (FOLD) {
#pragma unroll
      for (int mt = 0; mt < 2; ++mt)
#pragma unroll
        for (int nt = 0; nt < 4; ++nt) *(LAS f32x4*)(smem + HO_OFF + ((cw * 2 + mt) * 4 + nt) * 1024 + lane * 16) = acc[2 + mt][HN0 + nt];
      WAIT_L0();
      BARRIER();
    }
  }

  asm volatile("" : "+v"(lane));
  const int r = lane & 15, j = lane >> 4;
  const int ml = wm * (MT * 16) + (wid >= 4 ? 32 : 0) + kh * 32, m0 = bm * BM + ml;
  const unsigned imgl = (j & 1) * (OW / 32) * 1024u + (j >> 1) * 256 + r * 16;
  const unsigned trl = ((r >> 2) & 1) * 512 + (j & 1) * 256 + (r >> 3) * 128 + (r & 3) * 32 + (j >> 1) * 16;
  if (C::EPI == EPI_G1) {
    const int half = n0 >= D_, nn = n0 - half * D_;
    const rsrc_t ro = mk_rsrc(pO, 2 * S_ * D_ * 2);
    const unsigned o = (unsigned)(((half * S_ + m0) >> 5) * (D_ / 16) + (nn >> 4)) * 1024u + trl;
    f32x2 st0 = *(const LAS f32x2*)(smem + ST_OFF + (ml + r) * 8), st1 = *(const LAS f32x2*)(smem + ST_OFF + (ml + 16 + r) * 8);
    if (half) { st0 = -st0; st1 = -st1; }
#pragma unroll
    for (int nt = 0; nt < NT; ++nt) {
      const f32x4 cs = *(const LAS f32x4*)(smem + CS_OFF + (wn * NT * 16 + nt * 16 + j * 4) * 4);
      st16(ro, o + nt * 1024, pair_rows(pack4(acc[0][nt] * st0[0] + cs * st0[1]), pack4(acc[1][nt] * st1[0] + cs * st1[1])));
    }
  } else if (C::EPI == EPI_G2 || C::EPI == EPI_K5) {
    static_assert((C::EPI != EPI_G2 && C::EPI != EPI_K5) || NT == 4, "");
    if (SPLIT) {
      const rsrc_t rx = mk_rsrc(pO2, 256 * 16384);
      const unsigned so = (blockIdx.x * 4 + cw) * 4096 + lane * 16, go = ((blockIdx.x ^ 1) * 4 + cw) * 4096 + lane * 16;
#pragma unroll
      for (int i = 0; i < 4; ++i) { const u32x2 lo = pack4(acc[2][i]), hi = pack4(acc[3][i]); __builtin_amdgcn_raw_buffer_store_b128((u32x4){lo[0], lo[1], hi[0], hi[1]}, rx, so + i * 1024, 0, 16); }
      WAIT_V(0);
      unsigned* fl = (unsigned*)pX;
      if (lane == 0) __hip_atomic_store(fl + (blockIdx.x * 4 + cw) * 32, 1u, __ATOMIC_RELAXED, __HIP_MEMORY_SCOPE_AGENT);
      TS(8);
      for (unsigned spin = 0; spin < (1u << 22); ++spin) {
        if (__builtin_amdgcn_readfirstlane(__hip_atomic_load(fl + ((blockIdx.x ^ 1) * 4 + cw) * 32, __ATOMIC_RELAXED, __HIP_MEMORY_SCOPE_AGENT)) != 0) break;
        __builtin_amdgcn_s_sleep(1);
      }
      asm volatile("" ::: "memory");
      TS(9);
      u32x4 pv[4];
#pragma unroll
      for (int i = 0; i < 4; ++i) pv[i] = ld16<16>(rx, go + i * 1024);
#pragma unroll
      for (int i = 0; i < 4; ++i) { acc[0][i] += unpack4((u32x2){pv[i][0], pv[i][1]}); acc[1][i] += unpack4((u32x2){pv[i][2], pv[i][3]}); }
      TS(10);
    }
    u32x2 x[2][4], y[2][4];
#pragma unroll
    for (int i = 0; i < 4; ++i) {
      if (C::FLAG & 1) unpair_cols(rv[i], x[i >> 1][(i & 1) * 2], x[i >> 1][(i & 1) * 2 + 1]);
      else unpair_rows(rv[i], x[0][i], x[1][i]);
    }
#pragma unroll
    for (int mt = 0; mt < 2; ++mt)
#pragma unroll
      for (int nt = 0; nt < 4; ++nt) y[mt][nt] = pack4(acc[mt][nt] + unpack4(x[mt][nt]));
    const rsrc_t ro = mk_rsrc(pO, S_ * D_ * 2);
#pragma unroll
    for (int i = 0; i < 4; ++i) {
      if (C::FLAG & 2) st16(ro, ((m0 + (i >> 1) * 16 + r) * D_ + n0 + (i & 1) * 32 + j * 8) * 2, pair_cols(y[i >> 1][(i & 1) * 2], y[i >> 1][(i & 1) * 2 + 1]));
      else st16(ro, (unsigned)((m0 >> 4) * (OW / 32)) * 1024u + ((n0 >> 4) + i) * 512 + imgl, pair_rows(y[0][i], y[1][i]));
    }
    if (!(C::FLAG & 2)) {
      f32x2 ps[2];
#pragma unroll
      for (int mt = 0; mt < 2; ++mt) {
        f32x4 z[4], s4 = {0.f, 0.f, 0.f, 0.f}, q4 = {0.f, 0.f, 0.f, 0.f};
#pragma unroll
        for (int nt = 0; nt < 4; ++nt) { z[nt] = unpack4(y[mt][nt]); s4 += z[nt]; }
        const float mu = sum_groups((s4[0] + s4[1]) + (s4[2] + s4[3])) * (1.0f / 64.0f);
#pragma unroll
        for (int nt = 0; nt < 4; ++nt) { const f32x4 d = z[nt] - mu; q4 += d * d; }
        ps[mt] = (f32x2){mu, sum_groups((q4[0] + q4[1]) + (q4[2] + q4[3]))};
      }
      if (j < 2) *(f32x2*)(pSt + (size_t)(m0 + j * 16 + r) * 32 + (n0 >> 6) * 2) = j ? ps[1] : ps[0];
    }
  } else if (C::EPI == EPI_K3) {
    const int f0 = bn * 128 + wn * 64;
    constexpr unsigned PS_OFF = HO_OFF + 32768, FS_OFF = PS_OFF + 2048;
    TS(8);
    if (wid < 4) {
      const rsrc_t ro = mk_rsrc(pO, S_ * FH * 2);
#pragma unroll
      for (int mp = 0; mp < 2; ++mp) {
        const f32x2 st0 = *(const LAS f32x2*)(smem + ST_OFF + (ml + mp * 32 + r) * 8), st1 = *(const LAS f32x2*)(smem + ST_OFF + (ml + mp * 32 + 16 + r) * 8);
#pragma unroll
        for (int nt = 0; nt < 4; ++nt) {
          const f32x4 cs = *(const LAS f32x4*)(smem + CS_OFF + (wn * 128 + nt * 16 + j * 4) * 4);
          st16(ro, (unsigned)(((m0 >> 4) + mp * 2) * (OW / 32)) * 1024u + ((f0 >> 4) + nt) * 512 + imgl,
               pair_rows(pack4(acc[mp * 2][nt] * st0[0] + cs * st0[1]), pack4(acc[mp * 2 + 1][nt] * st1[0] + cs * st1[1])));
        }
      }
    }
    TS(12);
    f32x4 z[2][4];
    {
      f32x2 ps[2];
#pragma unroll
      for (int mt = 0; mt < 2; ++mt) {
        const f32x2 st = *(const LAS f32x2*)(smem + ST_OFF + (ml + mt * 16 + r) * 8);
        f32x4 s4 = {0.f, 0.f, 0.f, 0.f}, q4 = {0.f, 0.f, 0.f, 0.f};
#pragma unroll
        for (int nt = 0; nt < 4; ++nt) {
          const f32x4 cs = *(const LAS f32x4*)(smem + CS_OFF + (wn * 128 + 64 + nt * 16 + j * 4) * 4);
          z[mt][nt] = gelu_tanh4(acc[mt][4 + nt] * st[0] + cs * st[1]); s4 += z[mt][nt];
        }
        const float mu = sum_groups((s4[0] + s4[1]) + (s4[2] + s4[3])) * (1.0f / 64.0f);
#pragma unroll
        for (int nt = 0; nt < 4; ++nt) { const f32x4 d = z[mt][nt] - mu; q4 += d * d; }
        ps[mt] = (f32x2){mu, sum_groups((q4[0] + q4[1]) + (q4[2] + q4[3]))};
      }
      if (j < 2) *(LAS f32x2*)(smem + PS_OFF + (wn * 128 + ml + j * 16 + r) * 8) = j ? ps[1] : ps[0];
    }
    TS(9);
    WAIT_L0();
    BARRIER();
    TS(10);
    if (wid >= 4) {
      if (cw < 2) {
        unsigned long long* xrow = pX + bm * BM + cw * 64 + lane;
        const f32x2 p0 = *(const LAS f32x2*)(smem + PS_OFF + (cw * 64 + lane) * 8), p1 = *(const LAS f32x2*)(smem + PS_OFF + (128 + cw * 64 + lane) * 8);
        const float d0 = p0[0] - p1[0], mu0 = 0.5f * (p0[0] + p1[0]), q0 = p0[1] + p1[1] + 32.0f * d0 * d0;
        unsigned qb = __builtin_bit_cast(unsigned, q0);
        if (qb == 0xffffffffu) qb = 0x7fc00000u;
        __hip_atomic_store(xrow + bn * S_, ((unsigned long long)qb << 32) | __builtin_bit_cast(unsigned, mu0), __ATOMIC_RELAXED, __HIP_MEMORY_SCOPE_AGENT);
        unsigned long long g[14];
        for (unsigned spin = 0;; ++spin) {
          bool ok = true;
#pragma unroll
          for (int i = 0; i < 14; ++i) { g[i] = __hip_atomic_load(xrow + i * S_, __ATOMIC_RELAXED, __HIP_MEMORY_SCOPE_AGENT); ok &= (unsigned)(g[i] >> 32) != 0xffffffffu; }
          if (__all(ok) || spin > (1u << 22)) break;
        }
        float s = 0.f, q = 0.f, q2 = 0.f;
#pragma unroll
        for (int i = 0; i < 14; ++i) { s += __builtin_bit_cast(float, (unsigned)g[i]); q += __builtin_bit_cast(float, (unsigned)(g[i] >> 32)); }
        const float mu = s * (1.0f / 14.0f);
#pragma unroll
        for (int i = 0; i < 14; ++i) { const float d = __builtin_bit_cast(float, (unsigned)g[i]) - mu; q2 = __builtin_fmaf(d, d, q2); }
        const float rs = 1.0f / sqrtf(__builtin_fmaf(q2, 128.0f, q) * (1.0f / FH) + LN_EPS);
        *(LAS f32x2*)(smem + FS_OFF + (cw * 64 + lane) * 8) = (f32x2){rs, mu};
      }
    }
    TS(11);
    WAIT_L0();
    BARRIER();
    TS(13);
    const rsrc_t ro2 = mk_rsrc(pO2, S_ * FH * 2);
    const unsigned o = (unsigned)((m0 >> 5) * (FH / 16) + (f0 >> 4)) * 1024u + trl;
    const f32x2 fs0 = *(const LAS f32x2*)(smem + FS_OFF + (ml + r) * 8), fs1 = *(const LAS f32x2*)(smem + FS_OFF + (ml + 16 + r) * 8);
#pragma unroll
    for (int nt = 0; nt < 4; ++nt) st16(ro2, o + nt * 1024, pair_rows(pack4((z[0][nt] - fs0[1]) * fs0[0]), pack4((z[1][nt] - fs1[1]) * fs1[0])));
  } else if (C::EPI == EPI_K4) {
    const rsrc_t ro = mk_rsrc(pO, S_ * FH * 2);
#pragma unroll
    for (int nt = 0; nt < NT; ++nt) {
      u32x2 x0, x1; unpair_rows(rv[nt], x0, x1);
      st16(ro, (unsigned)((m0 >> 4) * (OW / 32)) * 1024u + ((n0 >> 4) + nt) * 512 + imgl, pair_rows(pack4((acc[0][nt] + bias[0]) * unpack4(x0)), pack4((acc[1][nt] + bias[1]) * unpack4(x1))));
    }
  }
}

__global__ __launch_bounds__(256) void prologue_k(const u16* x, const u16* wdre, const u16* wdim, const u16* win, float* stat, float* wsum, float* csum, u32x4* xch, unsigned* flags) {
  __shared__ float red[32][65];
  const int tid = threadIdx.x, lane = tid & 63, wid = tid >> 6, b = blockIdx.x;
  if (b < 56) {
    const int ch = tid & 7, rg = tid >> 3;
    float s[8] = {0.f, 0.f, 0.f, 0.f, 0.f, 0.f, 0.f, 0.f};
#pragma unroll 8
    for (int k = rg; k < D_; k += 32) {
      const u32x4 v = *(const u32x4*)(win + (size_t)k * F_ + b * 64 + ch * 8);
#pragma unroll
      for (int e = 0; e < 4; ++e) { s[2 * e] += bf_lo(v[e]); s[2 * e + 1] += bf_hi(v[e]); }
    }
#pragma unroll
    for (int e = 0; e < 8; ++e) red[rg][ch * 8 + e] = s[e];
    __syncthreads();
    if (tid < 64) { float t = 0.f; for (int i = 0; i < 32; ++i) t += red[i][tid]; csum[b * 64 + tid] = t; }
    return;
  }
  const int gw = (b - 56) * 4 + wid;
  if (gw < 4096) {
    const u16* row = gw < 2048 ? x + (size_t)gw * D_ : (gw < 3072 ? wdre + (size_t)(gw - 2048) * D_ : wdim + (size_t)(gw - 3072) * D_);
    const u32x4 v0 = *(const u32x4*)(row + lane * 16), v1 = *(const u32x4*)(row + lane * 16 + 8);
    float e[16];
#pragma unroll
    for (int i = 0; i < 4; ++i) { e[2 * i] = bf_lo(v0[i]); e[2 * i + 1] = bf_hi(v0[i]); e[8 + 2 * i] = bf_lo(v1[i]); e[9 + 2 * i] = bf_hi(v1[i]); }
    float s = 0.f;
#pragma unroll
    for (int i = 0; i < 16; ++i) s += e[i];
    s += __shfl_xor(s, 1); s += __shfl_xor(s, 2);
    if (gw < 2048) {
      const float mu = s * (1.0f / 64.0f); float q = 0.f;
#pragma unroll
      for (int i = 0; i < 16; ++i) { const float d = e[i] - mu; q = __builtin_fmaf(d, d, q); }
      q += __shfl_xor(q, 1); q += __shfl_xor(q, 2);
      if ((lane & 3) == 0) *(f32x2*)(stat + (size_t)gw * 32 + (lane >> 2) * 2) = (f32x2){mu, q};
      if (lane == 16) flags[gw * 32] = 0u;
      if (lane < 16) xch[gw * 16 + lane] = (u32x4){~0u, ~0u, ~0u, ~0u};
    } else {
      s += __shfl_xor(s, 4); s += __shfl_xor(s, 8); s += __shfl_xor(s, 16); s += __shfl_xor(s, 32);
      if (lane == 0) wsum[gw - 2048] = s;
    }
  }
}

using CG1a = Cfg<EPI_G1, 128, 128, 2, 2, 0, 0, 1024, 4, 16, 16, 4, 8, 2048,     2048,     0>;
using CG1b = Cfg<EPI_G1, 128, 128, 2, 2, 1, 0, 1024, 4, 16, 16, 4, 8, D_ / 32,  2048,     0>;
using CG2a = Cfg<EPI_G2, 128, 128, 2, 2, 0, 2, 2048, 4, 16, 16, 4, 8, 4096,     D_ / 16,  1>;
using CG2b = Cfg<EPI_G2, 128, 128, 2, 2, 0, 2, 2048, 4, 16, 16, 4, 8, 4096,     D_ / 16,  0>;
using CK3  = Cfg<EPI_K3, 128, 256, 2, 2, 1, 1, 1024, 3, 16, 14, 4, 7, D_ / 32,  F_ * 2,   0>;
using CK4  = Cfg<EPI_K4, 128, 112, 4, 1, 0, 2, 2048, 5, 16, 16, 4, 8, 4096,     FH / 16,  0>;
using CK5a = Cfg<EPI_K5, 128, 64,  4, 1, 1, 1, 1792, 5, 16, 16, 4, 8, FH / 32,  D_ * 2,   0>;
using CK5b = Cfg<EPI_K5, 128, 64,  4, 1, 1, 1, 1792, 5, 16, 16, 4, 8, FH / 32,  D_ * 2,   2>;

template <class C>
static void launch(Args a, hipStream_t st) {
  (void)hipFuncSetAttribute((const void*)gemm_k<C>, hipFuncAttributeMaxDynamicSharedMemorySize, C::LDS);
  hipLaunchKernelGGL(gemm_k<C>, dim3(C::GM * C::GN), dim3(512), C::LDS, st, a);
}

extern "C" void kernel_launch(void* const* d_in, const int* in_sizes, int n_in, void* d_out, int out_size, void* d_ws, size_t ws_size, hipStream_t stream) {
  const char* x = (const char*)d_in[0];
  const char* wsre = (const char*)d_in[1];
  const char* wsim = (const char*)d_in[2];
  const char* wdre = (const char*)d_in[3];
  const char* wdim = (const char*)d_in[4];
  const char* win = (const char*)d_in[6];
  const char* wsp = (const char*)d_in[7];
  const u16* bsp = (const u16*)d_in[8];
  const char* wout = (const char*)d_in[9];
  char* ws = (char*)d_ws;
  constexpr size_t MB = 1 << 20;
  char* ab = ws;
  char* x1 = ws + 8 * MB;
  char* x2 = ws + 12 * MB;
  char* z1 = ws + 16 * MB;
  char* z2 = ws + 24 * MB;
  char* h = ws + 32 * MB;
  float* st0 = (float*)(ws + 40 * MB);
  float* st1 = (float*)(ws + 40 * MB + 256 * 1024);
  float* st2 = (float*)(ws + 40 * MB + 512 * 1024);
  unsigned long long* xch = (unsigned long long*)(ws + 41 * MB);
  unsigned* flags = (unsigned*)(ws + 43 * MB);
  char* xbuf = ws + 48 * MB;
  float* wsum = (float*)(ws + 42 * MB);
  float* csum = wsum + 2048;

  hipLaunchKernelGGL(prologue_k, dim3(56 + 1024), dim3(256), 0, stream, (const u16*)x, (const u16*)wdre, (const u16*)wdim, (const u16*)win, st0, wsum, csum, (u32x4*)xch, flags);
  for (int l = 0; l < 2; ++l) {
    const char* xin = l == 0 ? x : x2;
    Args g1{xin, nullptr, wdre, wdim, ab, nullptr, nullptr, l == 0 ? st0 : st2, nullptr, wsum, nullptr, nullptr};
    if (l == 0) launch<CG1a>(g1, stream); else launch<CG1b>(g1, stream);
    Args g2{wsre, wsim, ab, nullptr, x1, xbuf, xin, nullptr, st1, nullptr, nullptr, (unsigned long long*)(flags + l * 1024 * 32)};
    if (l == 0) launch<CG2a>(g2, stream); else launch<CG2b>(g2, stream);
    Args k3{x1, nullptr, win, nullptr, z1, z2, nullptr, st1, nullptr, csum, nullptr, xch + (size_t)l * S_ * 16};
    launch<CK3>(k3, stream);
    Args k4{wsp, nullptr, z2, nullptr, h, nullptr, z1, nullptr, nullptr, nullptr, bsp, nullptr};
    launch<CK4>(k4, stream);
    Args k5{h, nullptr, wout, nullptr, l == 0 ? x2 : (char*)d_out, nullptr, x1, nullptr, st2, nullptr, nullptr, nullptr};
    if (l == 0) launch<CK5a>(k5, stream); else launch<CK5b>(k5, stream);
  }
}
```

```cpp
#include <hip/hip_runtime.h>
#include <hip/hip_bf16.h>
#include <stdint.h>

#define LAS __attribute__((address_space(3)))
typedef unsigned short u16;
typedef short s16x4 __attribute__((ext_vector_type(4)));
typedef short s16x8 __attribute__((ext_vector_type(8)));
typedef __bf16 bf16x8 __attribute__((ext_vector_type(8)));
typedef __bf16 bf16x2 __attribute__((ext_vector_type(2)));
typedef float f32x2 __attribute__((ext_vector_type(2)));
typedef float f32x4 __attribute__((ext_vector_type(4)));
typedef unsigned u32x2 __attribute__((ext_vector_type(2)));
typedef unsigned u32x4 __attribute__((ext_vector_type(4)));

constexpr int S_ = 2048, D_ = 1024, F_ = 3584, FH = 1792;
constexpr float LN_EPS = 1e-5f;

__device__ __forceinline__ unsigned pack_bf16(float lo, float hi) {
  f32x2 v = {lo, hi};
  return __builtin_bit_cast(unsigned, __builtin_convertvector(v, bf16x2));
}
__device__ __forceinline__ float bf_lo(unsigned u) { return __builtin_bit_cast(float, u << 16); }
__device__ __forceinline__ float bf_hi(unsigned u) { return __builtin_bit_cast(float, u & 0xffff0000u); }
__device__ __forceinline__ void swap16(unsigned& a, unsigned& b) { asm volatile("s_nop 1\n\tv_permlane16_swap_b32 %0, %1" : "+v"(a), "+v"(b)); }
__device__ __forceinline__ void swap32(unsigned& a, unsigned& b) { asm volatile("s_nop 1\n\tv_permlane32_swap_b32 %0, %1" : "+v"(a), "+v"(b)); }
__device__ __forceinline__ float sum32(float v) {
  unsigned a = __builtin_bit_cast(unsigned, v), b = a; swap32(a, b);
  return __builtin_bit_cast(float, a) + __builtin_bit_cast(float, b);
}
__device__ __forceinline__ float sum_groups(float v) {
  unsigned a = __builtin_bit_cast(unsigned, v), b = a; swap16(a, b);
  return sum32(__builtin_bit_cast(float, a) + __builtin_bit_cast(float, b));
}
__device__ __forceinline__ u32x4 pair_rows(u32x2 a, u32x2 b) {
  unsigned a0 = a[0], a1 = a[1], b0 = b[0], b1 = b[1]; swap16(a0, b0); swap16(a1, b1);
  return (u32x4){a0, a1, b0, b1};
}
__device__ __forceinline__ void unpair_rows(u32x4 v, u32x2& a, u32x2& b) {
  unsigned a0 = v[0], a1 = v[1], b0 = v[2], b1 = v[3]; swap16(a0, b0); swap16(a1, b1);
  a = (u32x2){a0, a1}; b = (u32x2){b0, b1};
}
__device__ __forceinline__ u32x4 pair_cols(u32x2 a, u32x2 b) {
  unsigned a0 = a[0], a1 = a[1], b0 = b[0], b1 = b[1]; swap32(a0, b0); swap32(a1, b1); swap16(a0, b0); swap16(a1, b1);
  return (u32x4){a0, a1, b0, b1};
}
__device__ __forceinline__ void unpair_cols(u32x4 v, u32x2& a, u32x2& b) {
  unsigned a0 = v[0], a1 = v[1], b0 = v[2], b1 = v[3]; swap16(a0, b0); swap16(a1, b1); swap32(a0, b0); swap32(a1, b1);
  a = (u32x2){a0, a1}; b = (u32x2){b0, b1};
}
__device__ __forceinline__ u32x2 pack4(f32x4 v) { return (u32x2){pack_bf16(v[0], v[1]), pack_bf16(v[2], v[3])}; }
__device__ __forceinline__ f32x4 unpack4(u32x2 y) { return (f32x4){bf_lo(y[0]), bf_hi(y[0]), bf_lo(y[1]), bf_hi(y[1])}; }
__device__ __forceinline__ f32x4 gelu_tanh4(f32x4 x) {
  const float c1 = -2.0f * 0.7978845608028654f * 1.4426950408889634f, c2 = c1 * 0.044715f;
  const f32x4 t = x * (x * x * c2 + c1);
  f32x4 e;
#pragma unroll
  for (int i = 0; i < 4; ++i) e[i] = __builtin_amdgcn_exp2f(t[i]);
  e = e + 1.0f;
#pragma unroll
  for (int i = 0; i < 4; ++i) e[i] = __builtin_amdgcn_rcpf(e[i]);
  return x * e;
}
#define STAUX 16
typedef __amdgpu_buffer_rsrc_t rsrc_t;
__device__ __forceinline__ rsrc_t mk_rsrc(const void* p, unsigned bytes) { return __builtin_amdgcn_make_buffer_rsrc((void*)p, 0, bytes, 0x00020000); }
__device__ __forceinline__ void st16(rsrc_t r, unsigned off, u32x4 v) { __builtin_amdgcn_raw_buffer_store_b128(v, r, off, 0, STAUX); }
template <int AUX> __device__ __forceinline__ u32x4 ld16(rsrc_t r, unsigned off) { return __builtin_amdgcn_raw_buffer_load_b128(r, off, 0, AUX); }
#define WAIT_V(n) asm volatile("s_waitcnt vmcnt(%0)" ::"n"(n) : "memory")
#define WAIT_L0() asm volatile("s_waitcnt lgkmcnt(0)" ::: "memory")
#define BARRIER() do { asm volatile("" ::: "memory"); __builtin_amdgcn_s_barrier(); asm volatile("" ::: "memory"); } while (0)
#define PIN_S(x) asm volatile("" : "+s"(x))

#define RLX_AGENT __ATOMIC_RELAXED, __HIP_MEMORY_SCOPE_AGENT
#define PF_THR 8
#define POLL_SLEEP 0
constexpr unsigned SPIN_MAX = 1u << 16;

constexpr size_t MB = 1 << 20, LSTRIDE = 48 * MB;
constexpr size_t O_X = 0, O_X1 = 4 * MB, O_AB = 8 * MB, O_Z1 = 16 * MB, O_Z2 = 24 * MB, O_H = 32 * MB, O_XB = 40 * MB;
constexpr size_t O_STX = 44 * MB, O_ST1 = O_STX + 256 * 1024;
constexpr size_t O_XCH = O_STX + 512 * 1024;
constexpr size_t O_XF = O_STX + 768 * 1024;
constexpr size_t O_SF = O_XF + 128 * 1024;
constexpr size_t O_SUM = 2 * LSTRIDE;
constexpr unsigned W_RING = 96 * 1024, A_RING = W_RING, A_SLOT = 16384, CS_OFF = 144 * 1024, ST_OFF = CS_OFF + 1024, PS_OFF = ST_OFF + 1024, FS_OFF = PS_OFF + 2048, CNT_OFF = FS_OFF + 1024;
constexpr unsigned LDS_BYTES = CNT_OFF + 64;

enum { G1 = 0, G2 = 1, K3 = 2, K4 = 3, K5 = 4 };

struct Params {
  const char *wsre, *wsim, *wdre, *wdim, *win, *wsp, *wout;
  const u16* bsp;
  char* out;
  char* ws;
};

template <int PH_, int BM_, int BN_, int WM_, int WN_, int AM_, int BMODE_, int K_, int GN_, int XM_, int XN_, int PA_, int PB_, int WSIDE_>
struct Cfg {
  static constexpr int PH = PH_, BM = BM_, BN = BN_, WM = WM_, WN = WN_, AM = AM_, BMODE = BMODE_, K = K_;
  static constexpr int GN = GN_, XM = XM_, XN = XN_, PA = PA_, PB = PB_, WSIDE = WSIDE_;
  static constexpr int MT = BM / WM / 16, NT = BN / WN / 16, T = K / 64;
};
using CG1 = Cfg<G1, 128, 128, 2, 2, 1, 0, 1024, 16, 4, 8, D_ / 32,  2048,    1>;
using CG2 = Cfg<G2, 128, 128, 2, 2, 0, 2, 2048, 16, 4, 8, 4096,     D_ / 16, 0>;
using CK3 = Cfg<K3, 128, 256, 2, 2, 1, 1, 1024, 14, 4, 7, D_ / 32,  F_ * 2,  1>;
using CK4 = Cfg<K4, 128, 112, 4, 1, 0, 2, 2048, 16, 4, 8, 4096,     FH / 16, 0>;
using CK5 = Cfg<K5, 128, 64,  4, 1, 1, 1, 1792, 16, 4, 8, FH / 32,  D_ * 2,  1>;

template <class C> struct Tile {
  int bm, bn, kh, cls;
  __device__ __forceinline__ Tile() {
    int bid = blockIdx.x; PIN_S(bid);
    const int xcd = bid & 7, idx = bid >> 3, RN = C::GN / C::XN, lm = idx / C::XN, ln = idx % C::XN;
    bm = (xcd / RN) * C::XM + lm; bn = C::PH == G2 ? ln : (xcd % RN) * C::XN + ln; kh = C::PH == G2 ? xcd & 1 : 0; cls = (lm + ln) & 3;
  }
};
template <class C, int SIDE> struct Ring {
  static constexpr bool ISW = C::WSIDE == SIDE;
  static constexpr unsigned BYTES = (SIDE ? C::BN : C::BM) * 128, BASE = ISW ? 0 : A_RING, SLOT = ISW ? BYTES : A_SLOT, D = ISW ? W_RING / BYTES : 3;
};

__device__ __forceinline__ unsigned* phase_flags(char* wl, int ph, int panel) { return (unsigned*)(wl + O_SF) + (ph * 16 + panel) * 64; }
__device__ __forceinline__ void signal_done(LAS char* smem, int slot, unsigned nw, unsigned* flag, int lane) {
  WAIT_V(0);
  if (lane == 0) {
    const unsigned old = __hip_atomic_fetch_add((LAS unsigned*)(smem + CNT_OFF) + slot, 1u, __ATOMIC_RELAXED, __HIP_MEMORY_SCOPE_WORKGROUP);
    if (old == nw - 1) __hip_atomic_store(flag, 1u, RLX_AGENT);
  }
}
__device__ __forceinline__ void wait_flags(unsigned* f) {
#pragma unroll 1
  for (unsigned spin = 0; spin < SPIN_MAX; ++spin) {
    if (__all(__hip_atomic_load(f, RLX_AGENT) != 0u)) break;
    if (POLL_SLEEP) __builtin_amdgcn_s_sleep(POLL_SLEEP);
  }
  asm volatile("" ::: "memory");
}

template <class C, int SIDE> struct Stream {
  using R = Ring<C, SIDE>;
  static constexpr int NP = R::BYTES / 2048, T = C::T, D = R::D, MODE = SIDE ? C::BMODE : (C::AM ? 3 : 0), PITCH = SIDE ? C::PB : C::PA;
  static constexpr unsigned KS = MODE == 0 ? 128 : (MODE == 1 ? 64 * PITCH : (MODE == 2 ? 2 * PITCH * 1024 : 2048));
  static_assert(T % 4 == 0 && D * NP <= 63, "");
  const char* base; unsigned voff; int cls, w;
  __device__ __forceinline__ Stream(const char* src, const Tile<C>& t, int w_, int lane) {
    w = w_; cls = t.cls;
    const int b = SIDE ? t.bn : t.bm;
    if (MODE == 0) { base = src + (size_t)((C::PH == G1 ? b & 7 : b) * 128 + w * 8) * PITCH; voff = (lane >> 3) * PITCH + (((lane & 7) ^ ((w << 2) | (lane >> 4))) << 4); }
    else if (MODE == 1) { base = src + (size_t)(w * 8) * PITCH + (C::PH == K3 ? b * 128 : b * C::BN) * 2; voff = (lane >> 3) * PITCH + (((lane & 7) ^ ((((lane >> 4) & 1) << 2) | (w << 1))) << 4); }
    else if (MODE == 2) { base = src + (size_t)(b * (C::BN / 16)) * 1024 + (size_t)t.kh * T * KS; voff = lane * 16; }
    else { base = src + ((size_t)(b * (C::BM / 16)) * PITCH + w) * 1024; voff = lane * 16; }
  }
  __device__ __forceinline__ unsigned poff(int i) const {
    if (MODE == 0) return i * 16 * PITCH;
    if (MODE == 1) { const int cg = i >> 2; return (i & 3) * 16 * PITCH + (C::PH == K3 ? (cg >> 1) * 128 + (cg & 1) * FH * 2 : cg * 128); }
    if (MODE == 2) { const int q = w + 2 * i, kk = q >= C::BN / 16; return (kk * PITCH + q - kk * (C::BN / 16)) * 1024; }
    return i * PITCH * 1024;
  }
  __device__ __forceinline__ void issue(LAS char* smem, int s, unsigned slot, bool thr = false) const {
    int ch = s;
    if ((s & 3) == cls) { ch = s + 4; if (ch >= T) ch -= T; }
    const char* g = base + (size_t)ch * KS;
#pragma unroll
    for (int i = 0; i < NP; ++i) {
      __builtin_amdgcn_global_load_lds((const unsigned*)(g + poff(i) + voff), (LAS unsigned*)(smem + R::BASE + slot + (w + 2 * i) * 1024), 16, 0, R::ISW ? 0 : 16);
      if (PF_THR && thr && (i & 1)) WAIT_V(PF_THR);
    }
  }
  __device__ __forceinline__ void fill(LAS char* smem, int s0, int s1) const {
#pragma unroll 1
    for (int s = s0; s < s1; ++s) issue(smem, s, s * R::SLOT, R::ISW);
  }
  template <int XTRA, class F> __device__ __forceinline__ void run(LAS char* smem, F tail) const {
    WAIT_V((D - 1) * NP);
    BARRIER();
    WAIT_V((D - 2) * NP);
    unsigned slot = 0;
#pragma unroll 1
    for (int s = 1; s < T - D; ++s) {
      BARRIER();
      issue(smem, s + D - 1, slot);
      slot += R::SLOT; if (slot == D * R::SLOT) slot = 0;
      WAIT_V((D - 2) * NP);
    }
    BARRIER();
    issue(smem, T - 1, slot);
    tail(0);
    WAIT_V((D - 2) * NP + XTRA);
    BARRIER();
    WAIT_V((D - 3) * NP);
#pragma unroll 1
    for (int s = T - D + 2; s < T - 1; ++s) { BARRIER(); WAIT_V(0); }
    BARRIER();
    tail(1);
    WAIT_L0();
    BARRIER();
  }
};

#define TS(i)
#define TSR(i)
#define TS_DECL
#define TS_OUT(ph)

__device__ __forceinline__ void k3_act(LAS char* smem, const f32x4 (&a)[2][4], f32x4 (&z)[2][4], int ml, int wn, int r, int j) {
  f32x2 ps[2];
#pragma unroll
  for (int mt = 0; mt < 2; ++mt) {
    const f32x2 st = *(const LAS f32x2*)(smem + ST_OFF + (ml + mt * 16 + r) * 8);
    f32x4 s4 = {0.f, 0.f, 0.f, 0.f}, q4 = {0.f, 0.f, 0.f, 0.f};
#pragma unroll
    for (int nt = 0; nt < 4; ++nt) {
      const f32x4 cs = *(const LAS f32x4*)(smem + CS_OFF + (wn * 128 + 64 + nt * 16 + j * 4) * 4);
      z[mt][nt] = gelu_tanh4(a[mt][nt] * st[0] + cs * st[1]); s4 += z[mt][nt];
    }
    const float mu = sum_groups((s4[0] + s4[1]) + (s4[2] + s4[3])) * (1.0f / 64.0f);
#pragma unroll
    for (int nt = 0; nt < 4; ++nt) { const f32x4 d = z[mt][nt] - mu; q4 += d * d; }
    ps[mt] = (f32x2){mu, sum_groups((q4[0] + q4[1]) + (q4[2] + q4[3]))};
  }
  if (j < 2) *(LAS f32x2*)(smem + PS_OFF + (wn * 128 + ml + j * 16 + r) * 8) = j ? ps[1] : ps[0];
  WAIT_L0();
}
__device__ __forceinline__ unsigned tr_lane(int r, int j) { return ((r >> 2) & 1) * 512 + (j & 1) * 256 + (r >> 3) * 128 + (r & 3) * 32 + (j >> 1) * 16; }
__device__ __forceinline__ void k3_norm(LAS char* smem, char* wl, const f32x4 (&z)[2][4], int bm, int bn, int ml, int wn, int l, int lane) {
  const int r = lane & 15, j = lane >> 4;
  const rsrc_t ro = mk_rsrc(wl + O_Z2, S_ * FH * 2);
  const unsigned o = (unsigned)(((bm * 128 + ml) >> 5) * (FH / 16) + bn * 8 + wn * 4) * 1024u + tr_lane(r, j);
  const f32x2 fs0 = *(const LAS f32x2*)(smem + FS_OFF + (ml + r) * 8), fs1 = *(const LAS f32x2*)(smem + FS_OFF + (ml + 16 + r) * 8);
#pragma unroll
  for (int nt = 0; nt < 4; ++nt) st16(ro, o + nt * 1024, pair_rows(pack4((z[0][nt] - fs0[1]) * fs0[0]), pack4((z[1][nt] - fs1[1]) * fs1[0])));
  signal_done(smem, l * 5 + K3, 8, phase_flags(wl, K3, bn) + bm, lane);
}

template <class C>
__device__ __forceinline__ void compute_phase(LAS char* smem, const Params& p, int l, int wid, int lane) {
  constexpr int BM = C::BM, BN = C::BN, MT = C::MT, NT = C::NT, T = C::T, PH = C::PH;
  using RX = Ring<C, 0>; using RY = Ring<C, 1>;
  TS_DECL; TSR(4); TS(0);
  asm volatile("" : "+v"(lane));
  const Tile<C> t;
  char* wl = p.ws + l * LSTRIDE;
  const int wm = wid / C::WN, wn = wid % C::WN, n0 = t.bn * BN + wn * (NT * 16);
  const int ml = wm * (MT * 16) + t.kh * 32, m0 = t.bm * BM + ml;
  constexpr int OW = PH == K4 || PH == K3 ? FH : D_;
  f32x4 acc[MT][NT];
  u32x4 rv[NT];
  float bias[2];
  {
    const int r = lane & 15, j = lane >> 4;
    unsigned aoff[2], aoff2[2], boff[4];
    if (C::AM == 0) { aoff[0] = RX::BASE + (wm * MT * 16 + r) * 128 + ((j ^ (r >> 1)) << 4); aoff[1] = aoff[0] ^ 64; }
    else { aoff[0] = RX::BASE + wm * MT * 2048 + lane * 16; aoff[1] = aoff[0] + 1024; }
    aoff2[0] = aoff[0] + 4096 - t.kh * 4096; aoff2[1] = aoff[1] + 4096 - t.kh * 4096; aoff[0] += t.kh * 4096; aoff[1] += t.kh * 4096;
    if (C::BMODE == 0) { boff[0] = RY::BASE + (wn * NT * 16 + r) * 128 + ((j ^ (r >> 1)) << 4); boff[1] = boff[0] ^ 64; boff[2] = boff[3] = 0; }
    else if (C::BMODE == 1) {
      const int q = (lane >> 2) & 3, pp = lane & 3, f = ((q >> 1) << 1) | (j & 1);
#pragma unroll
      for (int c = 0; c < 4; ++c) boff[c] = RY::BASE + wn * (NT / 4) * 8192 + (j * 8 + q) * 128 + ((c ^ f) << 5) + pp * 8;
    } else { boff[0] = RY::BASE + wn * NT * 1024 + lane * 8; boff[1] = boff[2] = boff[3] = 0; }
#pragma unroll
    for (int mt = 0; mt < MT; ++mt)
#pragma unroll
      for (int nt = 0; nt < NT; ++nt) acc[mt][nt] = (f32x4){0.f, 0.f, 0.f, 0.f};

    constexpr int BR = C::BMODE == 0 ? 1 : 2, NR = MT + NT * BR, NM = MT * NT;
    auto rd = [&](s16x8 (&fa)[MT], s16x8 (&fb)[NT], unsigned xs, unsigned ys, int kk, int i) {
      if (i >= BR && i < BR + MT) { const int mt = i - BR; fa[mt] = *(const LAS s16x8*)(smem + xs + (mt < 2 ? aoff[kk] : aoff2[kk]) + (mt & 1) * 2048); return; }
      const int ib = i < BR ? i : i - MT, nt = ib / BR, h = ib % BR;
      if (C::BMODE == 0) { fb[nt] = *(const LAS s16x8*)(smem + ys + boff[kk] + nt * 2048); return; }
      unsigned o;
      if (C::BMODE == 1) o = ys + boff[nt & 3] + kk * 4096 + (nt >> 2) * 8192;
      else o = ys + boff[0] + (kk * (BN / 16) + nt) * 1024;
      const s16x4 v = __builtin_amdgcn_ds_read_tr16_b64_v4i16((LAS s16x4*)(smem + o + h * 512));
      if (h == 0) fb[nt] = __builtin_shufflevector(v, v, 0, 1, 2, 3, -1, -1, -1, -1);
      else { const s16x4 lo = __builtin_shufflevector(fb[nt], fb[nt], 0, 1, 2, 3); fb[nt] = __builtin_shufflevector(lo, v, 0, 1, 2, 3, 4, 5, 6, 7); }
    };
    static_assert(C::BMODE != 1 || NT % 4 == 0, "");
    constexpr int GAPS = NR <= NM - 2 ? NR : NM - 2;
    auto half = [&](const s16x8 (&ca)[MT], const s16x8 (&cb)[NT], s16x8 (&na)[MT], s16x8 (&nb)[NT], unsigned xs, unsigned ys, int kk, bool rdnext) {
#pragma unroll
      for (int i = 0; i < NM; ++i) {
        const int nt = i / MT, mt = i % MT;
        acc[mt][nt] = __builtin_amdgcn_mfma_f32_16x16x32_bf16(__builtin_bit_cast(bf16x8, cb[nt]), __builtin_bit_cast(bf16x8, ca[mt]), acc[mt][nt], 0, 0, 0);
        if (rdnext) {
#pragma unroll
          for (int u = (i * NR + GAPS - 1) / GAPS; u < ((i + 1) * NR + GAPS - 1) / GAPS; ++u) if (u < NR) rd(na, nb, xs, ys, kk, u);
        }
        __builtin_amdgcn_sched_barrier(0);
      }
    };

    s16x8 fa0[MT], fb0[NT], fa1[MT], fb1[NT];
    BARRIER();
    TS(1); TSR(7);
#pragma unroll
    for (int i = 0; i < NR; ++i) rd(fa0, fb0, 0, 0, 0, i);
    if (PH == G2 || PH == K4 || PH == K5) {
      const rsrc_t rr = mk_rsrc(wl + (PH == G2 ? O_X : (PH == K4 ? O_Z1 : O_X1)), S_ * OW * 2);
      const unsigned img = (unsigned)(((m0 >> 4) + (j & 1)) * (OW / 32)) * 1024u + (n0 >> 4) * 512 + (j >> 1) * 256 + r * 16;
#pragma unroll
      for (int nt = 0; nt < NT; ++nt) rv[nt] = ld16<16>(rr, img + nt * 512);
      if (PH == K4) { bias[0] = __builtin_bit_cast(float, (unsigned)p.bsp[m0 + r] << 16); bias[1] = __builtin_bit_cast(float, (unsigned)p.bsp[m0 + 16 + r] << 16); }
    }
    __builtin_amdgcn_sched_barrier(0);
    half(fa0, fb0, fa1, fb1, 0, 0, 1, true);
    WAIT_L0();
    unsigned xs = 0, ys = 0;
    auto step = [&]() {
      xs += RX::SLOT; if (xs == RX::D * RX::SLOT) xs = 0;
      ys += RY::SLOT; if (ys == RY::D * RY::SLOT) ys = 0;
      BARRIER();
      __builtin_amdgcn_sched_barrier(0);
      half(fa1, fb1, fa0, fb0, xs, ys, 0, true);
      half(fa0, fb0, fa1, fb1, xs, ys, 1, true);
      WAIT_L0();
    };
#define UNR 1
    if (PH == K3 || !UNR) {
#pragma unroll 1
      for (int s = 1; s < T; ++s) step();
    } else {
      step();
#pragma unroll 1
      for (int s = 2; s < T; s += 2) { step(); step(); }
    }
    BARRIER();
    __builtin_amdgcn_sched_barrier(0);
    half(fa1, fb1, fa0, fb0, 0, 0, 0, false);
    TS(2);
  }

  asm volatile("" : "+v"(lane));
  const int r = lane & 15, j = lane >> 4;
  const unsigned imgl = (j & 1) * (OW / 32) * 1024u + (j >> 1) * 256 + r * 16;
  if (PH == G1) {
    const int hi = n0 >= D_, nn = n0 - hi * D_;
    const rsrc_t ro = mk_rsrc(wl + O_AB, 2 * S_ * D_ * 2);
    const unsigned o = (unsigned)(((hi * S_ + m0) >> 5) * (D_ / 16) + (nn >> 4)) * 1024u + tr_lane(r, j);
#pragma unroll
    for (int mp = 0; mp < 2; ++mp) {
      f32x2 st0 = *(const LAS f32x2*)(smem + ST_OFF + (ml + mp * 32 + r) * 8), st1 = *(const LAS f32x2*)(smem + ST_OFF + (ml + mp * 32 + 16 + r) * 8);
      if (hi) { st0 = -st0; st1 = -st1; }
#pragma unroll
      for (int nt = 0; nt < NT; ++nt) {
        const f32x4 cs = *(const LAS f32x4*)(smem + CS_OFF + (wn * NT * 16 + nt * 16 + j * 4) * 4);
        st16(ro, o + mp * (D_ / 16) * 1024 + nt * 1024, pair_rows(pack4(acc[mp * 2][nt] * st0[0] + cs * st0[1]), pack4(acc[mp * 2 + 1][nt] * st1[0] + cs * st1[1])));
      }
    }
    TS(3);
    signal_done(smem, l * 5 + G1, 4, phase_flags(wl, G1, t.bn) + t.bm, lane);
  } else if (PH == G2 || PH == K5) {
    if (PH == G2) {
      const rsrc_t rx = mk_rsrc(wl + O_XB, 256 * 16384);
      const unsigned so = (blockIdx.x * 4 + wid) * 4096 + lane * 16, go = ((blockIdx.x ^ 1) * 4 + wid) * 4096 + lane * 16;
#pragma unroll
      for (int i = 0; i < 4; ++i) { const u32x2 lo = pack4(acc[MT - 2][i]), hi = pack4(acc[MT - 1][i]); st16(rx, so + i * 1024, (u32x4){lo[0], lo[1], hi[0], hi[1]}); }
      WAIT_V(0);
      unsigned* fl = (unsigned*)(wl + O_XF);
      if (lane == 0) __hip_atomic_store(fl + (blockIdx.x * 4 + wid) * 32, 1u, RLX_AGENT);
      TS(8);
      wait_flags(fl + ((blockIdx.x ^ 1) * 4 + wid) * 32);
      TS(9);
      u32x4 pv[4];
#pragma unroll
      for (int i = 0; i < 4; ++i) pv[i] = ld16<16>(rx, go + i * 1024);
#pragma unroll
      for (int i = 0; i < 4; ++i) { acc[0][i] += unpack4((u32x2){pv[i][0], pv[i][1]}); acc[1][i] += unpack4((u32x2){pv[i][2], pv[i][3]}); }
    }
    u32x2 y[2][4];
#pragma unroll
    for (int nt = 0; nt < 4; ++nt) {
      u32x2 x0, x1; unpair_rows(rv[nt], x0, x1);
      y[0][nt] = pack4(acc[0][nt] + unpack4(x0)); y[1][nt] = pack4(acc[1][nt] + unpack4(x1));
    }
    if (PH == K5 && l == 1) {
      const rsrc_t ro = mk_rsrc(p.out, S_ * D_ * 2);
#pragma unroll
      for (int i = 0; i < 4; ++i)
        __builtin_amdgcn_raw_buffer_store_b128(pair_cols(y[i >> 1][(i & 1) * 2], y[i >> 1][(i & 1) * 2 + 1]), ro, ((m0 + (i >> 1) * 16 + r) * D_ + n0 + (i & 1) * 32 + j * 8) * 2, 0, 0);
      TS(3);
    } else {
      char* wo = PH == G2 ? wl : wl + LSTRIDE;
      const rsrc_t ro = mk_rsrc(wo + (PH == G2 ? O_X1 : O_X), S_ * D_ * 2);
#pragma unroll
      for (int i = 0; i < 4; ++i) st16(ro, (unsigned)((m0 >> 4) * (OW / 32)) * 1024u + ((n0 >> 4) + i) * 512 + imgl, pair_rows(y[0][i], y[1][i]));
      f32x2 ps[2];
#pragma unroll
      for (int mt = 0; mt < 2; ++mt) {
        f32x4 z[4], s4 = {0.f, 0.f, 0.f, 0.f}, q4 = {0.f, 0.f, 0.f, 0.f};
#pragma unroll
        for (int nt = 0; nt < 4; ++nt) { z[nt] = unpack4(y[mt][nt]); s4 += z[nt]; }
        const float mu = sum_groups((s4[0] + s4[1]) + (s4[2] + s4[3])) * (1.0f / 64.0f);
#pragma unroll
        for (int nt = 0; nt < 4; ++nt) { const f32x4 d = z[nt] - mu; q4 += d * d; }
        ps[mt] = (f32x2){mu, sum_groups((q4[0] + q4[1]) + (q4[2] + q4[3]))};
      }
      if (j < 2) __hip_atomic_store((unsigned long long*)(wo + (PH == G2 ? O_ST1 : O_STX)) + (n0 >> 6) * S_ + m0 + j * 16 + r, __builtin_bit_cast(unsigned long long, j ? ps[1] : ps[0]), RLX_AGENT);
      TS(3);
      signal_done(smem, l * 5 + PH, 4, phase_flags(wl, PH, t.bm) + (PH == G2 ? t.bn * 2 + t.kh : t.bn), lane);
    }
  } else if (PH == K3) {
#pragma unroll
    for (int mt = 0; mt < 2; ++mt)
#pragma unroll
      for (int nt = 0; nt < 4; ++nt) *(LAS f32x4*)(smem + A_RING + ((wid * 2 + mt) * 4 + nt) * 1024 + lane * 16) = acc[MT - 2 + mt][NT - 4 + nt];
    WAIT_L0();
    BARRIER();
    TS(8);
    f32x4 a2[2][4], z[2][4];
#pragma unroll
    for (int mt = 0; mt < 2; ++mt)
#pragma unroll
      for (int nt = 0; nt < 4; ++nt) a2[mt][nt] = acc[mt][NT - 4 + nt];
    k3_act(smem, a2, z, ml, wn, r, j);
    TS(9);
    BARRIER();
    TS(10);
    const rsrc_t ro = mk_rsrc(wl + O_Z1, S_ * FH * 2);
#pragma unroll
    for (int mp = 0; mp < 2; ++mp) {
      const f32x2 st0 = *(const LAS f32x2*)(smem + ST_OFF + (ml + mp * 32 + r) * 8), st1 = *(const LAS f32x2*)(smem + ST_OFF + (ml + mp * 32 + 16 + r) * 8);
#pragma unroll
      for (int nt = 0; nt < 4; ++nt) {
        const f32x4 cs = *(const LAS f32x4*)(smem + CS_OFF + (wn * 128 + nt * 16 + j * 4) * 4);
        st16(ro, (unsigned)(((m0 >> 4) + mp * 2) * (OW / 32)) * 1024u + (t.bn * 8 + wn * 4 + nt) * 512 + imgl,
             pair_rows(pack4(gelu_tanh4(acc[(mp * 2) % MT][nt] * st0[0] + cs * st0[1])), pack4(gelu_tanh4(acc[(mp * 2 + 1) % MT][nt] * st1[0] + cs * st1[1]))));
      }
    }
    TS(11);
    BARRIER();
    TS(13);
    k3_norm(smem, wl, z, t.bm, t.bn, ml, wn, l, lane);
    TS(3);
  } else if (PH == K4) {
    const rsrc_t ro = mk_rsrc(wl + O_H, S_ * FH * 2);
#pragma unroll
    for (int nt = 0; nt < NT; ++nt) {
      u32x2 x0, x1; unpair_rows(rv[nt], x0, x1);
      st16(ro, (unsigned)((m0 >> 4) * (OW / 32)) * 1024u + ((n0 >> 4) + nt) * 512 + imgl, pair_rows(pack4((acc[0][nt] + bias[0]) * unpack4(x0)), pack4((acc[1][nt] + bias[1]) * unpack4(x1))));
    }
    TS(3);
    signal_done(smem, l * 5 + K4, 4, phase_flags(wl, K4, t.bm) + t.bn, lane);
  }
  TS(6); TSR(5); TS_OUT(PH);
}

__device__ __forceinline__ void k3_help(LAS char* smem, f32x4 (&z)[2][4], int cw, int lane) {
  BARRIER();
  f32x4 a2[2][4];
#pragma unroll
  for (int mt = 0; mt < 2; ++mt)
#pragma unroll
    for (int nt = 0; nt < 4; ++nt) a2[mt][nt] = *(const LAS f32x4*)(smem + A_RING + ((cw * 2 + mt) * 4 + nt) * 1024 + lane * 16);
  k3_act(smem, a2, z, (cw >> 1) * 64 + 32, cw & 1, lane & 15, lane >> 4);
  BARRIER();
}

template <class C>
__device__ __forceinline__ void act_phase(LAS char* smem, const Params& p, int l, int w, int lane) {
  constexpr int PH = C::PH;
  const int wid = 6 + w;
  TS_DECL; TS(0);
  asm volatile("" : "+v"(lane));
  const Tile<C> t;
  char* wl = p.ws + l * LSTRIDE;
  const Stream<C, 1 - C::WSIDE> st(wl + (PH == G1 ? O_X : (PH == G2 ? O_AB : (PH == K3 ? O_X1 : (PH == K4 ? O_Z2 : O_H)))), t, w, lane);
  if (PH == G1) { if (l) wait_flags(phase_flags(wl - LSTRIDE, K5, t.bm) + (lane & 15)); }
  else if (PH == G2) wait_flags(phase_flags(wl, G1, t.kh * 8 + t.bn) + (lane & 15));
  else if (PH == K4) wait_flags(phase_flags(wl, K3, (t.bn * 112 + (lane & 16 ? 111 : 0)) >> 7) + (lane & 15));
  else wait_flags(phase_flags(wl, PH - 1, t.bm) + (lane & 15));
  TS(1);
  st.fill(smem, 0, 3);
  TS(2);
  if (PH == G1 || PH == K3) {
    unsigned long long g[16];
    st.template run<16>(smem, [&](int step) {
      if (step == 0) {
        const unsigned long long* sp = (const unsigned long long*)(wl + (PH == G1 ? O_STX : O_ST1)) + t.bm * 128 + w * 64 + lane;
#pragma unroll
        for (int i = 0; i < 16; ++i) g[i] = __hip_atomic_load(sp + i * S_, RLX_AGENT);
      } else {
        float s = 0.f, q = 0.f, q2 = 0.f;
#pragma unroll
        for (int i = 0; i < 16; ++i) { s += __builtin_bit_cast(float, (unsigned)g[i]); q += __builtin_bit_cast(float, (unsigned)(g[i] >> 32)); }
        const float mu = s * (1.0f / 16.0f);
#pragma unroll
        for (int i = 0; i < 16; ++i) { const float d = __builtin_bit_cast(float, (unsigned)g[i]) - mu; q2 = __builtin_fmaf(d, d, q2); }
        const float rs = 1.0f / sqrtf(__builtin_fmaf(q2, 64.0f, q) * (1.0f / D_) + LN_EPS);
        *(LAS f32x2*)(smem + ST_OFF + (w * 64 + lane) * 8) = (f32x2){rs, -mu * rs};
      }
    });
  } else {
    st.template run<0>(smem, [](int) {});
  }
  if (PH == K3) {
    f32x4 z[2][4];
    k3_help(smem, z, 2 + w, lane);
    unsigned long long* xrow = (unsigned long long*)(wl + O_XCH) + t.bm * 128 + w * 64 + lane;
    const f32x2 p0 = *(const LAS f32x2*)(smem + PS_OFF + (w * 64 + lane) * 8), p1 = *(const LAS f32x2*)(smem + PS_OFF + (128 + w * 64 + lane) * 8);
    const float d0 = p0[0] - p1[0], mu0 = 0.5f * (p0[0] + p1[0]), q0 = p0[1] + p1[1] + 32.0f * d0 * d0;
    unsigned qb = __builtin_bit_cast(unsigned, q0);
    if (qb == 0xffffffffu) qb = 0x7fc00000u;
    __hip_atomic_store(xrow + t.bn * S_, ((unsigned long long)qb << 32) | __builtin_bit_cast(unsigned, mu0), RLX_AGENT);
    unsigned long long g[14];
    for (unsigned spin = 0;; ++spin) {
      __builtin_amdgcn_s_sleep(8);
      bool ok = true;
#pragma unroll
      for (int i = 0; i < 14; ++i) { g[i] = __hip_atomic_load(xrow + i * S_, RLX_AGENT); ok &= (unsigned)(g[i] >> 32) != 0xffffffffu; }
      if (__all(ok) || spin > SPIN_MAX) break;
    }
    float s = 0.f, q = 0.f, q2 = 0.f;
#pragma unroll
    for (int i = 0; i < 14; ++i) { s += __builtin_bit_cast(float, (unsigned)g[i]); q += __builtin_bit_cast(float, (unsigned)(g[i] >> 32)); }
    const float mu = s * (1.0f / 14.0f);
#pragma unroll
    for (int i = 0; i < 14; ++i) { const float d = __builtin_bit_cast(float, (unsigned)g[i]) - mu; q2 = __builtin_fmaf(d, d, q2); }
    const float rs = 1.0f / sqrtf(__builtin_fmaf(q2, 128.0f, q) * (1.0f / FH) + LN_EPS);
    *(LAS f32x2*)(smem + FS_OFF + (w * 64 + lane) * 8) = (f32x2){rs, mu};
    WAIT_L0();
    BARRIER();
    k3_norm(smem, wl, z, t.bm, t.bn, 64 + 32, w, l, lane);
  }
  TS(3); TS_OUT(PH);
}

template <class C> __device__ __forceinline__ Stream<C, C::WSIDE> wt_stream(const Params& p, int w, int lane) {
  const Tile<C> t;
  const char* src = C::PH == G1 ? (t.bn >= 8 ? p.wdim : p.wdre) : (C::PH == G2 ? (t.kh ? p.wsim : p.wsre) : (C::PH == K3 ? p.win : (C::PH == K4 ? p.wsp : p.wout)));
  return Stream<C, C::WSIDE>(src, t, w, lane);
}
template <class C>
__device__ __forceinline__ void wt_phase(LAS char* smem, const Params& p, int l, int w, int lane, int first) {
  constexpr int PH = C::PH;
  asm volatile("" : "+v"(lane));
  const Tile<C> t;
  const auto st = wt_stream<C>(p, w, lane);
  if ((PH == G1 || PH == K3) && w == 0) {
    const float* cs = (const float*)(p.ws + O_SUM) + (PH == G1 ? t.bn * 128 + (lane & 31) * 4 : 2048 + t.bn * 128 + (lane >> 5) * 64 + ((lane >> 4) & 1) * FH + (lane & 15) * 4);
    __builtin_amdgcn_global_load_lds((const unsigned*)cs, (LAS unsigned*)(smem + CS_OFF), 16, 0, 0);
  }
  st.fill(smem, first, Ring<C, C::WSIDE>::D);
  st.template run<0>(smem, [](int) {});
  if (PH == K3) {
    const auto nx = wt_stream<CK4>(p, w, lane);
    nx.fill(smem, 0, 2);
    f32x4 z[2][4];
    k3_help(smem, z, w, lane);
    nx.fill(smem, 2, Ring<CK4, 0>::D);
    BARRIER();
    k3_norm(smem, p.ws + l * LSTRIDE, z, t.bm, t.bn, 32, w, l, lane);
  }
}

__global__ __launch_bounds__(512, 2) void mega_k(Params p) {
  extern __shared__ __attribute__((aligned(16))) char smem_[];
  LAS char* smem = (LAS char*)smem_;
  const int lane = threadIdx.x & 63, wid = __builtin_amdgcn_readfirstlane(threadIdx.x >> 6);
  const bool k3 = (blockIdx.x >> 3) < 28;
  if (wid < 4) {
    if (threadIdx.x < 16) *(LAS unsigned*)(smem + CNT_OFF + threadIdx.x * 4) = 0u;
    WAIT_L0();
#pragma unroll 1
    for (int l = 0; l < 2; ++l) {
      compute_phase<CG1>(smem, p, l, wid, lane);
      compute_phase<CG2>(smem, p, l, wid, lane);
      if (k3) compute_phase<CK3>(smem, p, l, wid, lane);
      compute_phase<CK4>(smem, p, l, wid, lane);
      compute_phase<CK5>(smem, p, l, wid, lane);
    }
  } else if (wid < 6) {
#pragma unroll 1
    for (int l = 0; l < 2; ++l) {
      wt_phase<CG1>(smem, p, l, wid - 4, lane, 0);
      wt_phase<CG2>(smem, p, l, wid - 4, lane, 0);
      if (k3) wt_phase<CK3>(smem, p, l, wid - 4, lane, 0);
      wt_phase<CK4>(smem, p, l, wid - 4, lane, k3 ? Ring<CK4, 0>::D : 0);
      wt_phase<CK5>(smem, p, l, wid - 4, lane, 0);
    }
  } else {
#pragma unroll 1
    for (int l = 0; l < 2; ++l) {
      act_phase<CG1>(smem, p, l, wid - 6, lane);
      act_phase<CG2>(smem, p, l, wid - 6, lane);
      if (k3) act_phase<CK3>(smem, p, l, wid - 6, lane);
      act_phase<CK4>(smem, p, l, wid - 6, lane);
      act_phase<CK5>(smem, p, l, wid - 6, lane);
    }
  }
}

__global__ __launch_bounds__(256) void prologue_k(const u16* x, const u16* wdre, const u16* wdim, const u16* win, char* ws) {
  __shared__ float red[32][65];
  const int tid = threadIdx.x, lane = tid & 63, wid = tid >> 6, b = blockIdx.x;
  float* wsum = (float*)(ws + O_SUM);
  if (b < 56) {
    const int ch = tid & 7, rg = tid >> 3;
    float s[8] = {0.f, 0.f, 0.f, 0.f, 0.f, 0.f, 0.f, 0.f};
#pragma unroll 8
    for (int k = rg; k < D_; k += 32) {
      const u32x4 v = *(const u32x4*)(win + (size_t)k * F_ + b * 64 + ch * 8);
#pragma unroll
      for (int e = 0; e < 4; ++e) { s[2 * e] += bf_lo(v[e]); s[2 * e + 1] += bf_hi(v[e]); }
    }
#pragma unroll
    for (int e = 0; e < 8; ++e) red[rg][ch * 8 + e] = s[e];
    __syncthreads();
    if (tid < 64) { float t = 0.f; for (int i = 0; i < 32; ++i) t += red[i][tid]; wsum[2048 + b * 64 + tid] = t; }
    return;
  }
  const int gw = (b - 56) * 4 + wid;
  if (gw < 4096) {
    const u16* row = gw < 2048 ? x + (size_t)gw * D_ : (gw < 3072 ? wdre + (size_t)(gw - 2048) * D_ : wdim + (size_t)(gw - 3072) * D_);
    const u32x4 v0 = *(const u32x4*)(row + lane * 16), v1 = *(const u32x4*)(row + lane * 16 + 8);
    float e[16];
#pragma unroll
    for (int i = 0; i < 4; ++i) { e[2 * i] = bf_lo(v0[i]); e[2 * i + 1] = bf_hi(v0[i]); e[8 + 2 * i] = bf_lo(v1[i]); e[9 + 2 * i] = bf_hi(v1[i]); }
    float s = 0.f;
#pragma unroll
    for (int i = 0; i < 16; ++i) s += e[i];
    s += __shfl_xor(s, 1); s += __shfl_xor(s, 2);
    if (gw < 2048) {
      const float mu = s * (1.0f / 64.0f); float q = 0.f;
#pragma unroll
      for (int i = 0; i < 16; ++i) { const float d = e[i] - mu; q = __builtin_fmaf(d, d, q); }
      q += __shfl_xor(q, 1); q += __shfl_xor(q, 2);
      if ((lane & 3) == 0) *(f32x2*)(ws + O_STX + ((size_t)(lane >> 2) * S_ + gw) * 8) = (f32x2){mu, q};
      char* img = ws + O_X + ((size_t)(gw >> 4) * (D_ / 32) + (lane >> 1)) * 1024 + (lane & 1) * 512 + (gw & 15) * 16;
      *(u32x4*)img = v0; *(u32x4*)(img + 256) = v1;
      char* wl = ws + (gw >> 10) * LSTRIDE; const int g = gw & 1023;
      if (lane < 16) *(u32x4*)(wl + O_XCH + (g * 16 + lane) * 16) = (u32x4){~0u, ~0u, ~0u, ~0u};
      else if (lane < 26 && g * 10 + lane - 16 < (128 + 20) * 64) *(u32x4*)(wl + O_XF + (g * 10 + lane - 16) * 16) = (u32x4){0u, 0u, 0u, 0u};
    } else {
      s += __shfl_xor(s, 4); s += __shfl_xor(s, 8); s += __shfl_xor(s, 16); s += __shfl_xor(s, 32);
      if (lane == 0) wsum[gw - 2048] = s;
    }
  }
}

extern "C" void kernel_launch(void* const* d_in, const int* in_sizes, int n_in, void* d_out, int out_size, void* d_ws, size_t ws_size, hipStream_t stream) {
  Params p;
  p.wsre = (const char*)d_in[1]; p.wsim = (const char*)d_in[2]; p.wdre = (const char*)d_in[3]; p.wdim = (const char*)d_in[4];
  p.win = (const char*)d_in[6]; p.wsp = (const char*)d_in[7]; p.wout = (const char*)d_in[9]; p.bsp = (const u16*)d_in[8];
  p.out = (char*)d_out; p.ws = (char*)d_ws;
  hipLaunchKernelGGL(prologue_k, dim3(56 + 1024), dim3(256), 0, stream, (const u16*)d_in[0], (const u16*)p.wdre, (const u16*)p.wdim, (const u16*)p.win, p.ws);
  (void)hipFuncSetAttribute((const void*)mega_k, hipFuncAttributeMaxDynamicSharedMemorySize, LDS_BYTES);
  hipLaunchKernelGGL(mega_k, dim3(256), dim3(512), LDS_BYTES, stream, p);
}
```

```cpp
#include <hip/hip_runtime.h>
#include <hip/hip_bf16.h>
#include <stdint.h>

#define LAS __attribute__((address_space(3)))
typedef unsigned short u16;
typedef short s16x4 __attribute__((ext_vector_type(4)));
typedef short s16x8 __attribute__((ext_vector_type(8)));
typedef __bf16 bf16x8 __attribute__((ext_vector_type(8)));
typedef __bf16 bf16x2 __attribute__((ext_vector_type(2)));
typedef float f32x2 __attribute__((ext_vector_type(2)));
typedef float f32x4 __attribute__((ext_vector_type(4)));
typedef unsigned u32x2 __attribute__((ext_vector_type(2)));
typedef unsigned u32x4 __attribute__((ext_vector_type(4)));

constexpr int S_ = 2048, D_ = 1024, F_ = 3584, FH = 1792;
constexpr float LN_EPS = 1e-5f;

__device__ __forceinline__ unsigned pack_bf16(float lo, float hi) {
  f32x2 v = {lo, hi};
  return __builtin_bit_cast(unsigned, __builtin_convertvector(v, bf16x2));
}
__device__ __forceinline__ float bf_lo(unsigned u) { return __builtin_bit_cast(float, u << 16); }
__device__ __forceinline__ float bf_hi(unsigned u) { return __builtin_bit_cast(float, u & 0xffff0000u); }
__device__ __forceinline__ void swap16(unsigned& a, unsigned& b) { asm volatile("s_nop 1\n\tv_permlane16_swap_b32 %0, %1" : "+v"(a), "+v"(b)); }
__device__ __forceinline__ void swap32(unsigned& a, unsigned& b) { asm volatile("s_nop 1\n\tv_permlane32_swap_b32 %0, %1" : "+v"(a), "+v"(b)); }
__device__ __forceinline__ float sum32(float v) {
  unsigned a = __builtin_bit_cast(unsigned, v), b = a; swap32(a, b);
  return __builtin_bit_cast(float, a) + __builtin_bit_cast(float, b);
}
__device__ __forceinline__ float sum_groups(float v) {
  unsigned a = __builtin_bit_cast(unsigned, v), b = a; swap16(a, b);
  return sum32(__builtin_bit_cast(float, a) + __builtin_bit_cast(float, b));
}
__device__ __forceinline__ u32x4 pair_rows(u32x2 a, u32x2 b) {
  unsigned a0 = a[0], a1 = a[1], b0 = b[0], b1 = b[1]; swap16(a0, b0); swap16(a1, b1);
  return (u32x4){a0, a1, b0, b1};
}
__device__ __forceinline__ void unpair_rows(u32x4 v, u32x2& a, u32x2& b) {
  unsigned a0 = v[0], a1 = v[1], b0 = v[2], b1 = v[3]; swap16(a0, b0); swap16(a1, b1);
  a = (u32x2){a0, a1}; b = (u32x2){b0, b1};
}
__device__ __forceinline__ u32x4 pair_cols(u32x2 a, u32x2 b) {
  unsigned a0 = a[0], a1 = a[1], b0 = b[0], b1 = b[1]; swap32(a0, b0); swap32(a1, b1); swap16(a0, b0); swap16(a1, b1);
  return (u32x4){a0, a1, b0, b1};
}
__device__ __forceinline__ void unpair_cols(u32x4 v, u32x2& a, u32x2& b) {
  unsigned a0 = v[0], a1 = v[1], b0 = v[2], b1 = v[3]; swap16(a0, b0); swap16(a1, b1); swap32(a0, b0); swap32(a1, b1);
  a = (u32x2){a0, a1}; b = (u32x2){b0, b1};
}
__device__ __forceinline__ u32x2 pack4(f32x4 v) { return (u32x2){pack_bf16(v[0], v[1]), pack_bf16(v[2], v[3])}; }
__device__ __forceinline__ f32x4 unpack4(u32x2 y) { return (f32x4){bf_lo(y[0]), bf_hi(y[0]), bf_lo(y[1]), bf_hi(y[1])}; }
constexpr float GC1 = -2.0f * 0.7978845608028654f * 1.4426950408889634f, GC2 = GC1 * 0.044715f;
__device__ __forceinline__ f32x4 gelu_tanh4(f32x4 a, f32x4 k) {
  const f32x4 t = a * (a * a * k[2] + k[1]);
  f32x4 e;
#pragma unroll
  for (int i = 0; i < 4; ++i) e[i] = __builtin_amdgcn_exp2f(t[i]);
  e = e * k[3] + k[3];
#pragma unroll
  for (int i = 0; i < 4; ++i) e[i] = __builtin_amdgcn_rcpf(e[i]);
  return a * e;
}
#define STAUX 16
typedef __amdgpu_buffer_rsrc_t rsrc_t;
__device__ __forceinline__ rsrc_t mk_rsrc(const void* p, unsigned bytes) { return __builtin_amdgcn_make_buffer_rsrc((void*)p, 0, bytes, 0x00020000); }
__device__ __forceinline__ void st16(rsrc_t r, unsigned off, u32x4 v) { __builtin_amdgcn_raw_buffer_store_b128(v, r, off, 0, STAUX); }
template <int AUX> __device__ __forceinline__ u32x4 ld16(rsrc_t r, unsigned off) { return __builtin_amdgcn_raw_buffer_load_b128(r, off, 0, AUX); }
#define WAIT_V(n) asm volatile("s_waitcnt vmcnt(%0)" ::"n"(n) : "memory")
#define WAIT_L(n) __builtin_amdgcn_s_waitcnt(0xc07f | ((n) << 8))
#define WAIT_L0() asm volatile("s_waitcnt lgkmcnt(0)" ::: "memory")
#define BARRIER() do { asm volatile("" ::: "memory"); __builtin_amdgcn_s_barrier(); asm volatile("" ::: "memory"); } while (0)
#define PIN_S(x) asm volatile("" : "+s"(x))

#define RLX_AGENT __ATOMIC_RELAXED, __HIP_MEMORY_SCOPE_AGENT
#define PF_THR 8
#define POLL_SLEEP 1
#define NORM_S 8
#define SIGA_S 4
constexpr unsigned SPIN_MAX = 1u << 18;
typedef unsigned long long u64;
template <int CTRL> __device__ __forceinline__ float dpp_add(float v) {
  return v + __builtin_bit_cast(float, __builtin_amdgcn_update_dpp(0, __builtin_bit_cast(int, v), CTRL, 0xf, 0xf, true));
}
__device__ __forceinline__ float sum8(float v) { return dpp_add<0x141>(dpp_add<0x4e>(dpp_add<0xb1>(v))); }

constexpr size_t MB = 1 << 20, LSTRIDE = 40 * MB;
constexpr size_t O_X = 0, O_X1 = 4 * MB, O_AB = 8 * MB, O_Z2 = 16 * MB, O_H = 24 * MB, O_XB = 32 * MB;
constexpr size_t O_STX = 36 * MB, O_ST1 = O_STX + 256 * 1024;
constexpr size_t O_XCH = O_STX + 512 * 1024;
constexpr size_t O_XF = O_STX + 768 * 1024;
constexpr size_t O_SF = O_XF + 128 * 1024;
constexpr size_t O_SD = 2 * LSTRIDE, O_SI = O_SD + 128 * 1024;
constexpr size_t O_EP = O_SD + 512 * 1024;
enum { F_X0, F_WD, F_WI, F_GA, F_GB, F_G2, F_Z, F_K4, F_K5 };
constexpr unsigned W_RING = 96 * 1024, A_RING = W_RING, A_BYTES = 48 * 1024, CS1_OFF = 144 * 1024, CS3_OFF = CS1_OFF + 512, ST_OFF = CS3_OFF + 1024, PS_OFF = ST_OFF + 2048,
                   FS_OFF = PS_OFF + 2048, CNT_OFF = FS_OFF + 1024, EP_OFF = CNT_OFF + 128;
constexpr unsigned LDS_BYTES = EP_OFF + 16;

enum { GA = 0, G2 = 1, ZZ = 2, K4 = 3, K5 = 4 };

struct Params {
  const char *x, *wsre, *wsim, *wdre, *wdim, *win, *wsp, *wout;
  const u16* bsp;
  char* out;
  char* ws;
};

template <int PH_, int BM_, int BN_, int WM_, int WN_, int AM_, int BMODE_, int T_, int PARTS_, int GN_, int XN_, int PA_, int PB_, int WSIDE_>
struct Cfg {
  static constexpr int PH = PH_, BM = BM_, BN = BN_, WM = WM_, WN = WN_, AM = AM_, BMODE = BMODE_, T = T_, PARTS = PARTS_;
  static constexpr int GN = GN_, XM = 4, XN = XN_, PA = PA_, PB = PB_, WSIDE = WSIDE_;
  static constexpr int MT = BM / WM / 16, NT = BN / WN / 16;
};
using CGA = Cfg<GA, 64,  128, 2, 2, 1, 0, 32, 2, 16, 8, D_ / 32, 2048,    1>;
using CG2 = Cfg<G2, 128, 128, 2, 2, 0, 2, 32, 1, 16, 8, 4096,    D_ / 16, 0>;
using CZZ = Cfg<ZZ, 128, 128, 2, 2, 1, 1, 32, 2, 14, 7, D_ / 32, F_ * 2,  1>;
using CK4 = Cfg<K4, 128, 128, 2, 2, 0, 2, 32, 1, 14, 7, 4096,    FH / 16, 0>;
using CK5 = Cfg<K5, 128, 64,  4, 1, 1, 1, 28, 1, 16, 8, FH / 32, D_ * 2,  1>;

template <class C> struct Tile {
  int bm, bn, kh, cls;
  __device__ __forceinline__ Tile() {
    int bid = blockIdx.x; PIN_S(bid);
    const int xcd = bid & 7, idx = bid >> 3, lm = idx / C::XN, ln = idx % C::XN;
    bm = (xcd >> 1) * C::XM + lm; bn = C::PH == G2 ? ln : (xcd & 1) * C::XN + ln; kh = C::PH == G2 ? xcd & 1 : 0; cls = (lm + ln) & 3;
  }
};
template <class C, int SIDE> struct Ring {
  static constexpr bool ISW = C::WSIDE == SIDE;
  static constexpr unsigned SLOT = (SIDE ? C::BN : C::BM) * 128, BASE = ISW ? 0 : A_RING, D = (ISW ? W_RING : A_BYTES) / SLOT;
};

__device__ __forceinline__ u64* flags(char* wl, int id, int panel) { return (u64*)(wl + O_SF) + (id * 16 + panel) * 16; }
__device__ __forceinline__ u64 tag_of(LAS char* smem) { return *(const volatile LAS u64*)(smem + EP_OFF); }
__device__ __forceinline__ void signal_done(LAS char* smem, int slot, u64* flag, int nf, int lane) {
  const u64 tag = tag_of(smem);
  WAIT_V(0);
  unsigned old = 0;
  if (lane == 0) old = __hip_atomic_fetch_add((LAS unsigned*)(smem + CNT_OFF) + slot, 1u, __ATOMIC_RELAXED, __HIP_MEMORY_SCOPE_WORKGROUP);
  if (__builtin_amdgcn_readfirstlane(old) == 3 && lane < nf) __hip_atomic_store(flag, tag, RLX_AGENT);
}
__device__ __forceinline__ u64 peek(u64* f) { return __hip_atomic_load(f, RLX_AGENT); }
__device__ __forceinline__ void ld8_async(u64& d, const void* sbase, unsigned voff) { asm volatile("s_nop 4\n\tglobal_load_dwordx2 %0, %1, %2 sc1" : "=v"(d) : "v"(voff), "s"(sbase) : "memory"); }
#define LCNT(n) (n)
#define LANDED1(n, a) asm volatile("s_waitcnt vmcnt(%1)" : "+v"(a) : "n"(LCNT(n)) : "memory")
#define LANDED16(n, g) asm volatile("s_waitcnt vmcnt(%16)" : "+v"(g[0]), "+v"(g[1]), "+v"(g[2]), "+v"(g[3]), "+v"(g[4]), "+v"(g[5]), "+v"(g[6]), "+v"(g[7]), \
  "+v"(g[8]), "+v"(g[9]), "+v"(g[10]), "+v"(g[11]), "+v"(g[12]), "+v"(g[13]), "+v"(g[14]), "+v"(g[15]) : "n"(LCNT(n)) : "memory")
__device__ __forceinline__ void wait_flags(LAS char* smem, u64* f, u64 first) {
  const u64 tag = tag_of(smem);
  if (!__all(first == tag)) {
#pragma unroll 1
    for (unsigned spin = 0; spin < SPIN_MAX; ++spin) {
      if (__all(peek(f) == tag)) break;
      if (POLL_SLEEP) __builtin_amdgcn_s_sleep(POLL_SLEEP);
    }
  }
  asm volatile("" ::: "memory");
}

template <class C, int SIDE> struct Stream {
  using R = Ring<C, SIDE>;
  static constexpr int NP = R::SLOT / 2048, T = C::T, D = R::D, MODE = SIDE ? C::BMODE : (C::AM ? 3 : 0), PITCH = SIDE ? C::PB : C::PA;
  static constexpr unsigned KS = MODE == 0 ? 128 : (MODE == 1 ? 64 * PITCH : (MODE == 2 ? 2 * PITCH * 1024 : 2048));
  static_assert(T % 4 == 0 && D * NP <= 63, "");
  rsrc_t rs; unsigned base, voff, ks, slot, swp; int cls, w;
  __device__ __forceinline__ Stream(const char* src, unsigned bytes, const Tile<C>& t, int w_, int lane, bool raw = false) {
    rs = mk_rsrc(src, bytes); w = w_; cls = t.cls; ks = KS; slot = 0; swp = t.kh * 32 * PITCH;
    const int b = SIDE ? t.bn : t.bm;
    const unsigned sw = ((lane & 7) ^ ((w << 2) | (lane >> 4))) << 4;
    if (MODE == 0) { base = ((C::PH == GA ? b & 7 : b) * 128 + w * 8) * PITCH; voff = (lane >> 3) * PITCH + sw; }
    else if (MODE == 1) { base = w * 8 * PITCH + b * C::BN * 2; voff = (lane >> 3) * PITCH + (((lane & 7) ^ ((((lane >> 4) & 1) << 2) | (w << 1))) << 4); }
    else if (MODE == 2) { base = (b * (C::BN / 16) + w) * 1024 + t.kh * T * KS; voff = lane * 16; }
    else if (raw) { base = (b * 128 + w * 8) * (PITCH * 64); voff = (lane >> 3) * (PITCH * 64) + sw; ks = 128; }
    else { base = (b * 8 * PITCH + w) * 1024; voff = lane * 16; }
  }
  __device__ __forceinline__ unsigned poff(int i) const {
    if (MODE == 0) return C::PH == G2 ? (i * 16 * PITCH) ^ swp : i * 16 * PITCH;
    if (MODE == 1) return (i & 3) * 16 * PITCH + (i >> 2) * 128;
    if (MODE == 2) return ((i >> 2) * PITCH + (i & 3) * 2) * 1024;
    return i * PITCH * 1024;
  }
  __device__ __forceinline__ void issue(LAS char* smem, int s, unsigned at, bool thr = false) const {
    constexpr int L = C::PH == K4 || C::PH == K5 ? T : 16;
    const int hi = L == 16 ? s >> 4 : 0;
    int ch = s - hi * L;
    if ((ch & 3) == cls) { ch += 4; if (ch >= L) ch -= L; }
    if (C::PH == G2) ch = 2 * ch + hi;
    unsigned g = base + ch * ks;
    if (C::PH == GA && SIDE == 0) g += hi * (64 * D_ * 2);
    if (C::PH == ZZ && SIDE == 1) g += (1 - hi) * (FH * 2);
#pragma unroll
    for (int i = 0; i < NP; ++i) {
      __builtin_amdgcn_raw_ptr_buffer_load_lds(rs, (LAS void*)(smem + R::BASE + at + (w + 2 * i) * 1024), 16, voff, g + poff(i), 0, R::ISW ? 0 : 16);
      if (PF_THR && thr && (i & 1)) WAIT_V(PF_THR);
    }
  }
  __device__ __forceinline__ void adv() { slot += R::SLOT; if (slot == D * R::SLOT) slot = 0; }
  __device__ __forceinline__ void fill(LAS char* smem, int s0, int s1, bool thr) {
#pragma unroll 1
    for (int s = s0; s < s1; ++s) { issue(smem, s, slot, thr); adv(); }
  }
  __device__ __forceinline__ void start() const {
    WAIT_V((D - 1) * NP);
    BARRIER();
    WAIT_V((D - 2) * NP);
  }
  template <int X, class F> __device__ __forceinline__ void step(LAS char* smem, int s, F pre) {
    BARRIER();
    pre();
    issue(smem, s + D - 1, slot);
    adv();
    WAIT_V((D - 2) * NP + X > 63 ? 63 : (D - 2) * NP + X);
  }
  template <int X = 0> __device__ __forceinline__ void steps(LAS char* smem, int s0, int s1) {
#pragma unroll 1
    for (int s = s0; s < s1; ++s) step<X>(smem, s, [] {});
  }
  template <class F> __device__ __forceinline__ void finish(LAS char* smem, int s0, F last) {
    steps(smem, s0, T - D + 1);
    BARRIER();
    WAIT_V((D - 3) * NP);
#pragma unroll 1
    for (int s = T - D + 2; s < T - 1; ++s) { BARRIER(); WAIT_V(0); }
    BARRIER();
    last();
    BARRIER();
  }
  template <class NX> __device__ __forceinline__ void hand_over(LAS char* smem, NX& nx) {
    constexpr int Q = NP / NX::NP;
    nx.slot = slot;
#pragma unroll 1
    for (int k = 0; k < D; ++k) { BARRIER(); nx.fill(smem, k * Q, k * Q + Q, false); WAIT_V((D - 2) * NP); }
  }
};

#define TS(i)
#define TSR(i)
#define TS_DECL
#define TS_OUT(ph)

__device__ __forceinline__ unsigned tr_lane(int r, int j) { return ((r >> 2) & 1) * 512 + (j & 1) * 256 + (r >> 3) * 128 + (r & 3) * 32 + (j >> 1) * 16; }
__device__ __forceinline__ void unpack8(u32x4 v, float (&e)[8]) {
#pragma unroll
  for (int i = 0; i < 4; ++i) { e[2 * i] = bf_lo(v[i]); e[2 * i + 1] = bf_hi(v[i]); }
}

__device__ __forceinline__ void first_job(LAS char* smem, const Params& p, int wid, int lane) {
  const Tile<CGA> t;
  const unsigned ro = (unsigned)(wid * 32 + (lane >> 3)) * (D_ * 2) + (lane & 7) * 16;
  const char* gx = p.x + (size_t)t.bm * 128 * (D_ * 2) + t.bn * 128 + ro;
  const char* gw = (t.bn >= 8 ? p.wdim : p.wdre) + (size_t)(t.bn & 7) * 128 * (D_ * 2) + t.bm * 128 + ro;
  u64 e0 = 0;
  if (wid == 3) e0 = peek((u64*)(p.ws + O_EP));
  u32x4 vx[4], vw[4];
#pragma unroll
  for (int i = 0; i < 4; ++i) vx[i] = *(const u32x4*)(gx + i * 8 * (D_ * 2));
#pragma unroll
  for (int i = 0; i < 4; ++i) vw[i] = *(const u32x4*)(gw + i * 8 * (D_ * 2));
  if (wid < 2) st16(mk_rsrc(p.ws + wid * LSTRIDE + O_XCH, 256 * 1024), blockIdx.x * 1024 + lane * 16, (u32x4){~0u, ~0u, ~0u, ~0u});
  if (wid == 2 && lane < 32) *(LAS unsigned*)(smem + CNT_OFF + lane * 4) = 0u;
  if (wid == 3) {
    u64 e = (e0 ^ (e0 >> 29)) * 0x9E3779B97F4A7C15ull + 0x632BE59BD9B4E019ull;
    if (e == e0) e = ~e0;
    if (lane == 0) *(LAS u64*)(smem + EP_OFF) = e;
  }
  f32x2 st = {0.f, 0.f}; float sw = 0.f;
#pragma unroll
  for (int i = 0; i < 4; ++i) {
    float e[8]; unpack8(vx[i], e);
    const float mu = sum8(((e[0] + e[1]) + (e[2] + e[3])) + ((e[4] + e[5]) + (e[6] + e[7]))) * (1.0f / 64.0f);
    float q = 0.f;
#pragma unroll
    for (int k = 0; k < 8; ++k) { const float d = e[k] - mu; q = __builtin_fmaf(d, d, q); }
    q = sum8(q);
    unpack8(vw[i], e);
    const float s = sum8(((e[0] + e[1]) + (e[2] + e[3])) + ((e[4] + e[5]) + (e[6] + e[7])));
    if ((lane & 7) == i) { st = (f32x2){mu, q}; sw = s; }
  }
  if ((lane & 7) < 4) {
    const int row = wid * 32 + (lane & 7) * 8 + (lane >> 3);
    __hip_atomic_store((u64*)(p.ws + O_STX) + t.bn * S_ + t.bm * 128 + row, __builtin_bit_cast(u64, st), RLX_AGENT);
    __hip_atomic_store((unsigned*)(p.ws + O_SD) + t.bm * 2048 + t.bn * 128 + row, __builtin_bit_cast(unsigned, sw), RLX_AGENT);
  }
  WAIT_L0();
}

template <class C>
__device__ __forceinline__ void compute_phase(LAS char* smem, const Params& p, int l, int wid, int lane, u32x2 (&zk)[4][4], unsigned& wpos) {
  constexpr int BN = C::BN, MT = C::MT, NT = C::NT, PH = C::PH, TP = C::T / C::PARTS;
  using RX = Ring<C, 0>; using RY = Ring<C, 1>;
  TS_DECL; TSR(4); TS(0);
  asm volatile("" : "+v"(lane));
  const Tile<C> t;
  char* wl = p.ws + l * LSTRIDE;
  const int wm = wid / C::WN, wn = wid % C::WN, n0 = t.bn * BN + wn * (NT * 16);
  const int ml = wm * (MT * 16) + t.kh * 32, m0 = t.bm * 128 + ml;
  constexpr int OW = PH == K4 ? FH : D_;
  f32x4 acc[MT][NT];
  u32x4 rv[4], wv[8];
  float ini[MT];
  const bool wjob = PH == G2 && l == 0 && blockIdx.x < 224;
  constexpr int NA = C::AM == 0 || PH == GA ? 2 : 1, NB = C::BMODE == 0 ? 2 : (C::BMODE == 1 ? 4 : 1);
  unsigned ad[NA + NB];
  const unsigned xs = C::WSIDE ? 0 : wpos, ys = C::WSIDE ? wpos : (PH == G2 ? RY::SLOT : 0);
  {
    const int r = lane & 15, j = lane >> 4;
    if (C::AM == 0 || (PH == GA && l == 0)) { ad[0] = RX::BASE + xs + (wm * MT * 16 + r) * 128 + ((j ^ (r >> 1)) << 4); ad[NA - 1] = ad[0] ^ 64; }
    else { ad[0] = RX::BASE + xs + wm * MT * 2048 + lane * 16; if (NA == 2) ad[1] = ad[0] + 1024; }
    if (C::BMODE == 0) { ad[NA] = RY::BASE + ys + (wn * NT * 16 + r) * 128 + ((j ^ (r >> 1)) << 4); ad[NA + NB - 1] = ad[NA] ^ 64; }
    else if (C::BMODE == 1) {
      const int q = (lane >> 2) & 3, pp = lane & 3, f = ((q >> 1) << 1) | (j & 1);
#pragma unroll
      for (int c = 0; c < NB; ++c) ad[NA + c] = RY::BASE + ys + wn * 8192 + (j * 8 + q) * 128 + ((c ^ f) << 5) + pp * 8;
    } else ad[NA] = RY::BASE + ys + wn * NT * 1024 + lane * 8;
#pragma unroll
    for (int u = 0; u < NA + NB; ++u) ad[u] += (unsigned)(size_t)smem;
#pragma unroll
    for (int mt = 0; mt < MT; ++mt) ini[mt] = PH == K4 ? __builtin_bit_cast(float, (unsigned)p.bsp[m0 + mt * 16 + r] << 16) : 0.f;
  }
  unsigned cx = RX::D - 1 - xs / RX::SLOT, cy = RY::D - 1 - ys / RY::SLOT, dx, dy;
  wpos += C::T * Ring<C, C::WSIDE>::SLOT % W_RING; if (wpos >= W_RING) wpos -= W_RING;
#define RING_NEXT(c, d, R) asm volatile("s_sub_u32 %0, %0, 1\n\ts_cselect_b32 %1, %2, %3\n\ts_cselect_b32 %0, %4, %0" : "+s"(c), "=&s"(d) : "n"(-(int)((R::D - 1) * R::SLOT)), "s"(R::SLOT), "n"(R::D - 1) : "scc")
  static_assert(NT == 4, "");
  constexpr int BR = C::BMODE == 0 ? 1 : 2, NR = MT + NT * BR, NM = MT * NT;
  auto rd = [&](s16x8 (&fa)[MT], s16x8 (&fb)[NT], int kk, int i) {
    if (i >= BR && i < BR + MT) { const int mt = i - BR; fa[mt] = *(const LAS s16x8*)((LAS char*)(size_t)ad[NA == 2 ? kk : 0] + (NA == 2 ? 0 : kk * 1024) + mt * 2048); return; }
    const int ib = i < BR ? i : i - MT, nt = ib / BR, h = ib % BR;
    if (C::BMODE == 0) { fb[nt] = *(const LAS s16x8*)((LAS char*)(size_t)ad[NA + kk] + nt * 2048); return; }
    const s16x4 v = __builtin_amdgcn_ds_read_tr16_b64_v4i16((LAS s16x4*)((LAS char*)(size_t)ad[NA + nt % NB] + (C::BMODE == 1 ? kk * 4096 : (kk * (BN / 16) + nt) * 1024) + h * 512));
    if (h == 0) fb[nt] = __builtin_shufflevector(v, v, 0, 1, 2, 3, -1, -1, -1, -1);
    else { const s16x4 lo = __builtin_shufflevector(fb[nt], fb[nt], 0, 1, 2, 3); fb[nt] = __builtin_shufflevector(lo, v, 0, 1, 2, 3, 4, 5, 6, 7); }
  };
  constexpr int GAPS = NR <= NM - 2 ? NR : NM - 2, FREE = NM - GAPS;
  auto half = [&](const s16x8 (&ca)[MT], const s16x8 (&cb)[NT], s16x8 (&na)[MT], s16x8 (&nb)[NT], int kk, bool rdnext) {
#pragma unroll
    for (int i = 0; i < NM; ++i) {
      const int nt = i / MT, mt = i % MT;
      if (MT == 4 && kk == 1 && i == 0) WAIT_L(NR - BR - MT);
      if (MT == 4 && kk == 1 && i == MT) WAIT_L(MT);
      asm volatile("v_mfma_f32_16x16x32_bf16 %0, %1, %2, %0" : "+v"(acc[mt][nt]) : "v"(cb[nt]), "v"(ca[mt]));
      __builtin_amdgcn_sched_barrier(0);
      if (rdnext) {
#pragma unroll
        for (int u = (i * NR + GAPS - 1) / GAPS; u < ((i + 1) * NR + GAPS - 1) / GAPS; ++u) if (u < NR) rd(na, nb, kk, u);
        if (kk == 0 && i == GAPS) RING_NEXT(cx, dx, RX);
        if (kk == 0 && i == GAPS + 1) RING_NEXT(cy, dy, RY);
        if (kk == 1 && i >= GAPS) {
#pragma unroll
          for (int u = i - GAPS; u < NA + NB; u += FREE) asm volatile("v_add_u32 %0, %1, %0" : "+v"(ad[u]) : "s"(u < NA ? dx : dy));
        }
      }
      __builtin_amdgcn_sched_barrier(0);
    }
  };

  s16x8 fa0[MT], fb0[NT], fa1[MT], fb1[NT];
  RING_NEXT(cx, dx, RX); RING_NEXT(cy, dy, RY);
  BARRIER();
  TS(1); TSR(7);
#pragma unroll
  for (int i = 0; i < NR; ++i) rd(fa0, fb0, 0, i);
  if (PH == G2 || PH == K5) {
    const int r = lane & 15, j = lane >> 4;
    const bool raw = PH == G2 && l == 0;
    const rsrc_t rr = mk_rsrc(raw ? p.x : wl + (PH == G2 ? O_X : O_X1), S_ * D_ * 2);
    const unsigned o = (unsigned)((m0 >> 4) + (j & 1)) * (D_ * 32) + (raw ? r * (D_ * 2) + n0 * 2 + (j >> 1) * 16 : (n0 >> 4) * 512 + (j >> 1) * 256 + r * 16);
#pragma unroll
    for (int nt = 0; nt < 4; ++nt) rv[nt] = __builtin_amdgcn_raw_buffer_load_b128(rr, o, nt * (raw ? 32 : 512), 16);
    if (wjob) {
      const char* g = p.win + (size_t)((blockIdx.x & 15) * 64 + (lane >> 3)) * (F_ * 2) + ((wid >> 1) * FH + (blockIdx.x >> 4) * 128 + (wid & 1) * 64) * 2 + (lane & 7) * 16;
#pragma unroll
      for (int i = 0; i < 8; ++i) wv[i] = *(const u32x4*)(g + i * 8 * (F_ * 2));
    }
  }
  auto step = [&]() {
    BARRIER();
    __builtin_amdgcn_sched_barrier(0);
    half(fa1, fb1, fa0, fb0, 0, true);
    half(fa0, fb0, fa1, fb1, 1, true);
    WAIT_L(0);
  };
#pragma unroll 1
  for (int part = 0; part < C::PARTS; ++part) {
    if (PH != ZZ || part == 0) {
#pragma unroll
      for (int mt = 0; mt < MT; ++mt)
#pragma unroll
        for (int nt = 0; nt < NT; ++nt) acc[mt][nt] = (f32x4){ini[mt], ini[mt], ini[mt], ini[mt]};
    }
#pragma unroll
    for (int mt = 0; mt < MT; ++mt) asm volatile("s_nop 1" : "+v"(acc[mt][0]), "+v"(acc[mt][1]), "+v"(acc[mt][2]), "+v"(acc[mt][3]));
    __builtin_amdgcn_sched_barrier(0);
    half(fa0, fb0, fa1, fb1, 1, true);
    WAIT_L(0);
    step();
    constexpr int NSEG = PH == ZZ ? 3 : (PH == GA || PH == G2 ? 2 : 1);
    int s = 2;
#pragma unroll 1
    for (int seg = 0; seg < NSEG; ++seg) {
      const int e = seg == NSEG - 1 ? TP : (PH == ZZ ? NORM_S + seg * 4 : SIGA_S);
#pragma unroll 1
      for (; s < e; s += 2) { step(); step(); }
      int ln = lane; asm volatile("" : "+v"(ln));
      if (PH == GA && seg == 0 && (part || l == 0)) {
        if (part) signal_done(smem, l * 8 + 0, flags(wl, F_GA, t.bn) + t.bm, 1, ln);
        else signal_done(smem, 16, ln ? flags(wl, F_WD, t.bn) + t.bm : flags(wl, F_X0, t.bm) + t.bn, 2, ln);
      }
      if (PH == G2 && seg == 0) {
        const Tile<CGA> ta;
        signal_done(smem, l * 8 + 1, flags(wl, F_GB, ta.bn) + ta.bm, 1, ln);
      }
      if (PH == ZZ && part && seg == 0) {
        const int r = ln & 15, j = ln >> 4;
        const rsrc_t ro = mk_rsrc(wl + O_Z2, S_ * FH * 2);
        const unsigned o = (unsigned)((m0 >> 5) * (FH / 16) + (n0 >> 4)) * 1024u + tr_lane(r, j);
        TS(10);
#pragma unroll
        for (int mp = 0; mp < 2; ++mp) {
          const f32x2 f0 = *(const LAS f32x2*)(smem + FS_OFF + (ml + mp * 32 + r) * 8), f1 = *(const LAS f32x2*)(smem + FS_OFF + (ml + mp * 32 + 16 + r) * 8);
#pragma unroll
          for (int nt = 0; nt < 4; ++nt) {
            asm volatile("" : "+v"(zk[mp * 2][nt]), "+v"(zk[mp * 2 + 1][nt]));
            st16(ro, o + mp * (FH / 16) * 1024 + nt * 1024, pair_rows(pack4(unpack4(zk[mp * 2][nt]) * f0[0] + f0[1]), pack4(unpack4(zk[mp * 2 + 1][nt]) * f1[0] + f1[1])));
          }
        }
        TS(11);
      }
      if (PH == ZZ && part && seg == 1) {
        signal_done(smem, l * 8 + 3, flags(wl, F_Z, t.bn) + t.bm, 1, ln);
        TS(12);
      }
    }
    BARRIER();
    __builtin_amdgcn_sched_barrier(0);
    half(fa1, fb1, fa0, fb0, 0, C::PARTS > 1);
#pragma unroll
    for (int mt = 0; mt < MT; ++mt) asm volatile("s_nop %4" : "+v"(acc[mt][0]), "+v"(acc[mt][1]), "+v"(acc[mt][2]), "+v"(acc[mt][3]) : "n"(mt ? 0 : 11));
    if (part == C::PARTS - 1) TS(2); else TS(8);

    int ln = lane; asm volatile("" : "+v"(ln));
    const int r = ln & 15, j = ln >> 4;
    if (PH == GA) {
      const int hi = n0 >= D_, nn = n0 - hi * D_;
      const rsrc_t ro = mk_rsrc(wl + O_AB, 2 * S_ * D_ * 2);
      const unsigned o = (unsigned)(((hi * S_ + m0 + part * 64) >> 5) * (D_ / 16) + (nn >> 4)) * 1024u + tr_lane(r, j);
      f32x2 st0 = *(const LAS f32x2*)(smem + ST_OFF + (part * 64 + ml + r) * 16), st1 = *(const LAS f32x2*)(smem + ST_OFF + (part * 64 + ml + 16 + r) * 16);
      if (hi) { st0 = -st0; st1 = -st1; }
#pragma unroll
      for (int nt = 0; nt < NT; ++nt) {
        const f32x4 cs = *(const LAS f32x4*)(smem + CS1_OFF + (wn * 64 + nt * 16 + j * 4) * 4);
        st16(ro, o + nt * 1024, pair_rows(pack4(acc[0][nt] * st0[0] + cs * st0[1]), pack4(acc[1][nt] * st1[0] + cs * st1[1])));
      }
    } else if (PH == ZZ) {
      f32x4 k[MT], cs[4];
#pragma unroll
      for (int mt = 0; mt < MT; ++mt) k[mt] = *(const LAS f32x4*)(smem + ST_OFF + (ml + mt * 16 + r) * 16);
      if (part == 0) {
#pragma unroll
        for (int nt = 0; nt < 4; ++nt) cs[nt] = *(const LAS f32x4*)(smem + CS3_OFF + (128 + wn * 64 + nt * 16 + j * 4) * 4);
#pragma unroll
        for (int mt = 0; mt < MT; ++mt)
#pragma unroll
          for (int nt = 0; nt < 4; ++nt) acc[mt][nt] += cs[nt] * k[mt][0];
      }
      f32x2 ps = {0.f, 0.f};
#pragma unroll
      for (int mt = 0; mt < MT; ++mt) {
        f32x4 z[4];
#pragma unroll
        for (int nt = 0; nt < 4; ++nt) { z[nt] = gelu_tanh4(acc[mt][nt], k[mt]); zk[mt % 4][nt] = pack4(z[nt]); }
        if (part == 0) {
          const f32x4 s4 = (z[0] + z[1]) + (z[2] + z[3]), q4 = z[0] * z[0] + z[1] * z[1] + z[2] * z[2] + z[3] * z[3];
          const float sm = sum_groups((s4[0] + s4[1]) + (s4[2] + s4[3])), q = sum_groups((q4[0] + q4[1]) + (q4[2] + q4[3]));
          if (j == mt) ps = (f32x2){sm, q};
        }
      }
      if (part == 0) {
        *(LAS f32x2*)(smem + PS_OFF + (wn * 128 + ml + ln) * 8) = ps;
#pragma unroll
        for (int nt = 0; nt < 4; ++nt) cs[nt] = *(const LAS f32x4*)(smem + CS3_OFF + (wn * 64 + nt * 16 + j * 4) * 4);
#pragma unroll
        for (int mt = 0; mt < MT; ++mt)
#pragma unroll
          for (int nt = 0; nt < 4; ++nt) acc[mt][nt] = cs[nt] * k[mt][0];
      }
    }
    if (part < C::PARTS - 1) TS(9);
  }

  asm volatile("" : "+v"(lane));
  const int r = lane & 15, j = lane >> 4;
  const unsigned imgl = (j & 1) * (OW / 32) * 1024u + (j >> 1) * 256 + r * 16;
  if (PH == G2 || PH == K5) {
    if (PH == G2) {
      const rsrc_t rx = mk_rsrc(wl + O_XB, 256 * 16384);
      const unsigned so = (blockIdx.x * 4 + wid) * 4096 + lane * 16, go = ((blockIdx.x ^ 1) * 4 + wid) * 4096 + lane * 16;
      const u64 tag = tag_of(smem);
#pragma unroll
      for (int i = 0; i < 4; ++i) { const u32x2 lo = pack4(acc[MT - 2][i]), hi = pack4(acc[MT - 1][i]); st16(rx, so + i * 1024, (u32x4){lo[0], lo[1], hi[0], hi[1]}); }
      WAIT_V(0);
      u64* fl = (u64*)(wl + O_XF);
      if (lane == 0) __hip_atomic_store(fl + (blockIdx.x * 4 + wid) * 16, tag, RLX_AGENT);
      TS(8);
      if (wjob) {
        float s[8], e[8];
        unpack8(wv[0], s);
#pragma unroll
        for (int i = 1; i < 8; ++i) {
          unpack8(wv[i], e);
#pragma unroll
          for (int k = 0; k < 8; ++k) s[k] += e[k];
        }
#pragma unroll
        for (int k = 0; k < 4; ++k) { unsigned a = __builtin_bit_cast(unsigned, s[k]), b = __builtin_bit_cast(unsigned, s[k + 4]); swap32(a, b); s[k] = __builtin_bit_cast(float, a) + __builtin_bit_cast(float, b); }
#pragma unroll
        for (int k = 0; k < 2; ++k) { unsigned a = __builtin_bit_cast(unsigned, s[k]), b = __builtin_bit_cast(unsigned, s[k + 2]); swap16(a, b); s[k] = dpp_add<0x128>(__builtin_bit_cast(float, a) + __builtin_bit_cast(float, b)); }
        const int col = (wid >> 1) * FH + (blockIdx.x >> 4) * 128 + (wid & 1) * 64 + (lane & 7) * 8 + (lane >> 4) * 2;
        if (!(lane & 8)) __hip_atomic_store((u64*)(p.ws + O_SI + ((blockIdx.x & 15) * F_ + col) * 4), __builtin_bit_cast(u64, (f32x2){s[0], s[1]}), RLX_AGENT);
      }
      wait_flags(smem, fl + ((blockIdx.x ^ 1) * 4 + wid) * 16, ~tag);
      TS(9);
      u32x4 pv[4];
#pragma unroll
      for (int i = 0; i < 4; ++i) pv[i] = ld16<16>(rx, go + i * 1024);
#pragma unroll
      for (int i = 0; i < 4; ++i) { acc[0][i] += unpack4((u32x2){pv[i][0], pv[i][1]}); acc[1][i] += unpack4((u32x2){pv[i][2], pv[i][3]}); }
    }
    u32x2 y[2][4];
#pragma unroll
    for (int nt = 0; nt < 4; ++nt) {
      u32x2 x0, x1; unpair_rows(rv[nt], x0, x1);
      y[0][nt] = pack4(acc[0][nt] + unpack4(x0)); y[1][nt] = pack4(acc[1][nt] + unpack4(x1));
    }
    if (PH == K5 && l == 1) {
      const rsrc_t ro = mk_rsrc(p.out, S_ * D_ * 2);
#pragma unroll
      for (int i = 0; i < 4; ++i)
        __builtin_amdgcn_raw_buffer_store_b128(pair_cols(y[i >> 1][(i & 1) * 2], y[i >> 1][(i & 1) * 2 + 1]), ro, ((m0 + (i >> 1) * 16 + r) * D_ + n0 + (i & 1) * 32 + j * 8) * 2, 0, 0);
      if (blockIdx.x == 0 && wid == 0 && lane == 0) __hip_atomic_store((u64*)(p.ws + O_EP), tag_of(smem), RLX_AGENT);
      TS(3);
    } else {
      char* wo = PH == G2 ? wl : wl + LSTRIDE;
      const rsrc_t ro = mk_rsrc(wo + (PH == G2 ? O_X1 : O_X), S_ * D_ * 2);
#pragma unroll
      for (int i = 0; i < 4; ++i) st16(ro, (unsigned)((m0 >> 4) * (OW / 32)) * 1024u + ((n0 >> 4) + i) * 512 + imgl, pair_rows(y[0][i], y[1][i]));
      f32x2 ps[2];
#pragma unroll
      for (int mt = 0; mt < 2; ++mt) {
        f32x4 z[4], s4 = {0.f, 0.f, 0.f, 0.f}, q4 = {0.f, 0.f, 0.f, 0.f};
#pragma unroll
        for (int nt = 0; nt < 4; ++nt) { z[nt] = unpack4(y[mt][nt]); s4 += z[nt]; }
        const float mu = sum_groups((s4[0] + s4[1]) + (s4[2] + s4[3])) * (1.0f / 64.0f);
#pragma unroll
        for (int nt = 0; nt < 4; ++nt) { const f32x4 d = z[nt] - mu; q4 += d * d; }
        ps[mt] = (f32x2){mu, sum_groups((q4[0] + q4[1]) + (q4[2] + q4[3]))};
      }
      if (j < 2) __hip_atomic_store((u64*)(wo + (PH == G2 ? O_ST1 : O_STX)) + (n0 >> 6) * S_ + m0 + j * 16 + r, __builtin_bit_cast(u64, j ? ps[1] : ps[0]), RLX_AGENT);
      TS(3);
      if (PH == G2) signal_done(smem, l * 8 + 2, lane ? flags(wl, F_WI, blockIdx.x >> 4) + (blockIdx.x & 15) : flags(wl, F_G2, t.bm) + t.bn * 2 + t.kh, wjob ? 2 : 1, lane);
      else signal_done(smem, l * 8 + 5, flags(wl, F_K5, t.bm) + t.bn, 1, lane);
    }
  } else if (PH == K4) {
    const rsrc_t ro = mk_rsrc(wl + O_H, S_ * FH * 2);
#pragma unroll
    for (int mp = 0; mp < 2; ++mp)
#pragma unroll
      for (int nt = 0; nt < NT; ++nt)
        st16(ro, (unsigned)(((m0 >> 4) + mp * 2) * (OW / 32)) * 1024u + ((n0 >> 4) + nt) * 512 + imgl,
             pair_rows(pack4(acc[(mp * 2) % MT][nt] * unpack4(zk[mp * 2][nt])), pack4(acc[(mp * 2 + 1) % MT][nt] * unpack4(zk[mp * 2 + 1][nt]))));
    TS(3);
    signal_done(smem, l * 8 + 4, flags(wl, F_K4, t.bm) + t.bn, 1, lane);
  } else {
    TS(3);
  }
  TS(6); TSR(5); TS_OUT(PH);
}

template <int N, int W> __device__ __forceinline__ f32x2 row_stat(const u64 (&g)[16]) {
  float s = 0.f, q = 0.f, q2 = 0.f;
#pragma unroll
  for (int i = 0; i < N; ++i) { s += __builtin_bit_cast(float, (unsigned)g[i]); q += __builtin_bit_cast(float, (unsigned)(g[i] >> 32)); }
  const float mu = s * (1.0f / N);
#pragma unroll
  for (int i = 0; i < N; ++i) { const float d = __builtin_bit_cast(float, (unsigned)g[i]) - mu; q2 = __builtin_fmaf(d, d, q2); }
  return (f32x2){mu, sqrtf(__builtin_fmaf(q2, (float)W, q) * (1.0f / (N * W)) + LN_EPS)};
}

template <class C> __device__ __forceinline__ u64* act_flag(char* wl) {
  const Tile<C> t;
  if (C::PH == GA) return flags(wl - LSTRIDE, F_K5, t.bm);
  if (C::PH == G2) return flags(wl, F_GA, t.kh * 8 + t.bn);
  if (C::PH == ZZ) return flags(wl, F_G2, t.bm);
  if (C::PH == K4) return flags(wl, F_Z, t.bn);
  return flags(wl, F_K4, t.bm);
}
template <class C> __device__ __forceinline__ Stream<C, 1 - C::WSIDE> act_stream(const Params& p, int l, int w, int lane) {
  constexpr int PH = C::PH;
  const Tile<C> t;
  const bool raw = PH == GA && l == 0;
  return Stream<C, 1 - C::WSIDE>(raw ? p.x : p.ws + l * LSTRIDE + (PH == GA ? O_X : (PH == G2 ? O_AB : (PH == ZZ ? O_X1 : (PH == K4 ? O_Z2 : O_H)))),
                                 S_ * 2 * (PH == G2 ? 2 * D_ : (PH == K4 || PH == K5 ? FH : D_)), t, w, lane, raw);
}
template <class C>
__device__ __forceinline__ void act_phase(LAS char* smem, const Params& p, int l, int w, int lane, u64& early) {
  constexpr int PH = C::PH;
  const int wid = 6 + w;
  TS_DECL; TS(0);
  asm volatile("" : "+v"(lane));
  char* wl = p.ws + l * LSTRIDE;
  auto st = act_stream<C>(p, l, w, lane);
  const int fi = PH == K5 && (lane & 15) >= 14 ? 0 : lane & 15;
  if (PH == G2) st.slot = 16384;
  else {
    if (PH != GA || l) wait_flags(smem, act_flag<C>(wl) + fi, PH == K4 ? early : ~tag_of(smem));
    TS(1);
    st.fill(smem, 0, st.D, false);
  }
  st.start();
  TS(2);
  if (PH == GA) {
    auto nx = act_stream<CG2>(p, l, w, lane);
    nx.slot = 16384;
    u64 seen;
    st.steps(smem, 1, 27);
    ld8_async(seen, act_flag<CG2>(wl), fi * 8);
    BARRIER();
    LANDED1(0, seen);
    wait_flags(smem, act_flag<CG2>(wl) + fi, seen);
#pragma unroll 1
    for (int k = 0; k < 3; ++k) { BARRIER(); nx.fill(smem, k, k + 1, false); if (k < 2) BARRIER(); }
  } else {
    st.finish(smem, 1, [&] { if (PH == ZZ) early = peek(act_flag<CK4>(wl) + fi); });
  }
  TS(3); TS_OUT(PH);
}

template <class C> __device__ __forceinline__ Stream<C, C::WSIDE> wt_stream(const Params& p, int w, int lane) {
  constexpr int PH = C::PH;
  const Tile<C> t;
  return Stream<C, C::WSIDE>(PH == GA ? (t.bn >= 8 ? p.wdim : p.wdre) : (PH == G2 ? (t.kh ? p.wsim : p.wsre) : (PH == ZZ ? p.win : (PH == K4 ? p.wsp : p.wout))),
                             2 * (PH == GA ? D_ * D_ : (PH == ZZ ? D_ * F_ : (PH == K5 ? FH * D_ : S_ * S_))), t, w, lane);
}
template <class C, class NEXT>
__device__ __forceinline__ void wt_phase(LAS char* smem, const Params& p, int l, int w, int lane, unsigned& wpos, bool mid) {
  constexpr int PH = C::PH;
  asm volatile("" : "+v"(lane));
  const Tile<C> t;
  char* wl = p.ws + l * LSTRIDE;
  auto st = wt_stream<C>(p, w, lane);
  st.slot = wpos;
  if (PH == GA) st.fill(smem, 0, st.D, l);
  st.start();
  const int row = w * 64 + lane;
  int s1 = 1;
  if (PH == GA || PH == ZZ) {
    constexpr int S0 = PH == GA ? SIGA_S + 4 : 4;
    const unsigned fo = (PH == GA ? (lane & 16 ? F_WD * 16 + t.bn : F_X0 * 16 + t.bm) : F_WI * 16 + t.bn) * 128 + (lane & 15) * 8;
    u64 g[16], c[16], seen;
    st.steps(smem, 1, S0);
    ld8_async(seen, p.ws + O_SF, fo);
    st.steps(smem, S0, S0 + 2);
    LANDED1(16, seen);
    wait_flags(smem, (u64*)(p.ws + O_SF + fo), seen);
#pragma unroll
    for (int i = 0; i < 16; ++i) ld8_async(g[i], wl + (PH == GA ? O_STX : O_ST1) + (i * S_ + t.bm * 128) * 8, row * 8);
#pragma unroll
    for (int i = 0; i < 16; ++i) ld8_async(c[i], PH == GA ? p.ws + O_SD + (i * 2048 + t.bn * 128) * 4 : p.ws + O_SI + (i * F_ + w * FH + t.bn * 128) * 4, lane * 8);
    st.template steps<32>(smem, S0 + 2, S0 + 5);
    LANDED16(24, g);
    LANDED16(24, c);
    {
      const f32x2 m = row_stat<16, 64>(g);
      const float rs = 1.0f / m[1];
      *(LAS f32x4*)(smem + ST_OFF + row * 16) = PH == GA ? (f32x4){rs, -m[0] * rs, 0.f, 0.f} : (f32x4){-m[0], GC1 * rs, GC2 * rs * rs * rs, m[1]};
      f32x2 s = {0.f, 0.f};
#pragma unroll
      for (int i = 0; i < 16; ++i) s += __builtin_bit_cast(f32x2, c[i]);
      *(LAS f32x2*)(smem + (PH == GA ? CS1_OFF + lane * 8 : CS3_OFF + row * 8)) = s;
    }
    WAIT_L0();
    s1 = S0 + 5;
    if (PH == ZZ) {
      u64* xrow = (u64*)(wl + O_XCH) + t.bm * 128 + row;
      st.steps(smem, S0 + 5, 17);
      st.template step<0>(smem, 17, [&] {
        const f32x2 q = *(const LAS f32x2*)(smem + PS_OFF + row * 8) + *(const LAS f32x2*)(smem + PS_OFF + (128 + row) * 8);
        const float mu0 = q[0] * (1.0f / 128.0f), d = q[1] - q[0] * mu0;
        unsigned qb = __builtin_bit_cast(unsigned, d < 0.f ? 0.f : d);
        if (qb == 0xffffffffu) qb = 0x7fc00000u;
        __hip_atomic_store(xrow + t.bn * S_, ((u64)qb << 32) | __builtin_bit_cast(unsigned, mu0), RLX_AGENT);
      });
      st.steps(smem, 18, 13 + NORM_S);
#pragma unroll
      for (int i = 0; i < 14; ++i) ld8_async(g[i], wl + O_XCH + (i * S_ + t.bm * 128) * 8, row * 8);
      st.template steps<14>(smem, 13 + NORM_S, 15 + NORM_S);
      LANDED16(16, g);
#pragma unroll 1
      for (unsigned spin = 0; spin < SPIN_MAX; ++spin) {
        bool ok = true;
#pragma unroll
        for (int i = 0; i < 14; ++i) ok &= (unsigned)(g[i] >> 32) != 0xffffffffu;
        if (__all(ok)) break;
#pragma unroll
        for (int i = 0; i < 14; ++i) ld8_async(g[i], wl + O_XCH + (i * S_ + t.bm * 128) * 8, row * 8);
        LANDED16(0, g);
      }
      const f32x2 m = row_stat<14, 128>(g);
      const float rs = 1.0f / m[1];
      *(LAS f32x2*)(smem + FS_OFF + row * 8) = (f32x2){rs, -m[0] * rs};
      WAIT_L0();
      s1 = 15 + NORM_S;
    }
  } else if (PH == G2) {
    u64* fl = flags(wl, F_GB, t.kh * 8 + t.bn);
    u64 seen;
    st.steps(smem, 1, 12);
    ld8_async(seen, fl, (lane & 15) * 8);
    st.steps(smem, 12, 14);
    LANDED1(16, seen);
    wait_flags(smem, fl + (lane & 15), seen);
    s1 = 14;
  }
  if (PH == K5) st.finish(smem, s1, [] {});
  else {
    st.steps(smem, s1, C::T - st.D + 1);
    if (PH != G2 || mid) { auto nx = wt_stream<NEXT>(p, w, lane); st.hand_over(smem, nx); }
    else { auto nx = wt_stream<CK5>(p, w, lane); st.hand_over(smem, nx); }
  }
  wpos = st.slot;
}

__device__ __forceinline__ void touch(LAS char* smem, const Params& p, int wid, int lane) {
  const int u = ((blockIdx.x >> 3) - 28 + (blockIdx.x & 7) * 4) * 8 + wid;
  BARRIER();
#pragma unroll 1
  for (int i = 0; i < 46; i += 2) {
    const char* g = (i < 32 ? p.wsp + (size_t)(u * 32 + i) * 1024 : p.wout + (size_t)(u * 14 + i - 32) * 1024) + lane * 16;
    __builtin_amdgcn_global_load_lds((const unsigned*)g, (LAS unsigned*)(smem + A_RING + wid * 1024), 16, 0, 0);
    __builtin_amdgcn_global_load_lds((const unsigned*)(g + 1024), (LAS unsigned*)(smem + A_RING + wid * 1024), 16, 0, 0);
    WAIT_V(8);
  }
  WAIT_V(0);
  BARRIER();
}

__global__ __launch_bounds__(512, 2) void mega_k(Params p) {
  extern __shared__ __attribute__((aligned(16))) char smem_[];
  LAS char* smem = (LAS char*)smem_;
  const int lane = threadIdx.x & 63, wid = __builtin_amdgcn_readfirstlane(threadIdx.x >> 6);
  const bool mid = (blockIdx.x >> 3) < 28;
  unsigned wpos = 0;
  if (wid < 4) {
    first_job(smem, p, wid, lane);
#pragma unroll 1
    for (int l = 0; l < 2; ++l) {
      u32x2 zk[4][4];
      compute_phase<CGA>(smem, p, l, wid, lane, zk, wpos);
      compute_phase<CG2>(smem, p, l, wid, lane, zk, wpos);
      if (mid) { compute_phase<CZZ>(smem, p, l, wid, lane, zk, wpos); compute_phase<CK4>(smem, p, l, wid, lane, zk, wpos); }
      else if (l == 0) touch(smem, p, wid, lane);
      compute_phase<CK5>(smem, p, l, wid, lane, zk, wpos);
    }
  } else if (wid < 6) {
#pragma unroll 1
    for (int l = 0; l < 2; ++l) {
      wt_phase<CGA, CG2>(smem, p, l, wid - 4, lane, wpos, mid);
      wt_phase<CG2, CZZ>(smem, p, l, wid - 4, lane, wpos, mid);
      if (mid) { wt_phase<CZZ, CK4>(smem, p, l, wid - 4, lane, wpos, mid); wt_phase<CK4, CK5>(smem, p, l, wid - 4, lane, wpos, mid); }
      else if (l == 0) touch(smem, p, wid, lane);
      wt_phase<CK5, CK5>(smem, p, l, wid - 4, lane, wpos, mid);
    }
  } else {
#pragma unroll 1
    for (int l = 0; l < 2; ++l) {
      u64 early = 0;
      act_phase<CGA>(smem, p, l, wid - 6, lane, early);
      act_phase<CG2>(smem, p, l, wid - 6, lane, early);
      if (mid) { act_phase<CZZ>(smem, p, l, wid - 6, lane, early); act_phase<CK4>(smem, p, l, wid - 6, lane, early); }
      else if (l == 0) touch(smem, p, wid, lane);
      act_phase<CK5>(smem, p, l, wid - 6, lane, early);
    }
  }
}

extern "C" void kernel_launch(void* const* d_in, const int* in_sizes, int n_in, void* d_out, int out_size, void* d_ws, size_t ws_size, hipStream_t stream) {
  Params p;
  p.x = (const char*)d_in[0];
  p.wsre = (const char*)d_in[1]; p.wsim = (const char*)d_in[2]; p.wdre = (const char*)d_in[3]; p.wdim = (const char*)d_in[4];
  p.win = (const char*)d_in[6]; p.wsp = (const char*)d_in[7]; p.wout = (const char*)d_in[9]; p.bsp = (const u16*)d_in[8];
  p.out = (char*)d_out; p.ws = (char*)d_ws;
  (void)hipFuncSetAttribute((const void*)mega_k, hipFuncAttributeMaxDynamicSharedMemorySize, LDS_BYTES);
  hipLaunchKernelGGL(mega_k, dim3(256), dim3(512), LDS_BYTES, stream, p);
}
```

```cpp
#include <hip/hip_runtime.h>
#include <hip/hip_bf16.h>
#include <stdint.h>

#define LAS __attribute__((address_space(3)))
typedef unsigned short u16;
typedef short s16x4 __attribute__((ext_vector_type(4)));
typedef short s16x8 __attribute__((ext_vector_type(8)));
typedef __bf16 bf16x8 __attribute__((ext_vector_type(8)));
typedef __bf16 bf16x2 __attribute__((ext_vector_type(2)));
typedef float f32x2 __attribute__((ext_vector_type(2)));
typedef float f32x4 __attribute__((ext_vector_type(4)));
typedef unsigned u32x2 __attribute__((ext_vector_type(2)));
typedef unsigned u32x4 __attribute__((ext_vector_type(4)));

constexpr int S_ = 2048, D_ = 1024, F_ = 3584, FH = 1792;
constexpr float LN_EPS = 1e-5f;

__device__ __forceinline__ unsigned pack_bf16(float lo, float hi) {
  f32x2 v = {lo, hi};
  return __builtin_bit_cast(unsigned, __builtin_convertvector(v, bf16x2));
}
__device__ __forceinline__ float bf_lo(unsigned u) { return __builtin_bit_cast(float, u << 16); }
__device__ __forceinline__ float bf_hi(unsigned u) { return __builtin_bit_cast(float, u & 0xffff0000u); }
__device__ __forceinline__ void swap16(unsigned& a, unsigned& b) { asm volatile("s_nop 1\n\tv_permlane16_swap_b32 %0, %1" : "+v"(a), "+v"(b)); }
__device__ __forceinline__ void swap32(unsigned& a, unsigned& b) { asm volatile("s_nop 1\n\tv_permlane32_swap_b32 %0, %1" : "+v"(a), "+v"(b)); }
__device__ __forceinline__ float sum32(float v) {
  unsigned a = __builtin_bit_cast(unsigned, v), b = a; swap32(a, b);
  return __builtin_bit_cast(float, a) + __builtin_bit_cast(float, b);
}
__device__ __forceinline__ float sum_groups(float v) {
  unsigned a = __builtin_bit_cast(unsigned, v), b = a; swap16(a, b);
  return sum32(__builtin_bit_cast(float, a) + __builtin_bit_cast(float, b));
}
__device__ __forceinline__ u32x4 pair_rows(u32x2 a, u32x2 b) {
  unsigned a0 = a[0], a1 = a[1], b0 = b[0], b1 = b[1]; swap16(a0, b0); swap16(a1, b1);
  return (u32x4){a0, a1, b0, b1};
}
__device__ __forceinline__ void unpair_rows(u32x4 v, u32x2& a, u32x2& b) {
  unsigned a0 = v[0], a1 = v[1], b0 = v[2], b1 = v[3]; swap16(a0, b0); swap16(a1, b1);
  a = (u32x2){a0, a1}; b = (u32x2){b0, b1};
}
__device__ __forceinline__ u32x4 pair_cols(u32x2 a, u32x2 b) {
  unsigned a0 = a[0], a1 = a[1], b0 = b[0], b1 = b[1]; swap32(a0, b0); swap32(a1, b1); swap16(a0, b0); swap16(a1, b1);
  return (u32x4){a0, a1, b0, b1};
}
__device__ __forceinline__ void unpair_cols(u32x4 v, u32x2& a, u32x2& b) {
  unsigned a0 = v[0], a1 = v[1], b0 = v[2], b1 = v[3]; swap16(a0, b0); swap16(a1, b1); swap32(a0, b0); swap32(a1, b1);
  a = (u32x2){a0, a1}; b = (u32x2){b0, b1};
}
__device__ __forceinline__ u32x2 pack4(f32x4 v) { return (u32x2){pack_bf16(v[0], v[1]), pack_bf16(v[2], v[3])}; }
__device__ __forceinline__ f32x4 unpack4(u32x2 y) { return (f32x4){bf_lo(y[0]), bf_hi(y[0]), bf_lo(y[1]), bf_hi(y[1])}; }
constexpr float GC1 = -2.0f * 0.7978845608028654f * 1.4426950408889634f, GC2 = GC1 * 0.044715f;
__device__ __forceinline__ f32x4 gelu_tanh4(f32x4 a, f32x4 k) {
  const f32x4 t = a * (a * a * k[2] + k[1]);
  f32x4 e;
#pragma unroll
  for (int i = 0; i < 4; ++i) e[i] = __builtin_amdgcn_exp2f(t[i]);
  e = e * k[3] + k[3];
#pragma unroll
  for (int i = 0; i < 4; ++i) e[i] = __builtin_amdgcn_rcpf(e[i]);
  return a * e;
}
#define STAUX 16
typedef __amdgpu_buffer_rsrc_t rsrc_t;
__device__ __forceinline__ rsrc_t mk_rsrc(const void* p, unsigned bytes) { return __builtin_amdgcn_make_buffer_rsrc((void*)p, 0, bytes, 0x00020000); }
__device__ __forceinline__ void st16(rsrc_t r, unsigned off, u32x4 v) { __builtin_amdgcn_raw_buffer_store_b128(v, r, off, 0, STAUX); }
template <int AUX> __device__ __forceinline__ u32x4 ld16(rsrc_t r, unsigned off) { return __builtin_amdgcn_raw_buffer_load_b128(r, off, 0, AUX); }
#define WAIT_V(n) asm volatile("s_waitcnt vmcnt(%0)" ::"n"(n) : "memory")
#define WAIT_L(n) __builtin_amdgcn_s_waitcnt(0xc07f | ((n) << 8))
#define WAIT_L0() asm volatile("s_waitcnt lgkmcnt(0)" ::: "memory")
#define BARRIER() do { asm volatile("" ::: "memory"); __builtin_amdgcn_s_barrier(); asm volatile("" ::: "memory"); } while (0)
#define PIN_S(x) asm volatile("" : "+s"(x))

#define RLX_AGENT __ATOMIC_RELAXED, __HIP_MEMORY_SCOPE_AGENT
#define PF_THR 8
#define POLL_SLEEP 1
#define NORM_S 8
#define SIGA_S 4
#define PFA 4
#define PFB 8
constexpr unsigned SPIN_MAX = 1u << 18;
typedef unsigned long long u64;
template <int CTRL> __device__ __forceinline__ float dpp_add(float v) {
  return v + __builtin_bit_cast(float, __builtin_amdgcn_update_dpp(0, __builtin_bit_cast(int, v), CTRL, 0xf, 0xf, true));
}
__device__ __forceinline__ float sum8(float v) { return dpp_add<0x141>(dpp_add<0x4e>(dpp_add<0xb1>(v))); }

constexpr size_t MB = 1 << 20, LSTRIDE = 40 * MB;
constexpr size_t O_X = 0, O_X1 = 4 * MB, O_AB = 8 * MB, O_Z2 = 16 * MB, O_H = 24 * MB, O_XB = 32 * MB;
constexpr size_t O_STX = 36 * MB, O_ST1 = O_STX + 256 * 1024;
constexpr size_t O_XCH = O_STX + 512 * 1024;
constexpr size_t O_SF = O_STX + 768 * 1024;
constexpr size_t O_SD = 2 * LSTRIDE, O_SI = O_SD + 128 * 1024;
constexpr size_t O_EP = O_SD + 512 * 1024;
enum { F_X0, F_WD, F_WI, F_GA, F_GB, F_G2, F_Z, F_K4, F_K5, F_XC };
constexpr unsigned W_RING = 96 * 1024, A_RING = W_RING, A_BYTES = 48 * 1024, CS1_OFF = 144 * 1024, CS3_OFF = CS1_OFF + 512, ST_OFF = CS3_OFF + 1024, PS_OFF = ST_OFF + 2048,
                   FS_OFF = PS_OFF + 2048, BS_OFF = FS_OFF + 1024, CNT_OFF = BS_OFF + 512, EP_OFF = CNT_OFF + 128;
constexpr unsigned LDS_BYTES = EP_OFF + 256;

enum { GA = 0, G2 = 1, ZZ = 2, K4 = 3, K5 = 4 };

struct Params {
  const char *x, *wsre, *wsim, *wdre, *wdim, *win, *wsp, *wout;
  const u16* bsp;
  char* out;
  char* ws;
};

template <int PH_, int BM_, int BN_, int WM_, int WN_, int AM_, int BMODE_, int T_, int PARTS_, int GN_, int XN_, int PA_, int PB_, int WSIDE_>
struct Cfg {
  static constexpr int PH = PH_, BM = BM_, BN = BN_, WM = WM_, WN = WN_, AM = AM_, BMODE = BMODE_, T = T_, PARTS = PARTS_;
  static constexpr int GN = GN_, XM = 4, XN = XN_, PA = PA_, PB = PB_, WSIDE = WSIDE_;
  static constexpr int MT = BM / WM / 16, NT = BN / WN / 16;
};
using CGA = Cfg<GA, 64,  128, 2, 2, 1, 0, 32, 2, 16, 8, D_ / 32, 2048,    1>;
using CG2 = Cfg<G2, 128, 128, 2, 2, 0, 2, 32, 1, 16, 8, 4096,    D_ / 16, 0>;
using CZZ = Cfg<ZZ, 128, 128, 2, 2, 1, 1, 32, 2, 14, 7, D_ / 32, F_ * 2,  1>;
using CK4 = Cfg<K4, 128, 128, 2, 2, 0, 2, 32, 1, 14, 7, 4096,    FH / 16, 0>;
using CK5 = Cfg<K5, 128, 64,  4, 1, 1, 1, 28, 1, 16, 8, FH / 32, D_ * 2,  1>;

template <class C> struct Tile {
  int bm, bn, kh, cls;
  __device__ __forceinline__ Tile() {
    int bid = blockIdx.x; PIN_S(bid);
    const int xcd = bid & 7, idx = bid >> 3, lm = idx / C::XN, ln = idx % C::XN;
    bm = (xcd >> 1) * C::XM + lm; bn = C::PH == G2 ? ln : (xcd & 1) * C::XN + ln; kh = C::PH == G2 ? xcd & 1 : 0; cls = (lm + ln) & 3;
  }
};
template <class C, int SIDE> struct Ring {
  static constexpr bool ISW = C::WSIDE == SIDE;
  static constexpr unsigned SLOT = (SIDE ? C::BN : C::BM) * 128, BASE = ISW ? 0 : A_RING, D = (ISW ? W_RING : A_BYTES) / SLOT;
};

#define FW 32
struct Flag { u64 v[FW]; };
__device__ __forceinline__ Flag* flags(char* wl, int id, int panel) { return (Flag*)(wl + O_SF) + (id * 16 + panel) * 16; }
__device__ __forceinline__ u64 tag_of(LAS char* smem) { return *(const volatile LAS u64*)(smem + EP_OFF); }
__device__ __forceinline__ void signal_done(LAS char* smem, int slot, Flag* flag, int nf, int lane) {
  const u64 tag = tag_of(smem);
  WAIT_V(0);
  unsigned old = 0;
  if (lane == 0) old = __hip_atomic_fetch_add((LAS unsigned*)(smem + CNT_OFF) + slot, 1u, __ATOMIC_RELAXED, __HIP_MEMORY_SCOPE_WORKGROUP);
  if (__builtin_amdgcn_readfirstlane(old) == 3 && lane < nf) __hip_atomic_store(flag->v, tag, RLX_AGENT);
}
__device__ __forceinline__ u64 peek(u64* f) { return __hip_atomic_load(f, RLX_AGENT); }
__device__ __forceinline__ void ld8_async(u64& d, const void* sbase, unsigned voff) { asm volatile("s_nop 4\n\tglobal_load_dwordx2 %0, %1, %2 sc1" : "=v"(d) : "v"(voff), "s"(sbase) : "memory"); }
#define LCNT(n) (n)
#define LANDED1(n, a) asm volatile("s_waitcnt vmcnt(%1)" : "+v"(a) : "n"(LCNT(n)) : "memory")
__device__ __forceinline__ void ld16_async(f32x4& d, const void* sbase, unsigned voff) { asm volatile("s_nop 4\n\tglobal_load_dwordx4 %0, %1, %2 sc1" : "=v"(d) : "v"(voff), "s"(sbase) : "memory"); }
#define LANDED8(n, g) asm volatile("s_waitcnt vmcnt(%8)" : "+v"(g[0]), "+v"(g[1]), "+v"(g[2]), "+v"(g[3]), "+v"(g[4]), "+v"(g[5]), "+v"(g[6]), "+v"(g[7]) : "n"(LCNT(n)) : "memory")
__device__ __forceinline__ u64* stat_slot(char* base, int g, int row) { return (u64*)base + ((g >> 1) * S_ + row) * 2 + (g & 1); }
__device__ __forceinline__ void wait_flags(LAS char* smem, Flag* f, u64 first) {
  const u64 tag = tag_of(smem);
  if (!__all(first == tag)) {
#pragma unroll 1
    for (unsigned spin = 0; spin < SPIN_MAX; ++spin) {
      if (__all(peek(f->v) == tag)) break;
      if (POLL_SLEEP) __builtin_amdgcn_s_sleep(POLL_SLEEP);
    }
  }
  asm volatile("" ::: "memory");
}

template <class C, int SIDE> struct Stream {
  using R = Ring<C, SIDE>;
  static constexpr int NP = R::SLOT / 2048, T = C::T, D = R::D, MODE = SIDE ? C::BMODE : (C::AM ? 3 : 0), PITCH = SIDE ? C::PB : C::PA;
  static constexpr unsigned KS = MODE == 0 ? 128 : (MODE == 1 ? 64 * PITCH : (MODE == 2 ? 2 * PITCH * 1024 : 2048));
  static_assert(T % 4 == 0 && D * NP <= 63, "");
  rsrc_t rs; unsigned base, voff, ks, slot, swp; int cls, w;
  __device__ __forceinline__ Stream(const char* src, unsigned bytes, const Tile<C>& t, int w_, int lane, bool raw = false) {
    rs = mk_rsrc(src, bytes); w = w_; cls = t.cls; ks = KS; slot = 0; swp = t.kh * 32 * PITCH;
    const int b = SIDE ? t.bn : t.bm;
    const unsigned sw = ((lane & 7) ^ ((w << 2) | (lane >> 4))) << 4;
    if (MODE == 0) { base = ((C::PH == GA ? b & 7 : b) * 128 + w * 8) * PITCH; voff = (lane >> 3) * PITCH + sw; }
    else if (MODE == 1) { base = w * 8 * PITCH + b * C::BN * 2; voff = (lane >> 3) * PITCH + (((lane & 7) ^ ((((lane >> 4) & 1) << 2) | (w << 1))) << 4); }
    else if (MODE == 2) { base = (b * (C::BN / 16) + w) * 1024 + t.kh * T * KS; voff = lane * 16; }
    else if (raw) { base = (b * 128 + w * 8) * (PITCH * 64); voff = (lane >> 3) * (PITCH * 64) + sw; ks = 128; }
    else { base = (b * 8 * PITCH + w) * 1024; voff = lane * 16; }
  }
  __device__ __forceinline__ unsigned poff(int i) const {
    if (MODE == 0) return C::PH == G2 ? (i * 16 * PITCH) ^ swp : i * 16 * PITCH;
    if (MODE == 1) return (i & 3) * 16 * PITCH + (i >> 2) * 128;
    if (MODE == 2) return ((i >> 2) * PITCH + (i & 3) * 2) * 1024;
    return i * PITCH * 1024;
  }
  __device__ __forceinline__ void issue(LAS char* smem, int s, unsigned at, bool thr = false) const {
    constexpr int L = T / C::PARTS;
    const int hi = L == 16 ? s >> 4 : 0;
    int ch = s - hi * L;
#define LEADMASK 23
    if ((LEADMASK >> C::PH & 1) && (ch & 3) == cls) { ch += 4; if (ch >= L) ch -= L; }
    if (C::PH == G2) ch = ch < 16 ? 2 * ch : 2 * ch - 31;
    unsigned g = base + ch * ks;
    if (C::PH == GA && SIDE == 0) g += hi * (64 * D_ * 2);
    if (C::PH == ZZ && SIDE == 1) g += (1 - hi) * (FH * 2);
#pragma unroll
    for (int i = 0; i < NP; ++i) {
      __builtin_amdgcn_raw_ptr_buffer_load_lds(rs, (LAS void*)(smem + R::BASE + at + (w + 2 * i) * 1024), 16, voff, g + poff(i), 0, R::ISW ? 0 : 16);
      if (PF_THR && thr && (i & 1)) WAIT_V(PF_THR);
    }
  }
  __device__ __forceinline__ void adv() { slot += R::SLOT; if (slot == D * R::SLOT) slot = 0; }
  __device__ __forceinline__ void fill(LAS char* smem, int s0, int s1, bool thr) {
#pragma unroll 1
    for (int s = s0; s < s1; ++s) { issue(smem, s, slot, thr); adv(); }
  }
  __device__ __forceinline__ void start() const {
    WAIT_V((D - 1) * NP);
    BARRIER();
    WAIT_V((D - 2) * NP);
  }
  __device__ __forceinline__ void ramp(LAS char* smem) {
    static_assert(C::PH != GA || D == 6, "");
    fill(smem, 0, 2, false); WAIT_V(NP); BARRIER();
    fill(smem, 2, 4, false); WAIT_V(2 * NP); BARRIER();
    fill(smem, 4, 6, false); WAIT_V(3 * NP); BARRIER();
    fill(smem, 6, 8, false); WAIT_V(4 * NP);
  }
  template <int X, class F> __device__ __forceinline__ void step(LAS char* smem, int s, F pre) {
    BARRIER();
    pre();
    issue(smem, s + D - 1, slot);
    adv();
    WAIT_V((D - 2) * NP + X > 63 ? 63 : (D - 2) * NP + X);
  }
  template <int X = 0> __device__ __forceinline__ void steps(LAS char* smem, int s0, int s1) {
#pragma unroll 1
    for (int s = s0; s < s1; ++s) step<X>(smem, s, [] {});
  }
  template <int N, int STR, class F> __device__ __forceinline__ void steps_pf(LAS char* smem, int s0, int s1, unsigned& junk, F addr) {
#pragma unroll 1
    for (int s = s0; s < s1; ++s) {
      step<0>(smem, s, [] {});
      const char* a = addr(s);
#pragma unroll
      for (int i = 0; i < N; ++i) asm volatile("s_load_dword %0, %1, %2" : "+s"(junk) : "s"(a), "n"((i >> 1) * STR + (i & 1) * 64));
    }
  }
  template <class F> __device__ __forceinline__ void finish(LAS char* smem, int s0, F last) {
    steps(smem, s0, T - D + 1);
    BARRIER();
    WAIT_V((D - 3) * NP);
#pragma unroll 1
    for (int s = T - D + 2; s < T - 1; ++s) { BARRIER(); WAIT_V(0); }
    BARRIER();
    last();
    BARRIER();
  }
  template <class NX, class F> __device__ __forceinline__ void hand_over(LAS char* smem, NX& nx, F pre) {
    constexpr int Q = NP / NX::NP;
    nx.slot = slot;
#pragma unroll 1
    for (int k = 0; k < D; ++k) { BARRIER(); pre(T - D + 1 + k); nx.fill(smem, k * Q, k * Q + Q, false); WAIT_V((D - 2) * NP); }
  }
};

#define TS(i)
#define TSR(i)
#define TS_DECL
#define TS_OUT(ph)

__device__ __forceinline__ unsigned tr_lane(int r, int j) { return ((r >> 2) & 1) * 512 + (j & 1) * 256 + (r >> 3) * 128 + (r & 3) * 32 + (j >> 1) * 16; }
__device__ __forceinline__ void unpack8(u32x4 v, float (&e)[8]) {
#pragma unroll
  for (int i = 0; i < 4; ++i) { e[2 * i] = bf_lo(v[i]); e[2 * i + 1] = bf_hi(v[i]); }
}

__device__ __forceinline__ float dot2(float a, float b, float c) { return __builtin_amdgcn_fdot2_f32_bf16(__builtin_bit_cast(bf16x2, a), __builtin_bit_cast(bf16x2, b), c, false); }
#define FIRST_SLEEP 4
__device__ __forceinline__ void first_reads(LAS char* smem, const Params& p, int wid, int lane, f32x4 (&v)[8], u64 (&tch)[2]) {
  const Tile<CGA> t;
  const char* gx = p.x + (t.bm * 128 + t.bn * 8 + wid * 2) * (D_ * 2);
  const char* gw = (t.bn >= 8 ? p.wdim : p.wdre) + ((t.bn & 7) * 128 + t.bm * 8 + wid * 2) * (D_ * 2);
  __builtin_amdgcn_s_sleep(FIRST_SLEEP);
#pragma unroll
  for (int i = 0; i < 8; ++i) ld16_async(v[i], (i < 4 ? gx : gw) + (i & 3) * 1024, lane * 16);
  const Tile<CG2> u; const Tile<CZZ> z;
  ld8_async(tch[0], (u.kh ? p.wsim : p.wsre) + (u.bm * 128 + u.bn * 16 + wid * 4) * (S_ * 2), (lane >> 4) * (S_ * 2) + ((lane >> 1) & 7) * 256 + (lane & 1) * 64);
  ld8_async(tch[1], p.win + ((z.bm & 15) * 40 + wid * 10) * (F_ * 2) + (FH + z.bn * 128) * 2, (lane >> 2) % 10 * (F_ * 2) + (lane & 3) * 64);
  if (wid == 2 && lane < 32) *(LAS unsigned*)(smem + CNT_OFF + lane * 4) = 0u;
  if (wid == 3) {
    u64 e0;
    asm volatile("s_load_dwordx2 %0, %1, 0x0\n\ts_waitcnt lgkmcnt(0)" : "=s"(e0) : "s"(p.ws + O_EP) : "memory");
    u64 e = (e0 ^ (e0 >> 29)) * 0x9E3779B97F4A7C15ull + 0x632BE59BD9B4E019ull;
    if (e == e0) e = ~e0;
    if (lane == 0) *(LAS u64*)(smem + EP_OFF) = e;
  }
  WAIT_L0();
}
__device__ __forceinline__ void first_sums(const Params& p, int wid, int lane, f32x4 (&v)[8], u64 (&tch)[2]) {
  const Tile<CGA> t;
  LANDED1(0, tch[0]); LANDED1(0, tch[1]); LANDED8(0, v);
  const float ones = __builtin_bit_cast(float, 0x3f803f80u);
  f32x2 st = {0.f, 0.f}; float sw = 0.f;
#pragma unroll
  for (int i = 0; i < 4; ++i) {
    float a = 0.f, q = 0.f, b = 0.f;
#pragma unroll
    for (int k = 0; k < 4; ++k) { a = dot2(v[i][k], ones, a); q = dot2(v[i][k], v[i][k], q); b = dot2(v[4 + i][k], ones, b); }
    a = sum8(a); q = sum8(q); b = sum8(b);
    if ((lane & 7) == i) { st = (f32x2){a, q}; sw = b; }
  }
  if ((lane & 7) < 4) {
    const int row = wid * 2 + ((lane >> 1) & 1), g = (lane & 1) * 8 + (lane >> 3);
    __hip_atomic_store(stat_slot(p.ws + O_STX, g, t.bm * 128 + t.bn * 8 + row), __builtin_bit_cast(u64, st), RLX_AGENT);
    __hip_atomic_store((unsigned*)(p.ws + O_SD) + ((g >> 2) * 2048 + t.bn * 128 + t.bm * 8 + row) * 4 + (g & 3), __builtin_bit_cast(unsigned, sw), RLX_AGENT);
  }
  if (wid < 2) st16(mk_rsrc(p.ws + wid * LSTRIDE + O_XCH, 256 * 1024), blockIdx.x * 1024 + lane * 16, (u32x4){~0u, ~0u, ~0u, ~0u});
}
__device__ __forceinline__ void column_sums(u64* dst, int lane, f32x4 (&v)[4], f32x4 (&w)[4]) {
  asm volatile("s_waitcnt vmcnt(0)" : "+v"(v[0]), "+v"(v[1]), "+v"(v[2]), "+v"(v[3]), "+v"(w[0]), "+v"(w[1]), "+v"(w[2]), "+v"(w[3]) :: "memory");
  float s[8], e[8];
  unpack8(__builtin_bit_cast(u32x4, v[0]), s);
#pragma unroll
  for (int i = 1; i < 8; ++i) {
    unpack8(__builtin_bit_cast(u32x4, i < 4 ? v[i & 3] : w[i & 3]), e);
#pragma unroll
    for (int k = 0; k < 8; ++k) s[k] += e[k];
  }
#pragma unroll
  for (int k = 0; k < 4; ++k) { unsigned a = __builtin_bit_cast(unsigned, s[k]), b = __builtin_bit_cast(unsigned, s[k + 4]); swap32(a, b); s[k] = __builtin_bit_cast(float, a) + __builtin_bit_cast(float, b); }
#pragma unroll
  for (int k = 0; k < 2; ++k) { unsigned a = __builtin_bit_cast(unsigned, s[k]), b = __builtin_bit_cast(unsigned, s[k + 2]); swap16(a, b); s[k] = dpp_add<0x128>(__builtin_bit_cast(float, a) + __builtin_bit_cast(float, b)); }
  if (!(lane & 8)) __hip_atomic_store(dst + (lane & 7) * 8 + (lane >> 4) * 2, __builtin_bit_cast(u64, (f32x2){s[0], s[1]}), RLX_AGENT);
}

template <class C>
__device__ __forceinline__ void compute_phase(LAS char* smem, const Params& p, int l, int wid, int lane, u32x2 (&zk)[4][4], unsigned& wpos) {
  constexpr int BN = C::BN, MT = C::MT, NT = C::NT, PH = C::PH, TP = C::T / C::PARTS;
  using RX = Ring<C, 0>; using RY = Ring<C, 1>;
  TS_DECL; TSR(4); TS(0);
#define TR(i)
  asm volatile("" : "+v"(lane));
  const Tile<C> t;
  char* wl = p.ws + l * LSTRIDE;
  const int wm = wid / C::WN, wn = wid % C::WN, n0 = t.bn * BN + wn * (NT * 16);
  const int ml = wm * (MT * 16) + t.kh * 32, m0 = t.bm * 128 + ml;
  constexpr int OW = PH == K4 ? FH : D_;
  f32x4 acc[MT][NT];
  f32x4 wv[8];
  u64 tch[2];
  u32x4 rv[4];
  f32x4 zf[MT][4];
  float ini[MT];
  constexpr int NA = C::AM == 0 || PH == GA ? 2 : 1, NB = C::BMODE == 0 ? 2 : (C::BMODE == 1 ? 4 : 1);
  unsigned ad[NA + NB];
  const unsigned xs = C::WSIDE ? 0 : wpos, ys = C::WSIDE ? wpos : (PH == G2 ? RY::SLOT : 0);
  {
    const int r = lane & 15, j = lane >> 4;
    if (C::AM == 0 || (PH == GA && l == 0)) { ad[0] = RX::BASE + xs + (wm * MT * 16 + r) * 128 + ((j ^ (r >> 1)) << 4); ad[NA - 1] = ad[0] ^ 64; }
    else { ad[0] = RX::BASE + xs + wm * MT * 2048 + lane * 16; if (NA == 2) ad[1] = ad[0] + 1024; }
    if (C::BMODE == 0) { ad[NA] = RY::BASE + ys + (wn * NT * 16 + r) * 128 + ((j ^ (r >> 1)) << 4); ad[NA + NB - 1] = ad[NA] ^ 64; }
    else if (C::BMODE == 1) {
      const int q = (lane >> 2) & 3, pp = lane & 3, f = ((q >> 1) << 1) | (j & 1);
#pragma unroll
      for (int c = 0; c < NB; ++c) ad[NA + c] = RY::BASE + ys + wn * 8192 + (j * 8 + q) * 128 + ((c ^ f) << 5) + pp * 8;
    } else ad[NA] = RY::BASE + ys + wn * NT * 1024 + lane * 8;
#pragma unroll
    for (int u = 0; u < NA + NB; ++u) ad[u] += (unsigned)(size_t)smem;
#pragma unroll
    for (int mt = 0; mt < MT; ++mt) ini[mt] = PH == K4 ? *(const LAS float*)(smem + BS_OFF + (ml + mt * 16 + r) * 4) : 0.f;
  }
  unsigned cx = RX::D - 1 - xs / RX::SLOT, cy = RY::D - 1 - ys / RY::SLOT, dx, dy;
  wpos += C::T * Ring<C, C::WSIDE>::SLOT % W_RING; if (wpos >= W_RING) wpos -= W_RING;
#define RING_NEXT(c, d, R) asm volatile("s_sub_u32 %0, %0, 1\n\ts_cselect_b32 %1, %2, %3\n\ts_cselect_b32 %0, %4, %0" : "+s"(c), "=&s"(d) : "n"(-(int)((R::D - 1) * R::SLOT)), "s"(R::SLOT), "n"(R::D - 1) : "scc")
  static_assert(NT == 4, "");
  constexpr int BR = C::BMODE == 0 ? 1 : 2, NR = MT + NT * BR, NM = MT * NT;
  auto rd = [&](s16x8 (&fa)[MT], s16x8 (&fb)[NT], int kk, int i) {
    if (i >= BR && i < BR + MT) { const int mt = i - BR; fa[mt] = *(const LAS s16x8*)((LAS char*)(size_t)ad[NA == 2 ? kk : 0] + (NA == 2 ? 0 : kk * 1024) + mt * 2048); return; }
    const int ib = i < BR ? i : i - MT, nt = ib / BR, h = ib % BR;
    if (C::BMODE == 0) { fb[nt] = *(const LAS s16x8*)((LAS char*)(size_t)ad[NA + kk] + nt * 2048); return; }
    const s16x4 v = __builtin_amdgcn_ds_read_tr16_b64_v4i16((LAS s16x4*)((LAS char*)(size_t)ad[NA + nt % NB] + (C::BMODE == 1 ? kk * 4096 : (kk * (BN / 16) + nt) * 1024) + h * 512));
    if (h == 0) fb[nt] = __builtin_shufflevector(v, v, 0, 1, 2, 3, -1, -1, -1, -1);
    else { const s16x4 lo = __builtin_shufflevector(fb[nt], fb[nt], 0, 1, 2, 3); fb[nt] = __builtin_shufflevector(lo, v, 0, 1, 2, 3, 4, 5, 6, 7); }
  };
  constexpr int GAPS = NR <= NM - 2 ? NR : NM - 2, FREE = NM - GAPS;
  auto half = [&](const s16x8 (&ca)[MT], const s16x8 (&cb)[NT], s16x8 (&na)[MT], s16x8 (&nb)[NT], int kk, bool rdnext, bool zero = false) {
#pragma unroll
    for (int i = 0; i < NM; ++i) {
      const int nt = i / MT, mt = i % MT;
      if (MT == 4 && kk == 1 && i == 0) WAIT_L(NR - BR - MT);
      if (MT == 4 && kk == 1 && i == MT) WAIT_L(MT);
      if (zero) asm volatile("v_mfma_f32_16x16x32_bf16 %0, %1, %2, 0" : "=&v"(acc[mt][nt]) : "v"(cb[nt]), "v"(ca[mt]));
      else asm volatile("v_mfma_f32_16x16x32_bf16 %0, %1, %2, %0" : "+v"(acc[mt][nt]) : "v"(cb[nt]), "v"(ca[mt]));
      __builtin_amdgcn_sched_barrier(0);
      if (rdnext) {
#pragma unroll
        for (int u = (i * NR + GAPS - 1) / GAPS; u < ((i + 1) * NR + GAPS - 1) / GAPS; ++u) if (u < NR) rd(na, nb, kk, u);
        if (kk == 0 && i == GAPS) RING_NEXT(cx, dx, RX);
        if (kk == 0 && i == GAPS + 1) RING_NEXT(cy, dy, RY);
        if (kk == 1 && i >= GAPS) {
#pragma unroll
          for (int u = i - GAPS; u < NA + NB; u += FREE) asm volatile("v_add_u32 %0, %1, %0" : "+v"(ad[u]) : "s"(u < NA ? dx : dy));
        }
      }
      __builtin_amdgcn_sched_barrier(0);
    }
  };

  s16x8 fa0[MT], fb0[NT], fa1[MT], fb1[NT];
  RING_NEXT(cx, dx, RX); RING_NEXT(cy, dy, RY);
  auto residual = [&]() {
    int ln = lane; asm volatile("" : "+v"(ln));
    const int r = ln & 15, j = ln >> 4;
    const bool raw = PH == G2 && l == 0;
    const rsrc_t rr = mk_rsrc(raw ? p.x : wl + (PH == G2 ? O_X : O_X1), S_ * D_ * 2);
    const unsigned o = (unsigned)((m0 >> 4) + (j & 1)) * (D_ * 32) + (raw ? r * (D_ * 2) + n0 * 2 + (j >> 1) * 16 : (n0 >> 4) * 512 + (j >> 1) * 256 + r * 16);
#pragma unroll
    for (int nt = 0; nt < 4; ++nt) rv[nt] = __builtin_amdgcn_raw_buffer_load_b128(rr, o, nt * (raw ? 32 : 512), 16);
  };
  auto add_residual = [&]() {
#pragma unroll
    for (int nt = 0; nt < 4; ++nt) {
      u32x2 x0, x1; unpair_rows(rv[nt], x0, x1);
      if (PH == K5) { acc[0][nt] = unpack4(x0); acc[1][nt] = unpack4(x1); } else { acc[0][nt] += unpack4(x0); acc[1][nt] += unpack4(x1); }
    }
  };
  if (PH == K5) {
    if ((blockIdx.x >> 3) >= 28) wait_flags(smem, flags(wl, F_G2, t.bm) + (lane & 15), ~tag_of(smem));
    residual();
    add_residual();
  }
  constexpr bool ZERO = PH == GA || PH == G2;
  if (!ZERO) {
#pragma unroll
    for (int mt = 0; mt < MT; ++mt) {
#pragma unroll
      for (int nt = 0; nt < NT; ++nt) if (PH != K5) acc[mt][nt] = (f32x4){ini[mt], ini[mt], ini[mt], ini[mt]};
      asm volatile("" : "+v"(acc[mt][0]), "+v"(acc[mt][1]), "+v"(acc[mt][2]), "+v"(acc[mt][3]));
    }
  }
  if (PH == GA && l == 0) first_reads(smem, p, wid, lane, wv, tch);
  const int wcol = (wid >> 1) * FH + t.bn * 128 + (wid & 1) * 64;
  if (PH == ZZ && l == 0) {
#pragma unroll
    for (int i = 0; i < 8; ++i) ld16_async(zf[i >> 2][i & 3], p.win + (t.bm * 64 + i * 8) * (F_ * 2) + wcol * 2, (lane >> 3) * (F_ * 2) + (lane & 7) * 16);
  }
  if (PH == ZZ) BARRIER();
  BARRIER();
  TS(1); TSR(7); TR(0);
#pragma unroll
  for (int i = 0; i < NR; ++i) rd(fa0, fb0, 0, i);
  auto step = [&]() {
    BARRIER();
    __builtin_amdgcn_sched_barrier(0);
    half(fa1, fb1, fa0, fb0, 0, true);
    half(fa0, fb0, fa1, fb1, 1, true);
    WAIT_L(0);
  };
#pragma unroll 1
  for (int part = 0; part < C::PARTS; ++part) {
    if (!ZERO) {
#pragma unroll
      for (int mt = 0; mt < MT; ++mt) asm volatile("s_nop 1" : "+v"(acc[mt][0]), "+v"(acc[mt][1]), "+v"(acc[mt][2]), "+v"(acc[mt][3]));
    }
    __builtin_amdgcn_sched_barrier(0);
    half(fa0, fb0, fa1, fb1, 1, true, ZERO);
    WAIT_L(0);
    if ((PH == GA || PH == ZZ) && l + part == 0) {
      int ln = lane; asm volatile("" : "+v"(ln));
      if (PH == GA) first_sums(p, wid, ln, wv, tch);
      else column_sums((u64*)(p.ws + O_SI) + (t.bm >> 1) * F_ + wcol + (t.bm & 1), ln, zf[0], zf[1]);
    }
    TR(part * TP + 1);
    step();
    int s = 2;
#pragma unroll 1
    for (int seg = PH != ZZ ? 2 : (part ? 0 : 1 + l); seg < 3; ++seg) {
      const int e = seg == 2 ? TP : (part ? NORM_S + seg * 4 : 4);
#pragma unroll 1
      for (; s < e; s += 2) { TR(part * TP + s); step(); TR(part * TP + s + 1); step(); if (PH == GA || PH == G2) WAIT_V(0); }
      if (PH == ZZ) {
        WAIT_V(0);
        if (seg == 0) {
          int ln = lane; asm volatile("" : "+v"(ln));
          const int r = ln & 15, j = ln >> 4;
          const rsrc_t ro = mk_rsrc(wl + O_Z2, S_ * FH * 2);
          const unsigned o = (unsigned)((m0 >> 5) * (FH / 16) + (n0 >> 4)) * 1024u + tr_lane(r, j);
          TS(10);
#pragma unroll
          for (int mp = 0; mp < 2; ++mp) {
            const f32x2 f0 = *(const LAS f32x2*)(smem + FS_OFF + (ml + mp * 32 + r) * 8), f1 = *(const LAS f32x2*)(smem + FS_OFF + (ml + mp * 32 + 16 + r) * 8);
#pragma unroll
            for (int nt = 0; nt < 4; ++nt)
              st16(ro, o + mp * (FH / 16) * 1024 + nt * 1024, pair_rows(pack4(zf[(mp * 2) % MT][nt] * f0[0] + f0[1]), pack4(zf[(mp * 2 + 1) % MT][nt] * f1[0] + f1[1])));
          }
          TS(11);
        }
      }
    }
    TR(part * TP + TP);
    BARRIER();
    __builtin_amdgcn_sched_barrier(0);
    half(fa1, fb1, fa0, fb0, 0, C::PARTS > 1);
#pragma unroll
    for (int mt = 0; mt < MT; ++mt) asm volatile("s_nop %4" : "+v"(acc[mt][0]), "+v"(acc[mt][1]), "+v"(acc[mt][2]), "+v"(acc[mt][3]) : "n"(mt ? 0 : 11));
    if (part == C::PARTS - 1) { TS(2); TSR(14); } else TS(8);

    int ln = lane; asm volatile("" : "+v"(ln));
    const int r = ln & 15, j = ln >> 4;
    if (PH == GA) {
      const int hi = n0 >= D_, nn = n0 - hi * D_;
      const rsrc_t ro = mk_rsrc(wl + O_AB, 2 * S_ * D_ * 2);
      const unsigned o = (unsigned)(((hi * S_ + m0 + part * 64) >> 5) * (D_ / 16) + (nn >> 4)) * 1024u + tr_lane(r, j);
      f32x2 st0 = *(const LAS f32x2*)(smem + ST_OFF + (part * 64 + ml + r) * 16), st1 = *(const LAS f32x2*)(smem + ST_OFF + (part * 64 + ml + 16 + r) * 16);
      if (hi) { st0 = -st0; st1 = -st1; }
#pragma unroll
      for (int nt = 0; nt < NT; ++nt) {
        const f32x4 cs = *(const LAS f32x4*)(smem + CS1_OFF + (wn * 64 + nt * 16 + j * 4) * 4);
        st16(ro, o + nt * 1024, pair_rows(pack4(acc[0][nt] * st0[0] + cs * st0[1]), pack4(acc[1][nt] * st1[0] + cs * st1[1])));
      }
    } else if (PH == ZZ) {
      f32x4 k[MT], cs[4];
#pragma unroll
      for (int mt = 0; mt < MT; ++mt) k[mt] = *(const LAS f32x4*)(smem + ST_OFF + (ml + mt * 16 + r) * 16);
      if (part == 0) {
#pragma unroll
        for (int nt = 0; nt < 4; ++nt) cs[nt] = *(const LAS f32x4*)(smem + CS3_OFF + (128 + wn * 64 + nt * 16 + j * 4) * 4);
#pragma unroll
        for (int mt = 0; mt < MT; ++mt)
#pragma unroll
          for (int nt = 0; nt < 4; ++nt) acc[mt][nt] += cs[nt] * k[mt][0];
      }
      f32x2 ps = {0.f, 0.f};
#pragma unroll
      for (int mt = 0; mt < MT; ++mt) {
        f32x4 (&z)[4] = zf[mt];
#pragma unroll
        for (int nt = 0; nt < 4; ++nt) z[nt] = gelu_tanh4(acc[mt][nt], k[mt]);
        if (part == 0) {
          const f32x4 s4 = (z[0] + z[1]) + (z[2] + z[3]), q4 = z[0] * z[0] + z[1] * z[1] + z[2] * z[2] + z[3] * z[3];
          const float sm = sum_groups((s4[0] + s4[1]) + (s4[2] + s4[3])), q = sum_groups((q4[0] + q4[1]) + (q4[2] + q4[3]));
          if (j == mt) ps = (f32x2){sm, q};
        }
      }
      if (part == 0) {
        *(LAS f32x2*)(smem + PS_OFF + (wn * 128 + ml + ln) * 8) = ps;
#pragma unroll
        for (int nt = 0; nt < 4; ++nt) cs[nt] = *(const LAS f32x4*)(smem + CS3_OFF + (wn * 64 + nt * 16 + j * 4) * 4);
#pragma unroll
        for (int mt = 0; mt < MT; ++mt)
#pragma unroll
          for (int nt = 0; nt < 4; ++nt) acc[mt][nt] = cs[nt] * k[mt][0];
      }
    }
    if (part < C::PARTS - 1) TS(9);
  }

  asm volatile("" : "+v"(lane));
  const int r = lane & 15, j = lane >> 4;
  const unsigned imgl = (j & 1) * (OW / 32) * 1024u + (j >> 1) * 256 + r * 16;
  if (PH == G2 || PH == K5) {
    if (PH == G2) {
      const rsrc_t rx = mk_rsrc(wl + O_XB, 256 * 16384);
      const unsigned so = (blockIdx.x * 4 + wid) * 4096 + lane * 16, go = ((blockIdx.x ^ 1) * 4 + wid) * 4096 + lane * 16;
      const u64 tag = tag_of(smem);
#pragma unroll
      for (int i = 0; i < 4; ++i) { const u32x2 lo = pack4(acc[MT - 2][i]), hi = pack4(acc[MT - 1][i]); st16(rx, so + i * 1024, (u32x4){lo[0], lo[1], hi[0], hi[1]}); }
      residual();
      WAIT_V(4);
      Flag* fl = flags(wl, F_XC, 0);
      if (lane == 0) __hip_atomic_store(fl[blockIdx.x * 4 + wid].v, tag, RLX_AGENT);
      TS(8);
      const u64 seen = peek(fl[(blockIdx.x ^ 1) * 4 + wid].v);
      add_residual();
      wait_flags(smem, fl + (blockIdx.x ^ 1) * 4 + wid, seen);
      u32x4 pv[4];
#pragma unroll
      for (int i = 0; i < 4; ++i) pv[i] = ld16<16>(rx, go + i * 1024);
      TSR(15);
      TS(9);
#pragma unroll
      for (int i = 0; i < 4; ++i) { acc[0][i] += unpack4((u32x2){pv[i][0], pv[i][1]}); acc[1][i] += unpack4((u32x2){pv[i][2], pv[i][3]}); }
    }
    u32x2 y[2][4];
#pragma unroll
    for (int nt = 0; nt < 4; ++nt) { y[0][nt] = pack4(acc[0][nt]); y[1][nt] = pack4(acc[1][nt]); }
    if (PH == K5 && l == 1) {
      const rsrc_t ro = mk_rsrc(p.out, S_ * D_ * 2);
#pragma unroll
      for (int i = 0; i < 4; ++i)
        __builtin_amdgcn_raw_buffer_store_b128(pair_cols(y[i >> 1][(i & 1) * 2], y[i >> 1][(i & 1) * 2 + 1]), ro, ((m0 + (i >> 1) * 16 + r) * D_ + n0 + (i & 1) * 32 + j * 8) * 2, 0, 0);
      if (blockIdx.x == 0 && wid == 0 && lane == 0) __hip_atomic_store((u64*)(p.ws + O_EP), tag_of(smem), RLX_AGENT);
      TS(3);
    } else {
      char* wo = PH == G2 ? wl : wl + LSTRIDE;
      const rsrc_t ro = mk_rsrc(wo + (PH == G2 ? O_X1 : O_X), S_ * D_ * 2);
#pragma unroll
      for (int i = 0; i < 4; ++i) st16(ro, (unsigned)((m0 >> 4) * (OW / 32)) * 1024u + ((n0 >> 4) + i) * 512 + imgl, pair_rows(y[0][i], y[1][i]));
      f32x2 ps[2];
#pragma unroll
      for (int mt = 0; mt < 2; ++mt) {
        f32x4 z[4];
#pragma unroll
        for (int nt = 0; nt < 4; ++nt) z[nt] = unpack4(y[mt][nt]);
        const f32x4 s4 = (z[0] + z[1]) + (z[2] + z[3]), q4 = z[0] * z[0] + z[1] * z[1] + z[2] * z[2] + z[3] * z[3];
        ps[mt] = (f32x2){sum_groups((s4[0] + s4[1]) + (s4[2] + s4[3])), sum_groups((q4[0] + q4[1]) + (q4[2] + q4[3]))};
      }
      if (j < 2) __hip_atomic_store(stat_slot(wo + (PH == G2 ? O_ST1 : O_STX), n0 >> 6, m0 + j * 16 + r), __builtin_bit_cast(u64, j ? ps[1] : ps[0]), RLX_AGENT);
      TS(3);
      if (PH == G2) signal_done(smem, l * 8 + 2, flags(wl, F_G2, t.bm) + t.bn * 2 + t.kh, 1, lane);
      else signal_done(smem, l * 8 + 5, flags(wl, F_K5, t.bm) + t.bn, 1, lane);
    }
  } else if (PH == K4) {
    const rsrc_t ro = mk_rsrc(wl + O_H, S_ * FH * 2);
#pragma unroll
    for (int mp = 0; mp < 2; ++mp)
#pragma unroll
      for (int nt = 0; nt < NT; ++nt)
        st16(ro, (unsigned)(((m0 >> 4) + mp * 2) * (OW / 32)) * 1024u + ((n0 >> 4) + nt) * 512 + imgl,
             pair_rows(pack4(acc[(mp * 2) % MT][nt] * unpack4(zk[mp * 2][nt])), pack4(acc[(mp * 2 + 1) % MT][nt] * unpack4(zk[mp * 2 + 1][nt]))));
    TS(3);
    signal_done(smem, l * 8 + 4, flags(wl, F_K4, t.bm) + t.bn, 1, lane);
  } else {
    if (PH == ZZ) {
#pragma unroll
      for (int mt = 0; mt < MT; ++mt)
#pragma unroll
        for (int nt = 0; nt < 4; ++nt) zk[mt % 4][nt] = pack4(zf[mt][nt]);
    }
    TS(3);
  }
  TS(6); TSR(5); TS_OUT(PH);
}

template <int N, int W> __device__ __forceinline__ f32x2 row_stat(const f32x4 (&g)[8]) {
  f32x4 a = g[0];
#pragma unroll
  for (int i = 1; i < N; ++i) a += g[i];
  const float mu = (a[0] + a[2]) * (1.0f / W), v = __builtin_fmaf(-mu, mu, (a[1] + a[3]) * (1.0f / W));
  return (f32x2){mu, (v < 0.f ? 0.f : v) + LN_EPS};
}

template <class C> __device__ __forceinline__ Flag* act_flag(char* wl) {
  const Tile<C> t;
  if (C::PH == GA) return flags(wl - LSTRIDE, F_K5, t.bm);
  if (C::PH == G2) return flags(wl, F_GA, t.kh * 8 + t.bn);
  if (C::PH == ZZ) return flags(wl, F_G2, t.bm);
  if (C::PH == K4) return flags(wl, F_Z, t.bn);
  return flags(wl, F_K4, t.bm);
}
template <class C> __device__ __forceinline__ Stream<C, 1 - C::WSIDE> act_stream(const Params& p, int l, int w, int lane) {
  constexpr int PH = C::PH;
  const Tile<C> t;
  const bool raw = PH == GA && l == 0;
  return Stream<C, 1 - C::WSIDE>(raw ? p.x : p.ws + l * LSTRIDE + (PH == GA ? O_X : (PH == G2 ? O_AB : (PH == ZZ ? O_X1 : (PH == K4 ? O_Z2 : O_H)))),
                                 S_ * 2 * (PH == G2 ? 2 * D_ : (PH == K4 || PH == K5 ? FH : D_)), t, w, lane, raw);
}
template <class C>
__device__ __forceinline__ void act_phase(LAS char* smem, const Params& p, int l, int w, int lane, u64& early) {
  constexpr int PH = C::PH;
  const int wid = 6 + w;
  TS_DECL; TS(0);
  asm volatile("" : "+v"(lane));
  char* wl = p.ws + l * LSTRIDE;
  auto st = act_stream<C>(p, l, w, lane);
  const int fi = PH == K5 && (lane & 15) >= 14 ? 0 : lane & 15;
  if (PH == G2) st.slot = 16384;
  else {
    if (PH != GA || l) wait_flags(smem, act_flag<C>(wl) + fi, PH == K4 ? early : ~tag_of(smem));
    TS(1);
    if (PH == ZZ) { st.fill(smem, 0, 1, false); BARRIER(); }
    if (PH != GA) st.fill(smem, PH == ZZ, st.D, false);
  }
  if (PH == GA) st.ramp(smem); else st.start();
  TS(2);
  if (PH == GA) {
    const Tile<C> t;
    const int row = w * 64 + lane;
    u64 seen;
    st.steps(smem, 3, 4);
    st.template step<0>(smem, 4, [&] {
      Flag* f = lane == 0 ? flags(wl, F_X0, t.bm) + t.bn : flags(wl, F_WD, t.bn) + t.bm;
      if (l == 0 && w == 0 && lane < 2) __hip_atomic_store(f->v, tag_of(smem), RLX_AGENT);
    });
    const unsigned fo = ((lane & 16 ? F_WD * 16 + t.bn : F_X0 * 16 + t.bm) * 16 + (lane & 15)) * sizeof(Flag);
    f32x4 g[8], c[8];
    st.steps(smem, 5, 8);
    ld8_async(seen, p.ws + O_SF, fo);
    st.steps(smem, 8, 10);
    LANDED1(8, seen);
    wait_flags(smem, (Flag*)(p.ws + O_SF + fo), seen);
#pragma unroll
    for (int i = 0; i < 6; ++i) ld16_async(g[i], wl + O_STX + (i * S_ + t.bm * 128) * 16, row * 16);
    st.template steps<6>(smem, 10, 11);
#pragma unroll
    for (int i = 6; i < 8; ++i) ld16_async(g[i], wl + O_STX + (i * S_ + t.bm * 128) * 16, row * 16);
#pragma unroll
    for (int i = 0; i < 4; ++i) ld16_async(c[i], p.ws + O_SD + (i * 2048 + t.bn * 128) * 16, row * 16);
    st.template steps<12>(smem, 11, 14);
    LANDED8(12, g);
    {
      const f32x2 m = row_stat<8, D_>(g);
      const float rs = __builtin_amdgcn_rsqf(m[1]);
      *(LAS f32x2*)(smem + ST_OFF + row * 16) = (f32x2){rs, -m[0] * rs};
    }
    st.steps(smem, 14, 15);
    asm volatile("" : "+v"(c[0]), "+v"(c[1]), "+v"(c[2]), "+v"(c[3]));
    {
      const f32x4 a = (c[0] + c[1]) + (c[2] + c[3]);
      *(LAS float*)(smem + CS1_OFF + row * 4) = (a[0] + a[1]) + (a[2] + a[3]);
    }
    WAIT_L0();
    st.steps(smem, 15, 20);
    st.template step<0>(smem, 20, [&] { if (w == 0 && lane == 0) __hip_atomic_store(flags(wl, F_GA, t.bn)[t.bm].v, tag_of(smem), RLX_AGENT); });
    auto nx = act_stream<CG2>(p, l, w, lane);
    nx.slot = 16384;
    st.steps(smem, 21, 27);
    ld8_async(seen, act_flag<CG2>(wl), fi * sizeof(Flag));
    BARRIER();
    LANDED1(0, seen);
    wait_flags(smem, act_flag<CG2>(wl) + fi, seen);
#pragma unroll 1
    for (int k = 0; k < 3; ++k) { BARRIER(); nx.fill(smem, k, k + 1, false); if (k < 2) BARRIER(); }
  } else {
    int s1 = 1;
    if (PH == G2 && l == 0 && PFB) {
      const char* wi = p.win + (blockIdx.x * 2 + w) * 14336;
      unsigned junk = 0;
      st.template steps_pf<PFB, 128>(smem, 1, 27, junk, [&](int s) { return wi + (s - 1) * (PFB * 64); });
      st.steps(smem, 27, 30);
      asm volatile("s_waitcnt lgkmcnt(0)" : "+s"(junk));
      s1 = 30;
    }
    st.finish(smem, s1, [&] { if (PH == ZZ) early = peek(act_flag<CK4>(wl)[fi].v); });
  }
  TS(3); TS_OUT(PH);
}

template <class C> __device__ __forceinline__ Stream<C, C::WSIDE> wt_stream(const Params& p, int w, int lane) {
  constexpr int PH = C::PH;
  const Tile<C> t;
  return Stream<C, C::WSIDE>(PH == GA ? (t.bn >= 8 ? p.wdim : p.wdre) : (PH == G2 ? (t.kh ? p.wsim : p.wsre) : (PH == ZZ ? p.win : (PH == K4 ? p.wsp : p.wout))),
                             2 * (PH == GA ? D_ * D_ : (PH == ZZ ? D_ * F_ : (PH == K5 ? FH * D_ : S_ * S_))), t, w, lane);
}
template <class C, class NEXT>
__device__ __forceinline__ void wt_phase(LAS char* smem, const Params& p, int l, int w, int lane, unsigned& wpos, bool mid) {
  constexpr int PH = C::PH;
  asm volatile("" : "+v"(lane));
  const Tile<C> t;
  char* wl = p.ws + l * LSTRIDE;
  auto st = wt_stream<C>(p, w, lane);
  st.slot = wpos;
  const int row = w * 64 + lane;
  int s1 = 1;
  if (PH == GA && l == 0) {
    st.ramp(smem); s1 = 3;
    if (PFA) {
      const Tile<CG2> u;
      const char* ws = (u.kh ? p.wsim : p.wsre) + (u.bm * 128 + u.bn * 16 + w * 8) * (S_ * 2);
      unsigned junk = 0;
      st.template steps_pf<PFA, S_ * 2>(smem, 3, 23, junk, [&](int s) { const int i = (s - 3) * (PFA / 2), q = 8 + (i >> 3); return ws + (i & 7) * (S_ * 2) + (q < 16 ? 2 * q : 2 * q - 31) * 128; });
      st.steps(smem, 23, 27);
      asm volatile("s_waitcnt lgkmcnt(0)" : "+s"(junk));
      s1 = 27;
    }
  }
  else {
    if (PH == GA) st.fill(smem, 0, st.D, true);
    if (PH != ZZ) st.start();
  }
  if (PH == ZZ) {
    f32x4 g[8];
    if (l == 0) *(LAS unsigned*)(smem + BS_OFF + row * 4) = (unsigned)p.bsp[t.bm * 128 + row] << 16;
    WAIT_V(0);
    BARRIER();
#pragma unroll
    for (int i = 0; i < 8; ++i) ld16_async(g[i], wl + O_ST1 + (i * S_ + t.bm * 128) * 16, row * 16);
    BARRIER();
    st.template steps<8>(smem, 1, 2);
    LANDED8(8, g);
    {
      const f32x2 m = row_stat<8, D_>(g);
      const float rs = __builtin_amdgcn_rsqf(m[1]);
      *(LAS f32x4*)(smem + ST_OFF + row * 16) = (f32x4){-m[0], GC1 * rs, GC2 * rs * rs * rs, m[1] * rs};
      WAIT_L0();
    }
    {
      int s2 = 2;
      if (l == 0) {
        Flag* fl = flags(wl, F_WI, t.bn);
        u64 seen;
        st.steps(smem, 2, 4);
        st.template step<0>(smem, 4, [&] { if (w == 0 && lane == 0) __hip_atomic_store(fl[t.bm].v, tag_of(smem), RLX_AGENT); });
        st.steps(smem, 5, 8);
        ld8_async(seen, fl, (lane & 15) * sizeof(Flag));
        st.steps(smem, 8, 10);
        LANDED1(16, seen);
        wait_flags(smem, fl + (lane & 15), seen);
#pragma unroll
        for (int i = 0; i < 8; ++i) ld16_async(g[i], p.ws + O_SI + (i * F_ + w * FH + t.bn * 128) * 8, lane * 16);
        st.template steps<8>(smem, 10, 13);
        LANDED8(24, g);
        const f32x4 a = ((g[0] + g[1]) + (g[2] + g[3])) + ((g[4] + g[5]) + (g[6] + g[7]));
        *(LAS f32x2*)(smem + CS3_OFF + row * 8) = (f32x2){a[0] + a[2], a[1] + a[3]};
        WAIT_L0();
        s2 = 13;
      }
      st.steps(smem, s2, 17);
      st.template step<0>(smem, 17, [&] {
        const u32x2 q = __builtin_bit_cast(u32x2, *(const LAS f32x2*)(smem + PS_OFF + row * 8) + *(const LAS f32x2*)(smem + PS_OFF + (128 + row) * 8));
        const u32x2 v = {q[0] < 0xfffffffeu ? q[0] : 0xfffffffeu, q[1] < 0xfffffffeu ? q[1] : 0xfffffffeu};
        __hip_atomic_store(stat_slot(wl + O_XCH, t.bn, t.bm * 128 + row), __builtin_bit_cast(u64, v), RLX_AGENT);
      });
      st.steps(smem, 18, 13 + NORM_S);
#pragma unroll
      for (int i = 0; i < 7; ++i) ld16_async(g[i], wl + O_XCH + (i * S_ + t.bm * 128) * 16, row * 16);
      st.template steps<7>(smem, 13 + NORM_S, 15 + NORM_S);
      LANDED8(16, g);
#pragma unroll 1
      for (unsigned spin = 0; spin < SPIN_MAX; ++spin) {
        unsigned mx = 0;
#pragma unroll
        for (int i = 0; i < 7; ++i) { const u32x4 u = __builtin_bit_cast(u32x4, g[i]); mx = max(max(mx, max(u[0], u[1])), max(u[2], u[3])); }
        if (__all(mx != 0xffffffffu)) break;
#pragma unroll
        for (int i = 0; i < 7; ++i) ld16_async(g[i], wl + O_XCH + (i * S_ + t.bm * 128) * 16, row * 16);
        LANDED8(0, g);
      }
      const f32x2 m = row_stat<7, FH>(g);
      const float rs = __builtin_amdgcn_rsqf(m[1]);
      *(LAS f32x2*)(smem + FS_OFF + row * 8) = (f32x2){rs, -m[0] * rs};
      WAIT_L0();
      s1 = 15 + NORM_S;
    }
    static_assert(NORM_S + 20 > C::T - 6, "");
  } else if (PH == G2) {
    Flag* fl = flags(wl, F_GB, t.kh * 8 + t.bn);
    const int fi = lane & 15;
    u64 seen;
    st.steps(smem, 1, 4);
    st.template step<0>(smem, 4, [&] {
      const Tile<CGA> ta;
      if (w == 0 && lane == 0) __hip_atomic_store(flags(wl, F_GB, ta.bn)[ta.bm].v, tag_of(smem), RLX_AGENT);
    });
    st.steps(smem, 5, 8);
    ld8_async(seen, fl, fi * sizeof(Flag));
    st.steps(smem, 8, 10);
    LANDED1(16, seen);
    wait_flags(smem, fl + fi, seen);
    s1 = 10;
  }
  if (PH == K5) st.finish(smem, s1, [] {});
  else {
    st.steps(smem, s1, C::T - st.D + 1);
    auto sig = [&](int s) { if (PH == ZZ && s == NORM_S + 20 && w == 0 && lane == 0) __hip_atomic_store(flags(wl, F_Z, t.bn)[t.bm].v, tag_of(smem), RLX_AGENT); };
    if (PH != G2 || mid) { auto nx = wt_stream<NEXT>(p, w, lane); st.hand_over(smem, nx, sig); }
    else { auto nx = wt_stream<CK5>(p, w, lane); st.hand_over(smem, nx, sig); }
  }
  wpos = st.slot;
}

__device__ __forceinline__ void touch(LAS char* smem, const Params& p, int wid, int lane) {
  const int u = ((blockIdx.x >> 3) - 28 + (blockIdx.x & 7) * 4) * 8 + wid;
  BARRIER();
#pragma unroll 1
  for (int i = 0; i < 46; i += 2) {
    const char* g = (i < 32 ? p.wsp + (size_t)(u * 32 + i) * 1024 : p.wout + (size_t)(u * 14 + i - 32) * 1024) + lane * 16;
    __builtin_amdgcn_global_load_lds((const unsigned*)g, (LAS unsigned*)(smem + A_RING + wid * 1024), 16, 0, 0);
    __builtin_amdgcn_global_load_lds((const unsigned*)(g + 1024), (LAS unsigned*)(smem + A_RING + wid * 1024), 16, 0, 0);
    WAIT_V(8);
  }
  __builtin_amdgcn_s_waitcnt(0x0f70);
  BARRIER();
}

__global__ __launch_bounds__(512, 2) void mega_k(Params p) {
  extern __shared__ __attribute__((aligned(16))) char smem_[];
  LAS char* smem = (LAS char*)smem_;
  const int lane = threadIdx.x & 63, wid = __builtin_amdgcn_readfirstlane(threadIdx.x >> 6);
  const bool mid = (blockIdx.x >> 3) < 28;
  unsigned wpos = 0;
  if (wid < 4) {
    __builtin_amdgcn_s_waitcnt(0x0f70);
#pragma unroll 1
    for (int l = 0; l < 2; ++l) {
      u32x2 zk[4][4];
      compute_phase<CGA>(smem, p, l, wid, lane, zk, wpos);
      compute_phase<CG2>(smem, p, l, wid, lane, zk, wpos);
      if (mid) { compute_phase<CZZ>(smem, p, l, wid, lane, zk, wpos); compute_phase<CK4>(smem, p, l, wid, lane, zk, wpos); }
      else if (l == 0) touch(smem, p, wid, lane);
      compute_phase<CK5>(smem, p, l, wid, lane, zk, wpos);
    }
  } else if (wid < 6) {
#pragma unroll 1
    for (int l = 0; l < 2; ++l) {
      wt_phase<CGA, CG2>(smem, p, l, wid - 4, lane, wpos, mid);
      wt_phase<CG2, CZZ>(smem, p, l, wid - 4, lane, wpos, mid);
      if (mid) { wt_phase<CZZ, CK4>(smem, p, l, wid - 4, lane, wpos, mid); wt_phase<CK4, CK5>(smem, p, l, wid - 4, lane, wpos, mid); }
      else if (l == 0) touch(smem, p, wid, lane);
      wt_phase<CK5, CK5>(smem, p, l, wid - 4, lane, wpos, mid);
    }
  } else {
#pragma unroll 1
    for (int l = 0; l < 2; ++l) {
      u64 early = 0;
      act_phase<CGA>(smem, p, l, wid - 6, lane, early);
      act_phase<CG2>(smem, p, l, wid - 6, lane, early);
      if (mid) { act_phase<CZZ>(smem, p, l, wid - 6, lane, early); act_phase<CK4>(smem, p, l, wid - 6, lane, early); }
      else if (l == 0) touch(smem, p, wid, lane);
      act_phase<CK5>(smem, p, l, wid - 6, lane, early);
    }
  }
}

extern "C" void kernel_launch(void* const* d_in, const int* in_sizes, int n_in, void* d_out, int out_size, void* d_ws, size_t ws_size, hipStream_t stream) {
  Params p;
  p.x = (const char*)d_in[0];
  p.wsre = (const char*)d_in[1]; p.wsim = (const char*)d_in[2]; p.wdre = (const char*)d_in[3]; p.wdim = (const char*)d_in[4];
  p.win = (const char*)d_in[6]; p.wsp = (const char*)d_in[7]; p.wout = (const char*)d_in[9]; p.bsp = (const u16*)d_in[8];
  p.out = (char*)d_out; p.ws = (char*)d_ws;
  (void)hipFuncSetAttribute((const void*)mega_k, hipFuncAttributeMaxDynamicSharedMemorySize, LDS_BYTES);
  hipLaunchKernelGGL(mega_k, dim3(256), dim3(512), LDS_BYTES, stream, p);
}
```

```cpp
#include <hip/hip_runtime.h>
#include <hip/hip_bf16.h>
#include <stdint.h>

#define LAS __attribute__((address_space(3)))
typedef unsigned short u16;
typedef short s16x4 __attribute__((ext_vector_type(4)));
typedef short s16x8 __attribute__((ext_vector_type(8)));
typedef __bf16 bf16x8 __attribute__((ext_vector_type(8)));
typedef __bf16 bf16x2 __attribute__((ext_vector_type(2)));
typedef float f32x2 __attribute__((ext_vector_type(2)));
typedef float f32x4 __attribute__((ext_vector_type(4)));
typedef unsigned u32x2 __attribute__((ext_vector_type(2)));
typedef unsigned u32x4 __attribute__((ext_vector_type(4)));

constexpr int S_ = 2048, D_ = 1024, F_ = 3584, FH = 1792;
constexpr float LN_EPS = 1e-5f;

__device__ __forceinline__ unsigned pack_bf16(float lo, float hi) {
  f32x2 v = {lo, hi};
  return __builtin_bit_cast(unsigned, __builtin_convertvector(v, bf16x2));
}
__device__ __forceinline__ float bf_lo(unsigned u) { return __builtin_bit_cast(float, u << 16); }
__device__ __forceinline__ float bf_hi(unsigned u) { return __builtin_bit_cast(float, u & 0xffff0000u); }
__device__ __forceinline__ void swap16(unsigned& a, unsigned& b) { asm volatile("s_nop 1\n\tv_permlane16_swap_b32 %0, %1" : "+v"(a), "+v"(b)); }
__device__ __forceinline__ void swap32(unsigned& a, unsigned& b) { asm volatile("s_nop 1\n\tv_permlane32_swap_b32 %0, %1" : "+v"(a), "+v"(b)); }
__device__ __forceinline__ float sum32(float v) {
  unsigned a = __builtin_bit_cast(unsigned, v), b = a; swap32(a, b);
  return __builtin_bit_cast(float, a) + __builtin_bit_cast(float, b);
}
__device__ __forceinline__ float sum_groups(float v) {
  unsigned a = __builtin_bit_cast(unsigned, v), b = a; swap16(a, b);
  return sum32(__builtin_bit_cast(float, a) + __builtin_bit_cast(float, b));
}
__device__ __forceinline__ u32x4 pair_rows(u32x2 a, u32x2 b) {
  unsigned a0 = a[0], a1 = a[1], b0 = b[0], b1 = b[1]; swap16(a0, b0); swap16(a1, b1);
  return (u32x4){a0, a1, b0, b1};
}
__device__ __forceinline__ void unpair_rows(u32x4 v, u32x2& a, u32x2& b) {
  unsigned a0 = v[0], a1 = v[1], b0 = v[2], b1 = v[3]; swap16(a0, b0); swap16(a1, b1);
  a = (u32x2){a0, a1}; b = (u32x2){b0, b1};
}
__device__ __forceinline__ u32x4 pair_cols(u32x2 a, u32x2 b) {
  unsigned a0 = a[0], a1 = a[1], b0 = b[0], b1 = b[1]; swap32(a0, b0); swap32(a1, b1); swap16(a0, b0); swap16(a1, b1);
  return (u32x4){a0, a1, b0, b1};
}
__device__ __forceinline__ void unpair_cols(u32x4 v, u32x2& a, u32x2& b) {
  unsigned a0 = v[0], a1 = v[1], b0 = v[2], b1 = v[3]; swap16(a0, b0); swap16(a1, b1); swap32(a0, b0); swap32(a1, b1);
  a = (u32x2){a0, a1}; b = (u32x2){b0, b1};
}
__device__ __forceinline__ u32x2 pack4(f32x4 v) { return (u32x2){pack_bf16(v[0], v[1]), pack_bf16(v[2], v[3])}; }
__device__ __forceinline__ f32x4 unpack4(u32x2 y) { return (f32x4){bf_lo(y[0]), bf_hi(y[0]), bf_lo(y[1]), bf_hi(y[1])}; }
constexpr float GC1 = -2.0f * 0.7978845608028654f * 1.4426950408889634f, GC2 = GC1 * 0.044715f;
__device__ __forceinline__ f32x4 gelu_tanh4(f32x4 a, f32x4 k) {
  const f32x4 t = a * (a * a * k[2] + k[1]);
  f32x4 e;
#pragma unroll
  for (int i = 0; i < 4; ++i) e[i] = __builtin_amdgcn_exp2f(t[i]);
  e = e * k[3] + k[3];
#pragma unroll
  for (int i = 0; i < 4; ++i) e[i] = __builtin_amdgcn_rcpf(e[i]);
  return a * e;
}
#define STAUX 16
typedef __amdgpu_buffer_rsrc_t rsrc_t;
__device__ __forceinline__ rsrc_t mk_rsrc(const void* p, unsigned bytes) { return __builtin_amdgcn_make_buffer_rsrc((void*)p, 0, bytes, 0x00020000); }
__device__ __forceinline__ void st16(rsrc_t r, unsigned off, u32x4 v) { __builtin_amdgcn_raw_buffer_store_b128(v, r, off, 0, STAUX); }
template <int AUX> __device__ __forceinline__ u32x4 ld16(rsrc_t r, unsigned off) { return __builtin_amdgcn_raw_buffer_load_b128(r, off, 0, AUX); }
#define WAIT_V(n) asm volatile("s_waitcnt vmcnt(%0)" ::"n"(n) : "memory")
#define WAIT_L(n) __builtin_amdgcn_s_waitcnt(0xc07f | ((n) << 8))
#define WAIT_L0() asm volatile("s_waitcnt lgkmcnt(0)" ::: "memory")
#define BARRIER() do { asm volatile("" ::: "memory"); __builtin_amdgcn_s_barrier(); asm volatile("" ::: "memory"); } while (0)
#define PIN_S(x) asm volatile("" : "+s"(x))

#define RLX_AGENT __ATOMIC_RELAXED, __HIP_MEMORY_SCOPE_AGENT
#define PF_THR 8
#define POLL_SLEEP 1
#define NORM_S 8
#define SIGA_S 4
constexpr unsigned SPIN_MAX = 1u << 18;
typedef unsigned long long u64;
template <int CTRL> __device__ __forceinline__ float dpp_add(float v) {
  return v + __builtin_bit_cast(float, __builtin_amdgcn_update_dpp(0, __builtin_bit_cast(int, v), CTRL, 0xf, 0xf, true));
}
__device__ __forceinline__ float sum8(float v) { return dpp_add<0x141>(dpp_add<0x4e>(dpp_add<0xb1>(v))); }

constexpr size_t MB = 1 << 20, LSTRIDE = 40 * MB;
constexpr size_t O_X = 0, O_X1 = 4 * MB, O_AB = 8 * MB, O_Z2 = 16 * MB, O_H = 24 * MB, O_XB = 32 * MB;
constexpr size_t O_STX = 36 * MB, O_ST1 = O_STX + 256 * 1024;
constexpr size_t O_XCH = O_STX + 512 * 1024;
constexpr size_t O_SF = O_STX + 768 * 1024;
constexpr size_t O_SD = 2 * LSTRIDE, O_SI = O_SD + 128 * 1024;
constexpr size_t O_EP = O_SD + 512 * 1024;
enum { F_X0, F_WD, F_WI, F_GA, F_GB, F_G2, F_Z, F_K4, F_K5, F_XC };
constexpr unsigned W_RING = 96 * 1024, A_RING = W_RING, A_BYTES = 48 * 1024, CS1_OFF = 144 * 1024, CS3_OFF = CS1_OFF + 512, ST_OFF = CS3_OFF + 1024, PS_OFF = ST_OFF + 2048,
                   FS_OFF = PS_OFF + 2048, CNT_OFF = FS_OFF + 1024, EP_OFF = CNT_OFF + 128;
constexpr unsigned LDS_BYTES = EP_OFF + 256;

enum { GA = 0, G2 = 1, ZZ = 2, K4 = 3, K5 = 4 };

struct Params {
  const char *x, *wsre, *wsim, *wdre, *wdim, *win, *wsp, *wout;
  const u16* bsp;
  char* out;
  char* ws;
};

template <int PH_, int BM_, int BN_, int WM_, int WN_, int AM_, int BMODE_, int T_, int PARTS_, int GN_, int XN_, int PA_, int PB_, int WSIDE_>
struct Cfg {
  static constexpr int PH = PH_, BM = BM_, BN = BN_, WM = WM_, WN = WN_, AM = AM_, BMODE = BMODE_, T = T_, PARTS = PARTS_;
  static constexpr int GN = GN_, XM = 4, XN = XN_, PA = PA_, PB = PB_, WSIDE = WSIDE_;
  static constexpr int MT = BM / WM / 16, NT = BN / WN / 16;
};
using CGA = Cfg<GA, 64,  128, 2, 2, 1, 0, 32, 2, 16, 8, D_ / 32, 2048,    1>;
using CG2 = Cfg<G2, 128, 128, 2, 2, 0, 2, 32, 1, 16, 8, 4096,    D_ / 16, 0>;
using CZZ = Cfg<ZZ, 128, 128, 2, 2, 1, 1, 32, 2, 14, 7, D_ / 32, F_ * 2,  1>;
using CK4 = Cfg<K4, 128, 128, 2, 2, 0, 2, 32, 1, 14, 7, 4096,    FH / 16, 0>;
using CK5 = Cfg<K5, 128, 64,  4, 1, 1, 1, 28, 1, 16, 8, FH / 32, D_ * 2,  1>;

template <class C> struct Tile {
  int bm, bn, kh, cls;
  __device__ __forceinline__ Tile() {
    int bid = blockIdx.x; PIN_S(bid);
    const int xcd = bid & 7, idx = bid >> 3, lm = idx / C::XN, ln = idx % C::XN;
    bm = (xcd >> 1) * C::XM + lm; bn = C::PH == G2 ? ln : (xcd & 1) * C::XN + ln; kh = C::PH == G2 ? xcd & 1 : 0; cls = (lm + ln) & 3;
  }
};
template <class C, int SIDE> struct Ring {
  static constexpr bool ISW = C::WSIDE == SIDE;
  static constexpr unsigned SLOT = (SIDE ? C::BN : C::BM) * 128, BASE = ISW ? 0 : A_RING, D = (ISW ? W_RING : A_BYTES) / SLOT;
};

#define FW 32
struct Flag { u64 v[FW]; };
__device__ __forceinline__ Flag* flags(char* wl, int id, int panel) { return (Flag*)(wl + O_SF) + (id * 16 + panel) * 16; }
__device__ __forceinline__ u64 tag_of(LAS char* smem) { return *(const volatile LAS u64*)(smem + EP_OFF); }
__device__ __forceinline__ void signal_done(LAS char* smem, int slot, Flag* flag, int nf, int lane) {
  const u64 tag = tag_of(smem);
  WAIT_V(0);
  unsigned old = 0;
  if (lane == 0) old = __hip_atomic_fetch_add((LAS unsigned*)(smem + CNT_OFF) + slot, 1u, __ATOMIC_RELAXED, __HIP_MEMORY_SCOPE_WORKGROUP);
  if (__builtin_amdgcn_readfirstlane(old) == 3 && lane < nf) __hip_atomic_store(flag->v, tag, RLX_AGENT);
}
__device__ __forceinline__ u64 peek(u64* f) { return __hip_atomic_load(f, RLX_AGENT); }
__device__ __forceinline__ void ld8_async(u64& d, const void* sbase, unsigned voff) { asm volatile("s_nop 4\n\tglobal_load_dwordx2 %0, %1, %2 sc1" : "=v"(d) : "v"(voff), "s"(sbase) : "memory"); }
#define LCNT(n) (n)
#define LANDED1(n, a) asm volatile("s_waitcnt vmcnt(%1)" : "+v"(a) : "n"(LCNT(n)) : "memory")
__device__ __forceinline__ void ld16_async(f32x4& d, const void* sbase, unsigned voff) { asm volatile("s_nop 4\n\tglobal_load_dwordx4 %0, %1, %2 sc1" : "=v"(d) : "v"(voff), "s"(sbase) : "memory"); }
#define LANDED8(n, g) asm volatile("s_waitcnt vmcnt(%8)" : "+v"(g[0]), "+v"(g[1]), "+v"(g[2]), "+v"(g[3]), "+v"(g[4]), "+v"(g[5]), "+v"(g[6]), "+v"(g[7]) : "n"(LCNT(n)) : "memory")
__device__ __forceinline__ u64* stat_slot(char* base, int g, int row) { return (u64*)base + ((g >> 1) * S_ + row) * 2 + (g & 1); }
__device__ __forceinline__ void wait_flags(LAS char* smem, Flag* f, u64 first) {
  const u64 tag = tag_of(smem);
  if (!__all(first == tag)) {
#pragma unroll 1
    for (unsigned spin = 0; spin < SPIN_MAX; ++spin) {
      if (__all(peek(f->v) == tag)) break;
      if (POLL_SLEEP) __builtin_amdgcn_s_sleep(POLL_SLEEP);
    }
  }
  asm volatile("" ::: "memory");
}

template <class C, int SIDE> struct Stream {
  using R = Ring<C, SIDE>;
  static constexpr int NP = R::SLOT / 2048, T = C::T, D = R::D, MODE = SIDE ? C::BMODE : (C::AM ? 3 : 0), PITCH = SIDE ? C::PB : C::PA;
  static constexpr unsigned KS = MODE == 0 ? 128 : (MODE == 1 ? 64 * PITCH : (MODE == 2 ? 2 * PITCH * 1024 : 2048));
  static_assert(T % 4 == 0 && D * NP <= 63, "");
  rsrc_t rs; unsigned base, voff, ks, slot, swp; int cls, w;
  __device__ __forceinline__ Stream(const char* src, unsigned bytes, const Tile<C>& t, int w_, int lane, bool raw = false) {
    rs = mk_rsrc(src, bytes); w = w_; cls = t.cls; ks = KS; slot = 0; swp = t.kh * 32 * PITCH;
    const int b = SIDE ? t.bn : t.bm;
    const unsigned sw = ((lane & 7) ^ ((w << 2) | (lane >> 4))) << 4;
    if (MODE == 0) { base = ((C::PH == GA ? b & 7 : b) * 128 + w * 8) * PITCH; voff = (lane >> 3) * PITCH + sw; }
    else if (MODE == 1) { base = w * 8 * PITCH + b * C::BN * 2; voff = (lane >> 3) * PITCH + (((lane & 7) ^ ((((lane >> 4) & 1) << 2) | (w << 1))) << 4); }
    else if (MODE == 2) { base = (b * (C::BN / 16) + w) * 1024 + t.kh * T * KS; voff = lane * 16; }
    else if (raw) { base = (b * 128 + w * 8) * (PITCH * 64); voff = (lane >> 3) * (PITCH * 64) + sw; ks = 128; }
    else { base = (b * 8 * PITCH + w) * 1024; voff = lane * 16; }
  }
  __device__ __forceinline__ unsigned poff(int i) const {
    if (MODE == 0) return C::PH == G2 ? (i * 16 * PITCH) ^ swp : i * 16 * PITCH;
    if (MODE == 1) return (i & 3) * 16 * PITCH + (i >> 2) * 128;
    if (MODE == 2) return ((i >> 2) * PITCH + (i & 3) * 2) * 1024;
    return i * PITCH * 1024;
  }
  __device__ __forceinline__ void issue(LAS char* smem, int s, unsigned at, bool thr = false) const {
    constexpr int L = T / C::PARTS;
    const int hi = L == 16 ? s >> 4 : 0;
    int ch = s - hi * L;
#define LEADMASK 23
    if ((LEADMASK >> C::PH & 1) && (ch & 3) == cls) { ch += 4; if (ch >= L) ch -= L; }
    if (C::PH == G2) ch = ch < 16 ? 2 * ch : 2 * ch - 31;
    unsigned g = base + ch * ks;
    if (C::PH == GA && SIDE == 0) g += hi * (64 * D_ * 2);
    if (C::PH == ZZ && SIDE == 1) g += (1 - hi) * (FH * 2);
#pragma unroll
    for (int i = 0; i < NP; ++i) {
      __builtin_amdgcn_raw_ptr_buffer_load_lds(rs, (LAS void*)(smem + R::BASE + at + (w + 2 * i) * 1024), 16, voff, g + poff(i), 0, R::ISW ? 0 : 16);
      if (PF_THR && thr && (i & 1)) WAIT_V(PF_THR);
    }
  }
  __device__ __forceinline__ void adv() { slot += R::SLOT; if (slot == D * R::SLOT) slot = 0; }
  __device__ __forceinline__ void fill(LAS char* smem, int s0, int s1, bool thr) {
#pragma unroll 1
    for (int s = s0; s < s1; ++s) { issue(smem, s, slot, thr); adv(); }
  }
  __device__ __forceinline__ void start() const {
    WAIT_V((D - 1) * NP);
    BARRIER();
    WAIT_V((D - 2) * NP);
  }
  __device__ __forceinline__ void ramp(LAS char* smem) {
    static_assert(C::PH != GA || D == 6, "");
    fill(smem, 0, 2, false); WAIT_V(NP); BARRIER();
    fill(smem, 2, 4, false); WAIT_V(2 * NP); BARRIER();
    fill(smem, 4, 6, false); WAIT_V(3 * NP); BARRIER();
    fill(smem, 6, 8, false); WAIT_V(4 * NP);
  }
  template <int X, class F> __device__ __forceinline__ void step(LAS char* smem, int s, F pre) {
    BARRIER();
    pre();
    issue(smem, s + D - 1, slot);
    adv();
    WAIT_V((D - 2) * NP + X > 63 ? 63 : (D - 2) * NP + X);
  }
  template <int X = 0> __device__ __forceinline__ void steps(LAS char* smem, int s0, int s1) {
#pragma unroll 1
    for (int s = s0; s < s1; ++s) step<X>(smem, s, [] {});
  }
  template <class F> __device__ __forceinline__ void finish(LAS char* smem, int s0, F last) {
    steps(smem, s0, T - D + 1);
    BARRIER();
    WAIT_V((D - 3) * NP);
#pragma unroll 1
    for (int s = T - D + 2; s < T - 1; ++s) { BARRIER(); WAIT_V(0); }
    BARRIER();
    last();
    BARRIER();
  }
  template <class NX, class F> __device__ __forceinline__ void hand_over(LAS char* smem, NX& nx, F pre) {
    constexpr int Q = NP / NX::NP;
    nx.slot = slot;
#pragma unroll 1
    for (int k = 0; k < D; ++k) { BARRIER(); pre(T - D + 1 + k); nx.fill(smem, k * Q, k * Q + Q, false); WAIT_V((D - 2) * NP); }
  }
};

#define TS(i)
#define TSR(i)
#define TS_DECL
#define TS_OUT(ph)

__device__ __forceinline__ unsigned tr_lane(int r, int j) { return ((r >> 2) & 1) * 512 + (j & 1) * 256 + (r >> 3) * 128 + (r & 3) * 32 + (j >> 1) * 16; }
__device__ __forceinline__ void unpack8(u32x4 v, float (&e)[8]) {
#pragma unroll
  for (int i = 0; i < 4; ++i) { e[2 * i] = bf_lo(v[i]); e[2 * i + 1] = bf_hi(v[i]); }
}

__device__ __forceinline__ void first_job(LAS char* smem, const Params& p, int wid, int lane) {
  const Tile<CGA> t;
  const unsigned ro = (unsigned)(wid * 32 + (lane >> 3)) * (D_ * 2) + (lane & 7) * 16;
  const char* gx = p.x + (size_t)t.bm * 128 * (D_ * 2) + t.bn * 128 + ro;
  const char* gw = (t.bn >= 8 ? p.wdim : p.wdre) + (size_t)(t.bn & 7) * 128 * (D_ * 2) + t.bm * 128 + ro;
  u32x4 vx[4], vw[4];
#pragma unroll
  for (int i = 0; i < 4; ++i) vx[i] = *(const u32x4*)(gx + i * 8 * (D_ * 2));
#pragma unroll
  for (int i = 0; i < 4; ++i) vw[i] = *(const u32x4*)(gw + i * 8 * (D_ * 2));
  unsigned tch;
  { const Tile<CG2> u; tch = *(const unsigned*)((u.kh ? p.wsim : p.wsre) + (size_t)(u.bm * 128 + u.bn * 16 + (lane >> 2)) * (S_ * 2) + ((wid >> 1) * 4 + (lane & 3)) * 256 + (wid & 1) * 64); }
  const u64 e0 = peek((u64*)(p.ws + O_EP));
  f32x2 st = {0.f, 0.f}; float sw = 0.f;
#pragma unroll
  for (int i = 0; i < 4; ++i) {
    float e[8]; unpack8(vx[i], e);
    const float mu = sum8(((e[0] + e[1]) + (e[2] + e[3])) + ((e[4] + e[5]) + (e[6] + e[7])));
    float q = 0.f;
#pragma unroll
    for (int k = 0; k < 8; ++k) q = __builtin_fmaf(e[k], e[k], q);
    q = sum8(q);
    unpack8(vw[i], e);
    const float s = sum8(((e[0] + e[1]) + (e[2] + e[3])) + ((e[4] + e[5]) + (e[6] + e[7])));
    if ((lane & 7) == i) { st = (f32x2){mu, q}; sw = s; }
  }
  asm volatile("" :: "v"(tch));
  u64 e = (e0 ^ (e0 >> 29)) * 0x9E3779B97F4A7C15ull + 0x632BE59BD9B4E019ull;
  if (e == e0) e = ~e0;
  if (wid == 2 && lane < 32) *(LAS unsigned*)(smem + CNT_OFF + lane * 4) = 0u;
  if (wid == 3 && lane == 0) *(LAS u64*)(smem + EP_OFF) = e;
  if ((lane & 7) < 4) {
    const int row = wid * 32 + (lane & 7) * 8 + (lane >> 3);
    __hip_atomic_store(stat_slot(p.ws + O_STX, t.bn, t.bm * 128 + row), __builtin_bit_cast(u64, st), RLX_AGENT);
    __hip_atomic_store((unsigned*)(p.ws + O_SD) + ((t.bm >> 2) * 2048 + t.bn * 128 + row) * 4 + (t.bm & 3), __builtin_bit_cast(unsigned, sw), RLX_AGENT);
  }
  if (wid < 2) st16(mk_rsrc(p.ws + wid * LSTRIDE + O_XCH, 256 * 1024), blockIdx.x * 1024 + lane * 16, (u32x4){~0u, ~0u, ~0u, ~0u});
  WAIT_L0();
}

template <class C>
__device__ __forceinline__ void compute_phase(LAS char* smem, const Params& p, int l, int wid, int lane, u32x2 (&zk)[4][4], unsigned& wpos) {
  constexpr int BN = C::BN, MT = C::MT, NT = C::NT, PH = C::PH, TP = C::T / C::PARTS;
  using RX = Ring<C, 0>; using RY = Ring<C, 1>;
  TS_DECL; TSR(4); TS(0);
#define TR(i)
  asm volatile("" : "+v"(lane));
  const Tile<C> t;
  char* wl = p.ws + l * LSTRIDE;
  const int wm = wid / C::WN, wn = wid % C::WN, n0 = t.bn * BN + wn * (NT * 16);
  const int ml = wm * (MT * 16) + t.kh * 32, m0 = t.bm * 128 + ml;
  constexpr int OW = PH == K4 ? FH : D_;
  f32x4 acc[MT][NT];
  f32x4 wv[8];
  u32x4 rv[4];
  f32x4 zf[MT][4];
  float ini[MT];
  constexpr int NA = C::AM == 0 || PH == GA ? 2 : 1, NB = C::BMODE == 0 ? 2 : (C::BMODE == 1 ? 4 : 1);
  unsigned ad[NA + NB];
  const unsigned xs = C::WSIDE ? 0 : wpos, ys = C::WSIDE ? wpos : (PH == G2 ? RY::SLOT : 0);
  {
    const int r = lane & 15, j = lane >> 4;
    if (C::AM == 0 || (PH == GA && l == 0)) { ad[0] = RX::BASE + xs + (wm * MT * 16 + r) * 128 + ((j ^ (r >> 1)) << 4); ad[NA - 1] = ad[0] ^ 64; }
    else { ad[0] = RX::BASE + xs + wm * MT * 2048 + lane * 16; if (NA == 2) ad[1] = ad[0] + 1024; }
    if (C::BMODE == 0) { ad[NA] = RY::BASE + ys + (wn * NT * 16 + r) * 128 + ((j ^ (r >> 1)) << 4); ad[NA + NB - 1] = ad[NA] ^ 64; }
    else if (C::BMODE == 1) {
      const int q = (lane >> 2) & 3, pp = lane & 3, f = ((q >> 1) << 1) | (j & 1);
#pragma unroll
      for (int c = 0; c < NB; ++c) ad[NA + c] = RY::BASE + ys + wn * 8192 + (j * 8 + q) * 128 + ((c ^ f) << 5) + pp * 8;
    } else ad[NA] = RY::BASE + ys + wn * NT * 1024 + lane * 8;
#pragma unroll
    for (int u = 0; u < NA + NB; ++u) ad[u] += (unsigned)(size_t)smem;
#pragma unroll
    for (int mt = 0; mt < MT; ++mt) ini[mt] = PH == K4 ? __builtin_bit_cast(float, (unsigned)p.bsp[m0 + mt * 16 + r] << 16) : 0.f;
  }
  unsigned cx = RX::D - 1 - xs / RX::SLOT, cy = RY::D - 1 - ys / RY::SLOT, dx, dy;
  wpos += C::T * Ring<C, C::WSIDE>::SLOT % W_RING; if (wpos >= W_RING) wpos -= W_RING;
#define RING_NEXT(c, d, R) asm volatile("s_sub_u32 %0, %0, 1\n\ts_cselect_b32 %1, %2, %3\n\ts_cselect_b32 %0, %4, %0" : "+s"(c), "=&s"(d) : "n"(-(int)((R::D - 1) * R::SLOT)), "s"(R::SLOT), "n"(R::D - 1) : "scc")
  static_assert(NT == 4, "");
  constexpr int BR = C::BMODE == 0 ? 1 : 2, NR = MT + NT * BR, NM = MT * NT;
  auto rd = [&](s16x8 (&fa)[MT], s16x8 (&fb)[NT], int kk, int i) {
    if (i >= BR && i < BR + MT) { const int mt = i - BR; fa[mt] = *(const LAS s16x8*)((LAS char*)(size_t)ad[NA == 2 ? kk : 0] + (NA == 2 ? 0 : kk * 1024) + mt * 2048); return; }
    const int ib = i < BR ? i : i - MT, nt = ib / BR, h = ib % BR;
    if (C::BMODE == 0) { fb[nt] = *(const LAS s16x8*)((LAS char*)(size_t)ad[NA + kk] + nt * 2048); return; }
    const s16x4 v = __builtin_amdgcn_ds_read_tr16_b64_v4i16((LAS s16x4*)((LAS char*)(size_t)ad[NA + nt % NB] + (C::BMODE == 1 ? kk * 4096 : (kk * (BN / 16) + nt) * 1024) + h * 512));
    if (h == 0) fb[nt] = __builtin_shufflevector(v, v, 0, 1, 2, 3, -1, -1, -1, -1);
    else { const s16x4 lo = __builtin_shufflevector(fb[nt], fb[nt], 0, 1, 2, 3); fb[nt] = __builtin_shufflevector(lo, v, 0, 1, 2, 3, 4, 5, 6, 7); }
  };
  constexpr int GAPS = NR <= NM - 2 ? NR : NM - 2, FREE = NM - GAPS;
  auto half = [&](const s16x8 (&ca)[MT], const s16x8 (&cb)[NT], s16x8 (&na)[MT], s16x8 (&nb)[NT], int kk, bool rdnext, bool zero = false) {
#pragma unroll
    for (int i = 0; i < NM; ++i) {
      const int nt = i / MT, mt = i % MT;
      if (MT == 4 && kk == 1 && i == 0) WAIT_L(NR - BR - MT);
      if (MT == 4 && kk == 1 && i == MT) WAIT_L(MT);
      if (zero) asm volatile("v_mfma_f32_16x16x32_bf16 %0, %1, %2, 0" : "=&v"(acc[mt][nt]) : "v"(cb[nt]), "v"(ca[mt]));
      else asm volatile("v_mfma_f32_16x16x32_bf16 %0, %1, %2, %0" : "+v"(acc[mt][nt]) : "v"(cb[nt]), "v"(ca[mt]));
      __builtin_amdgcn_sched_barrier(0);
      if (rdnext) {
#pragma unroll
        for (int u = (i * NR + GAPS - 1) / GAPS; u < ((i + 1) * NR + GAPS - 1) / GAPS; ++u) if (u < NR) rd(na, nb, kk, u);
        if (kk == 0 && i == GAPS) RING_NEXT(cx, dx, RX);
        if (kk == 0 && i == GAPS + 1) RING_NEXT(cy, dy, RY);
        if (kk == 1 && i >= GAPS) {
#pragma unroll
          for (int u = i - GAPS; u < NA + NB; u += FREE) asm volatile("v_add_u32 %0, %1, %0" : "+v"(ad[u]) : "s"(u < NA ? dx : dy));
        }
      }
      __builtin_amdgcn_sched_barrier(0);
    }
  };

  s16x8 fa0[MT], fb0[NT], fa1[MT], fb1[NT];
  RING_NEXT(cx, dx, RX); RING_NEXT(cy, dy, RY);
  auto residual = [&]() {
    int ln = lane; asm volatile("" : "+v"(ln));
    const int r = ln & 15, j = ln >> 4;
    const bool raw = PH == G2 && l == 0;
    const rsrc_t rr = mk_rsrc(raw ? p.x : wl + (PH == G2 ? O_X : O_X1), S_ * D_ * 2);
    const unsigned o = (unsigned)((m0 >> 4) + (j & 1)) * (D_ * 32) + (raw ? r * (D_ * 2) + n0 * 2 + (j >> 1) * 16 : (n0 >> 4) * 512 + (j >> 1) * 256 + r * 16);
#pragma unroll
    for (int nt = 0; nt < 4; ++nt) rv[nt] = __builtin_amdgcn_raw_buffer_load_b128(rr, o, nt * (raw ? 32 : 512), 16);
  };
  auto add_residual = [&]() {
#pragma unroll
    for (int nt = 0; nt < 4; ++nt) {
      u32x2 x0, x1; unpair_rows(rv[nt], x0, x1);
      if (PH == K5) { acc[0][nt] = unpack4(x0); acc[1][nt] = unpack4(x1); } else { acc[0][nt] += unpack4(x0); acc[1][nt] += unpack4(x1); }
    }
  };
  if (PH == K5) {
    if ((blockIdx.x >> 3) >= 28) wait_flags(smem, flags(wl, F_G2, t.bm) + (lane & 15), ~tag_of(smem));
    residual();
    add_residual();
  }
  constexpr bool ZERO = PH == GA || PH == G2;
  if (!ZERO) {
#pragma unroll
    for (int mt = 0; mt < MT; ++mt) {
#pragma unroll
      for (int nt = 0; nt < NT; ++nt) if (PH != K5) acc[mt][nt] = (f32x4){ini[mt], ini[mt], ini[mt], ini[mt]};
      asm volatile("" : "+v"(acc[mt][0]), "+v"(acc[mt][1]), "+v"(acc[mt][2]), "+v"(acc[mt][3]));
    }
  }
  if (PH == ZZ) BARRIER();
  BARRIER();
  TS(1); TSR(7); TR(0);
#pragma unroll
  for (int i = 0; i < NR; ++i) rd(fa0, fb0, 0, i);
  auto step = [&]() {
    BARRIER();
    __builtin_amdgcn_sched_barrier(0);
    half(fa1, fb1, fa0, fb0, 0, true);
    half(fa0, fb0, fa1, fb1, 1, true);
    WAIT_L(0);
  };
#pragma unroll 1
  for (int part = 0; part < C::PARTS; ++part) {
    if (!ZERO) {
#pragma unroll
      for (int mt = 0; mt < MT; ++mt) asm volatile("s_nop 1" : "+v"(acc[mt][0]), "+v"(acc[mt][1]), "+v"(acc[mt][2]), "+v"(acc[mt][3]));
    }
    __builtin_amdgcn_sched_barrier(0);
    half(fa0, fb0, fa1, fb1, 1, true, ZERO);
    WAIT_L(0);
    TR(part * TP + 1);
    step();
    int s = 2;
#pragma unroll 1
    for (int seg = PH == ZZ && part ? 0 : 2; seg < 3; ++seg) {
      const int e = seg == 2 ? TP : NORM_S + seg * 4;
#pragma unroll 1
      for (; s < e; s += 2) { TR(part * TP + s); step(); TR(part * TP + s + 1); step(); if (PH == GA || PH == G2) WAIT_V(0); }
      if (PH == ZZ) {
        WAIT_V(0);
        if (seg == 0) {
          int ln = lane; asm volatile("" : "+v"(ln));
          const int r = ln & 15, j = ln >> 4;
          const rsrc_t ro = mk_rsrc(wl + O_Z2, S_ * FH * 2);
          const unsigned o = (unsigned)((m0 >> 5) * (FH / 16) + (n0 >> 4)) * 1024u + tr_lane(r, j);
          TS(10);
#pragma unroll
          for (int mp = 0; mp < 2; ++mp) {
            const f32x2 f0 = *(const LAS f32x2*)(smem + FS_OFF + (ml + mp * 32 + r) * 8), f1 = *(const LAS f32x2*)(smem + FS_OFF + (ml + mp * 32 + 16 + r) * 8);
#pragma unroll
            for (int nt = 0; nt < 4; ++nt)
              st16(ro, o + mp * (FH / 16) * 1024 + nt * 1024, pair_rows(pack4(zf[(mp * 2) % MT][nt] * f0[0] + f0[1]), pack4(zf[(mp * 2 + 1) % MT][nt] * f1[0] + f1[1])));
          }
          TS(11);
        }
      }
    }
    TR(part * TP + TP);
    BARRIER();
    __builtin_amdgcn_sched_barrier(0);
    half(fa1, fb1, fa0, fb0, 0, C::PARTS > 1);
#pragma unroll
    for (int mt = 0; mt < MT; ++mt) asm volatile("s_nop %4" : "+v"(acc[mt][0]), "+v"(acc[mt][1]), "+v"(acc[mt][2]), "+v"(acc[mt][3]) : "n"(mt ? 0 : 11));
    if (part == C::PARTS - 1) { TS(2); TSR(14); } else TS(8);

    int ln = lane; asm volatile("" : "+v"(ln));
    const int r = ln & 15, j = ln >> 4;
    if (PH == GA) {
      const int hi = n0 >= D_, nn = n0 - hi * D_;
      const rsrc_t ro = mk_rsrc(wl + O_AB, 2 * S_ * D_ * 2);
      const unsigned o = (unsigned)(((hi * S_ + m0 + part * 64) >> 5) * (D_ / 16) + (nn >> 4)) * 1024u + tr_lane(r, j);
      f32x2 st0 = *(const LAS f32x2*)(smem + ST_OFF + (part * 64 + ml + r) * 16), st1 = *(const LAS f32x2*)(smem + ST_OFF + (part * 64 + ml + 16 + r) * 16);
      if (hi) { st0 = -st0; st1 = -st1; }
#pragma unroll
      for (int nt = 0; nt < NT; ++nt) {
        const f32x4 cs = *(const LAS f32x4*)(smem + CS1_OFF + (wn * 64 + nt * 16 + j * 4) * 4);
        st16(ro, o + nt * 1024, pair_rows(pack4(acc[0][nt] * st0[0] + cs * st0[1]), pack4(acc[1][nt] * st1[0] + cs * st1[1])));
      }
      const int wb = blockIdx.x < 224 ? blockIdx.x : blockIdx.x - 32, col0 = (wid >> 1) * FH + (wb >> 4) * 128 + (wid & 1) * 64 + (ln & 7) * 8;
      if (l == 0 && part == 0) {
#pragma unroll
        for (int i = 0; i < 8; ++i) ld16_async(wv[i], p.win + ((wb & 15) * 64 + i * 8) * (F_ * 2), (ln >> 3) * (F_ * 2) + col0 * 2);
      } else if (l == 0) {
        LANDED8(4, wv);
        float s[8], e[8];
        unpack8(__builtin_bit_cast(u32x4, wv[0]), s);
#pragma unroll
        for (int i = 1; i < 8; ++i) {
          unpack8(__builtin_bit_cast(u32x4, wv[i]), e);
#pragma unroll
          for (int k = 0; k < 8; ++k) s[k] += e[k];
        }
#pragma unroll
        for (int k = 0; k < 4; ++k) { unsigned a = __builtin_bit_cast(unsigned, s[k]), b = __builtin_bit_cast(unsigned, s[k + 4]); swap32(a, b); s[k] = __builtin_bit_cast(float, a) + __builtin_bit_cast(float, b); }
#pragma unroll
        for (int k = 0; k < 2; ++k) { unsigned a = __builtin_bit_cast(unsigned, s[k]), b = __builtin_bit_cast(unsigned, s[k + 2]); swap16(a, b); s[k] = dpp_add<0x128>(__builtin_bit_cast(float, a) + __builtin_bit_cast(float, b)); }
        if (!(ln & 8) && blockIdx.x < 224) __hip_atomic_store((u64*)(p.ws + O_SI) + ((wb >> 1) & 7) * F_ + col0 + (ln >> 4) * 2 + (wb & 1), __builtin_bit_cast(u64, (f32x2){s[0], s[1]}), RLX_AGENT);
      }
    } else if (PH == ZZ) {
      f32x4 k[MT], cs[4];
#pragma unroll
      for (int mt = 0; mt < MT; ++mt) k[mt] = *(const LAS f32x4*)(smem + ST_OFF + (ml + mt * 16 + r) * 16);
      if (part == 0) {
#pragma unroll
        for (int nt = 0; nt < 4; ++nt) cs[nt] = *(const LAS f32x4*)(smem + CS3_OFF + (128 + wn * 64 + nt * 16 + j * 4) * 4);
#pragma unroll
        for (int mt = 0; mt < MT; ++mt)
#pragma unroll
          for (int nt = 0; nt < 4; ++nt) acc[mt][nt] += cs[nt] * k[mt][0];
      }
      f32x2 ps = {0.f, 0.f};
#pragma unroll
      for (int mt = 0; mt < MT; ++mt) {
        f32x4 (&z)[4] = zf[mt];
#pragma unroll
        for (int nt = 0; nt < 4; ++nt) z[nt] = gelu_tanh4(acc[mt][nt], k[mt]);
        if (part == 0) {
          const f32x4 s4 = (z[0] + z[1]) + (z[2] + z[3]), q4 = z[0] * z[0] + z[1] * z[1] + z[2] * z[2] + z[3] * z[3];
          const float sm = sum_groups((s4[0] + s4[1]) + (s4[2] + s4[3])), q = sum_groups((q4[0] + q4[1]) + (q4[2] + q4[3]));
          if (j == mt) ps = (f32x2){sm, q};
        }
      }
      if (part == 0) {
        *(LAS f32x2*)(smem + PS_OFF + (wn * 128 + ml + ln) * 8) = ps;
#pragma unroll
        for (int nt = 0; nt < 4; ++nt) cs[nt] = *(const LAS f32x4*)(smem + CS3_OFF + (wn * 64 + nt * 16 + j * 4) * 4);
#pragma unroll
        for (int mt = 0; mt < MT; ++mt)
#pragma unroll
          for (int nt = 0; nt < 4; ++nt) acc[mt][nt] = cs[nt] * k[mt][0];
      }
    }
    if (part < C::PARTS - 1) TS(9);
  }

  asm volatile("" : "+v"(lane));
  const int r = lane & 15, j = lane >> 4;
  const unsigned imgl = (j & 1) * (OW / 32) * 1024u + (j >> 1) * 256 + r * 16;
  if (PH == G2 || PH == K5) {
    if (PH == G2) {
      const rsrc_t rx = mk_rsrc(wl + O_XB, 256 * 16384);
      const unsigned so = (blockIdx.x * 4 + wid) * 4096 + lane * 16, go = ((blockIdx.x ^ 1) * 4 + wid) * 4096 + lane * 16;
      const u64 tag = tag_of(smem);
#pragma unroll
      for (int i = 0; i < 4; ++i) { const u32x2 lo = pack4(acc[MT - 2][i]), hi = pack4(acc[MT - 1][i]); st16(rx, so + i * 1024, (u32x4){lo[0], lo[1], hi[0], hi[1]}); }
      residual();
      WAIT_V(4);
      Flag* fl = flags(wl, F_XC, 0);
      if (lane == 0) __hip_atomic_store(fl[blockIdx.x * 4 + wid].v, tag, RLX_AGENT);
      TS(8);
      const u64 seen = peek(fl[(blockIdx.x ^ 1) * 4 + wid].v);
      add_residual();
      wait_flags(smem, fl + (blockIdx.x ^ 1) * 4 + wid, seen);
      u32x4 pv[4];
#pragma unroll
      for (int i = 0; i < 4; ++i) pv[i] = ld16<16>(rx, go + i * 1024);
      TSR(15);
      TS(9);
#pragma unroll
      for (int i = 0; i < 4; ++i) { acc[0][i] += unpack4((u32x2){pv[i][0], pv[i][1]}); acc[1][i] += unpack4((u32x2){pv[i][2], pv[i][3]}); }
    }
    u32x2 y[2][4];
#pragma unroll
    for (int nt = 0; nt < 4; ++nt) { y[0][nt] = pack4(acc[0][nt]); y[1][nt] = pack4(acc[1][nt]); }
    if (PH == K5 && l == 1) {
      const rsrc_t ro = mk_rsrc(p.out, S_ * D_ * 2);
#pragma unroll
      for (int i = 0; i < 4; ++i)
        __builtin_amdgcn_raw_buffer_store_b128(pair_cols(y[i >> 1][(i & 1) * 2], y[i >> 1][(i & 1) * 2 + 1]), ro, ((m0 + (i >> 1) * 16 + r) * D_ + n0 + (i & 1) * 32 + j * 8) * 2, 0, 0);
      if (blockIdx.x == 0 && wid == 0 && lane == 0) __hip_atomic_store((u64*)(p.ws + O_EP), tag_of(smem), RLX_AGENT);
      TS(3);
    } else {
      char* wo = PH == G2 ? wl : wl + LSTRIDE;
      const rsrc_t ro = mk_rsrc(wo + (PH == G2 ? O_X1 : O_X), S_ * D_ * 2);
#pragma unroll
      for (int i = 0; i < 4; ++i) st16(ro, (unsigned)((m0 >> 4) * (OW / 32)) * 1024u + ((n0 >> 4) + i) * 512 + imgl, pair_rows(y[0][i], y[1][i]));
      f32x2 ps[2];
#pragma unroll
      for (int mt = 0; mt < 2; ++mt) {
        f32x4 z[4];
#pragma unroll
        for (int nt = 0; nt < 4; ++nt) z[nt] = unpack4(y[mt][nt]);
        const f32x4 s4 = (z[0] + z[1]) + (z[2] + z[3]), q4 = z[0] * z[0] + z[1] * z[1] + z[2] * z[2] + z[3] * z[3];
        ps[mt] = (f32x2){sum_groups((s4[0] + s4[1]) + (s4[2] + s4[3])), sum_groups((q4[0] + q4[1]) + (q4[2] + q4[3]))};
      }
      if (j < 2) __hip_atomic_store(stat_slot(wo + (PH == G2 ? O_ST1 : O_STX), n0 >> 6, m0 + j * 16 + r), __builtin_bit_cast(u64, j ? ps[1] : ps[0]), RLX_AGENT);
      TS(3);
      if (PH == G2) signal_done(smem, l * 8 + 2, flags(wl, F_G2, t.bm) + t.bn * 2 + t.kh, 1, lane);
      else signal_done(smem, l * 8 + 5, flags(wl, F_K5, t.bm) + t.bn, 1, lane);
    }
  } else if (PH == K4) {
    const rsrc_t ro = mk_rsrc(wl + O_H, S_ * FH * 2);
#pragma unroll
    for (int mp = 0; mp < 2; ++mp)
#pragma unroll
      for (int nt = 0; nt < NT; ++nt)
        st16(ro, (unsigned)(((m0 >> 4) + mp * 2) * (OW / 32)) * 1024u + ((n0 >> 4) + nt) * 512 + imgl,
             pair_rows(pack4(acc[(mp * 2) % MT][nt] * unpack4(zk[mp * 2][nt])), pack4(acc[(mp * 2 + 1) % MT][nt] * unpack4(zk[mp * 2 + 1][nt]))));
    TS(3);
    signal_done(smem, l * 8 + 4, flags(wl, F_K4, t.bm) + t.bn, 1, lane);
  } else {
    if (PH == ZZ) {
#pragma unroll
      for (int mt = 0; mt < MT; ++mt)
#pragma unroll
        for (int nt = 0; nt < 4; ++nt) zk[mt % 4][nt] = pack4(zf[mt][nt]);
    }
    TS(3);
  }
  TS(6); TSR(5); TS_OUT(PH);
}

template <int N, int W> __device__ __forceinline__ f32x2 row_stat(const f32x4 (&g)[8]) {
  f32x4 a = g[0];
#pragma unroll
  for (int i = 1; i < N; ++i) a += g[i];
  const float mu = (a[0] + a[2]) * (1.0f / W), v = __builtin_fmaf(-mu, mu, (a[1] + a[3]) * (1.0f / W));
  return (f32x2){mu, (v < 0.f ? 0.f : v) + LN_EPS};
}

template <class C> __device__ __forceinline__ Flag* act_flag(char* wl) {
  const Tile<C> t;
  if (C::PH == GA) return flags(wl - LSTRIDE, F_K5, t.bm);
  if (C::PH == G2) return flags(wl, F_GA, t.kh * 8 + t.bn);
  if (C::PH == ZZ) return flags(wl, F_G2, t.bm);
  if (C::PH == K4) return flags(wl, F_Z, t.bn);
  return flags(wl, F_K4, t.bm);
}
template <class C> __device__ __forceinline__ Stream<C, 1 - C::WSIDE> act_stream(const Params& p, int l, int w, int lane) {
  constexpr int PH = C::PH;
  const Tile<C> t;
  const bool raw = PH == GA && l == 0;
  return Stream<C, 1 - C::WSIDE>(raw ? p.x : p.ws + l * LSTRIDE + (PH == GA ? O_X : (PH == G2 ? O_AB : (PH == ZZ ? O_X1 : (PH == K4 ? O_Z2 : O_H)))),
                                 S_ * 2 * (PH == G2 ? 2 * D_ : (PH == K4 || PH == K5 ? FH : D_)), t, w, lane, raw);
}
template <class C>
__device__ __forceinline__ void act_phase(LAS char* smem, const Params& p, int l, int w, int lane, u64& early) {
  constexpr int PH = C::PH;
  const int wid = 6 + w;
  TS_DECL; TS(0);
  asm volatile("" : "+v"(lane));
  char* wl = p.ws + l * LSTRIDE;
  auto st = act_stream<C>(p, l, w, lane);
  const int fi = PH == K5 && (lane & 15) >= 14 ? 0 : lane & 15;
  if (PH == G2) st.slot = 16384;
  else {
    if (PH != GA || l) wait_flags(smem, act_flag<C>(wl) + fi, PH == K4 ? early : ~tag_of(smem));
    TS(1);
    if (PH == ZZ) { st.fill(smem, 0, 1, false); BARRIER(); }
    if (PH != GA) st.fill(smem, PH == ZZ, st.D, false);
  }
  if (PH == GA) st.ramp(smem); else st.start();
  TS(2);
  if (PH == GA) {
    const Tile<C> t;
    const int row = w * 64 + lane;
    u64 seen;
    st.steps(smem, 3, 4);
    st.template step<0>(smem, 4, [&] {
      Flag* f = lane == 0 ? flags(wl, F_X0, t.bm) + t.bn : flags(wl, F_WD, t.bn) + t.bm;
      if (l == 0 && w == 0 && lane < 2) __hip_atomic_store(f->v, tag_of(smem), RLX_AGENT);
    });
    const unsigned fo = ((lane & 16 ? F_WD * 16 + t.bn : F_X0 * 16 + t.bm) * 16 + (lane & 15)) * sizeof(Flag);
    f32x4 g[8], c[8];
    st.steps(smem, 5, 8);
    ld8_async(seen, p.ws + O_SF, fo);
    st.steps(smem, 8, 10);
    LANDED1(8, seen);
    wait_flags(smem, (Flag*)(p.ws + O_SF + fo), seen);
#pragma unroll
    for (int i = 0; i < 6; ++i) ld16_async(g[i], wl + O_STX + (i * S_ + t.bm * 128) * 16, row * 16);
    st.template steps<6>(smem, 10, 11);
#pragma unroll
    for (int i = 6; i < 8; ++i) ld16_async(g[i], wl + O_STX + (i * S_ + t.bm * 128) * 16, row * 16);
#pragma unroll
    for (int i = 0; i < 4; ++i) ld16_async(c[i], p.ws + O_SD + (i * 2048 + t.bn * 128) * 16, row * 16);
    st.template steps<12>(smem, 11, 14);
    LANDED8(12, g);
    {
      const f32x2 m = row_stat<8, D_>(g);
      const float rs = __builtin_amdgcn_rsqf(m[1]);
      *(LAS f32x2*)(smem + ST_OFF + row * 16) = (f32x2){rs, -m[0] * rs};
    }
    st.steps(smem, 14, 15);
    asm volatile("" : "+v"(c[0]), "+v"(c[1]), "+v"(c[2]), "+v"(c[3]));
    {
      const f32x4 a = (c[0] + c[1]) + (c[2] + c[3]);
      *(LAS float*)(smem + CS1_OFF + row * 4) = (a[0] + a[1]) + (a[2] + a[3]);
    }
    WAIT_L0();
    st.steps(smem, 15, 20);
    st.template step<0>(smem, 20, [&] { if (w == 0 && lane == 0) __hip_atomic_store(flags(wl, F_GA, t.bn)[t.bm].v, tag_of(smem), RLX_AGENT); });
    auto nx = act_stream<CG2>(p, l, w, lane);
    nx.slot = 16384;
    st.steps(smem, 21, 27);
    ld8_async(seen, act_flag<CG2>(wl), fi * sizeof(Flag));
    BARRIER();
    LANDED1(0, seen);
    wait_flags(smem, act_flag<CG2>(wl) + fi, seen);
#pragma unroll 1
    for (int k = 0; k < 3; ++k) { BARRIER(); nx.fill(smem, k, k + 1, false); if (k < 2) BARRIER(); }
  } else {
    st.finish(smem, 1, [&] { if (PH == ZZ) early = peek(act_flag<CK4>(wl)[fi].v); });
  }
  TS(3); TS_OUT(PH);
}

template <class C> __device__ __forceinline__ Stream<C, C::WSIDE> wt_stream(const Params& p, int w, int lane) {
  constexpr int PH = C::PH;
  const Tile<C> t;
  return Stream<C, C::WSIDE>(PH == GA ? (t.bn >= 8 ? p.wdim : p.wdre) : (PH == G2 ? (t.kh ? p.wsim : p.wsre) : (PH == ZZ ? p.win : (PH == K4 ? p.wsp : p.wout))),
                             2 * (PH == GA ? D_ * D_ : (PH == ZZ ? D_ * F_ : (PH == K5 ? FH * D_ : S_ * S_))), t, w, lane);
}
template <class C, class NEXT>
__device__ __forceinline__ void wt_phase(LAS char* smem, const Params& p, int l, int w, int lane, unsigned& wpos, bool mid) {
  constexpr int PH = C::PH;
  asm volatile("" : "+v"(lane));
  const Tile<C> t;
  char* wl = p.ws + l * LSTRIDE;
  auto st = wt_stream<C>(p, w, lane);
  st.slot = wpos;
  const int row = w * 64 + lane;
  int s1 = 1;
  if (PH == GA && l == 0) { st.ramp(smem); s1 = 3; }
  else {
    if (PH == GA) st.fill(smem, 0, st.D, true);
    if (PH != ZZ) st.start();
  }
  if (PH == ZZ) {
    f32x4 g[8];
    if (l == 0) {
      wait_flags(smem, flags(wl, F_WI, t.bn) + (lane & 15), ~tag_of(smem));
#pragma unroll
      for (int i = 0; i < 8; ++i) ld16_async(g[i], p.ws + O_SI + (i * F_ + w * FH + t.bn * 128) * 8, lane * 16);
      LANDED8(0, g);
      const f32x4 a = ((g[0] + g[1]) + (g[2] + g[3])) + ((g[4] + g[5]) + (g[6] + g[7]));
      *(LAS f32x2*)(smem + CS3_OFF + row * 8) = (f32x2){a[0] + a[2], a[1] + a[3]};
    }
    WAIT_V(0);
    BARRIER();
#pragma unroll
    for (int i = 0; i < 8; ++i) ld16_async(g[i], wl + O_ST1 + (i * S_ + t.bm * 128) * 16, row * 16);
    BARRIER();
    st.template steps<8>(smem, 1, 2);
    LANDED8(8, g);
    {
      const f32x2 m = row_stat<8, D_>(g);
      const float rs = __builtin_amdgcn_rsqf(m[1]);
      *(LAS f32x4*)(smem + ST_OFF + row * 16) = (f32x4){-m[0], GC1 * rs, GC2 * rs * rs * rs, m[1] * rs};
      WAIT_L0();
    }
    {
      st.steps(smem, 2, 17);
      st.template step<0>(smem, 17, [&] {
        const u32x2 q = __builtin_bit_cast(u32x2, *(const LAS f32x2*)(smem + PS_OFF + row * 8) + *(const LAS f32x2*)(smem + PS_OFF + (128 + row) * 8));
        const u32x2 v = {q[0] < 0xfffffffeu ? q[0] : 0xfffffffeu, q[1] < 0xfffffffeu ? q[1] : 0xfffffffeu};
        __hip_atomic_store(stat_slot(wl + O_XCH, t.bn, t.bm * 128 + row), __builtin_bit_cast(u64, v), RLX_AGENT);
      });
      st.steps(smem, 18, 13 + NORM_S);
#pragma unroll
      for (int i = 0; i < 7; ++i) ld16_async(g[i], wl + O_XCH + (i * S_ + t.bm * 128) * 16, row * 16);
      st.template steps<7>(smem, 13 + NORM_S, 15 + NORM_S);
      LANDED8(16, g);
#pragma unroll 1
      for (unsigned spin = 0; spin < SPIN_MAX; ++spin) {
        unsigned mx = 0;
#pragma unroll
        for (int i = 0; i < 7; ++i) { const u32x4 u = __builtin_bit_cast(u32x4, g[i]); mx = max(max(mx, max(u[0], u[1])), max(u[2], u[3])); }
        if (__all(mx != 0xffffffffu)) break;
#pragma unroll
        for (int i = 0; i < 7; ++i) ld16_async(g[i], wl + O_XCH + (i * S_ + t.bm * 128) * 16, row * 16);
        LANDED8(0, g);
      }
      const f32x2 m = row_stat<7, FH>(g);
      const float rs = __builtin_amdgcn_rsqf(m[1]);
      *(LAS f32x2*)(smem + FS_OFF + row * 8) = (f32x2){rs, -m[0] * rs};
      WAIT_L0();
      s1 = 15 + NORM_S;
    }
    static_assert(NORM_S + 20 > C::T - 6, "");
  } else if (PH == G2) {
    Flag* fl = flags(wl, F_GB, t.kh * 8 + t.bn);
    const int fi = lane & 15;
    u64 seen;
    st.steps(smem, 1, 4);
    st.template step<0>(smem, 4, [&] {
      const Tile<CGA> ta;
      Flag* f = lane == 0 ? flags(wl, F_GB, ta.bn) + ta.bm : flags(wl, F_WI, blockIdx.x >> 4) + (blockIdx.x & 15);
      if (w == 0 && lane < 2) __hip_atomic_store(f->v, tag_of(smem), RLX_AGENT);
    });
    st.steps(smem, 5, 8);
    ld8_async(seen, fl, fi * sizeof(Flag));
    st.steps(smem, 8, 10);
    LANDED1(16, seen);
    wait_flags(smem, fl + fi, seen);
    s1 = 10;
  }
  if (PH == K5) st.finish(smem, s1, [] {});
  else {
    st.steps(smem, s1, C::T - st.D + 1);
    auto sig = [&](int s) { if (PH == ZZ && s == NORM_S + 20 && w == 0 && lane == 0) __hip_atomic_store(flags(wl, F_Z, t.bn)[t.bm].v, tag_of(smem), RLX_AGENT); };
    if (PH != G2 || mid) { auto nx = wt_stream<NEXT>(p, w, lane); st.hand_over(smem, nx, sig); }
    else { auto nx = wt_stream<CK5>(p, w, lane); st.hand_over(smem, nx, sig); }
  }
  wpos = st.slot;
}

__device__ __forceinline__ void touch(LAS char* smem, const Params& p, int wid, int lane) {
  const int u = ((blockIdx.x >> 3) - 28 + (blockIdx.x & 7) * 4) * 8 + wid;
  BARRIER();
#pragma unroll 1
  for (int i = 0; i < 46; i += 2) {
    const char* g = (i < 32 ? p.wsp + (size_t)(u * 32 + i) * 1024 : p.wout + (size_t)(u * 14 + i - 32) * 1024) + lane * 16;
    __builtin_amdgcn_global_load_lds((const unsigned*)g, (LAS unsigned*)(smem + A_RING + wid * 1024), 16, 0, 0);
    __builtin_amdgcn_global_load_lds((const unsigned*)(g + 1024), (LAS unsigned*)(smem + A_RING + wid * 1024), 16, 0, 0);
    WAIT_V(8);
  }
  WAIT_V(0);
  BARRIER();
}

__global__ __launch_bounds__(512, 2) void mega_k(Params p) {
  extern __shared__ __attribute__((aligned(16))) char smem_[];
  LAS char* smem = (LAS char*)smem_;
  const int lane = threadIdx.x & 63, wid = __builtin_amdgcn_readfirstlane(threadIdx.x >> 6);
  const bool mid = (blockIdx.x >> 3) < 28;
  unsigned wpos = 0;
  if (wid < 4) {
    first_job(smem, p, wid, lane);
#pragma unroll 1
    for (int l = 0; l < 2; ++l) {
      u32x2 zk[4][4];
      compute_phase<CGA>(smem, p, l, wid, lane, zk, wpos);
      compute_phase<CG2>(smem, p, l, wid, lane, zk, wpos);
      if (mid) { compute_phase<CZZ>(smem, p, l, wid, lane, zk, wpos); compute_phase<CK4>(smem, p, l, wid, lane, zk, wpos); }
      else if (l == 0) touch(smem, p, wid, lane);
      compute_phase<CK5>(smem, p, l, wid, lane, zk, wpos);
    }
  } else if (wid < 6) {
#pragma unroll 1
    for (int l = 0; l < 2; ++l) {
      wt_phase<CGA, CG2>(smem, p, l, wid - 4, lane, wpos, mid);
      wt_phase<CG2, CZZ>(smem, p, l, wid - 4, lane, wpos, mid);
      if (mid) { wt_phase<CZZ, CK4>(smem, p, l, wid - 4, lane, wpos, mid); wt_phase<CK4, CK5>(smem, p, l, wid - 4, lane, wpos, mid); }
      else if (l == 0) touch(smem, p, wid, lane);
      wt_phase<CK5, CK5>(smem, p, l, wid - 4, lane, wpos, mid);
    }
  } else {
#pragma unroll 1
    for (int l = 0; l < 2; ++l) {
      u64 early = 0;
      act_phase<CGA>(smem, p, l, wid - 6, lane, early);
      act_phase<CG2>(smem, p, l, wid - 6, lane, early);
      if (mid) { act_phase<CZZ>(smem, p, l, wid - 6, lane, early); act_phase<CK4>(smem, p, l, wid - 6, lane, early); }
      else if (l == 0) touch(smem, p, wid, lane);
      act_phase<CK5>(smem, p, l, wid - 6, lane, early);
    }
  }
}

extern "C" void kernel_launch(void* const* d_in, const int* in_sizes, int n_in, void* d_out, int out_size, void* d_ws, size_t ws_size, hipStream_t stream) {
  Params p;
  p.x = (const char*)d_in[0];
  p.wsre = (const char*)d_in[1]; p.wsim = (const char*)d_in[2]; p.wdre = (const char*)d_in[3]; p.wdim = (const char*)d_in[4];
  p.win = (const char*)d_in[6]; p.wsp = (const char*)d_in[7]; p.wout = (const char*)d_in[9]; p.bsp = (const u16*)d_in[8];
  p.out = (char*)d_out; p.ws = (char*)d_ws;
  (void)hipFuncSetAttribute((const void*)mega_k, hipFuncAttributeMaxDynamicSharedMemorySize, LDS_BYTES);
  hipLaunchKernelGGL(mega_k, dim3(256), dim3(512), LDS_BYTES, stream, p);
}
```

```cpp
#include <hip/hip_runtime.h>
#include <hip/hip_bf16.h>
#include <stdint.h>

#define LAS __attribute__((address_space(3)))
typedef unsigned short u16;
typedef short s16x4 __attribute__((ext_vector_type(4)));
typedef short s16x8 __attribute__((ext_vector_type(8)));
typedef __bf16 bf16x8 __attribute__((ext_vector_type(8)));
typedef __bf16 bf16x2 __attribute__((ext_vector_type(2)));
typedef float f32x2 __attribute__((ext_vector_type(2)));
typedef float f32x4 __attribute__((ext_vector_type(4)));
typedef unsigned u32x2 __attribute__((ext_vector_type(2)));
typedef unsigned u32x4 __attribute__((ext_vector_type(4)));

constexpr int S_ = 2048, D_ = 1024, F_ = 3584, FH = 1792;
constexpr float LN_EPS = 1e-5f;

__device__ __forceinline__ unsigned pack_bf16(float lo, float hi) {
  f32x2 v = {lo, hi};
  return __builtin_bit_cast(unsigned, __builtin_convertvector(v, bf16x2));
}
__device__ __forceinline__ float bf_lo(unsigned u) { return __builtin_bit_cast(float, u << 16); }
__device__ __forceinline__ float bf_hi(unsigned u) { return __builtin_bit_cast(float, u & 0xffff0000u); }
__device__ __forceinline__ void swap16(unsigned& a, unsigned& b) { asm volatile("s_nop 1\n\tv_permlane16_swap_b32 %0, %1" : "+v"(a), "+v"(b)); }
__device__ __forceinline__ void swap32(unsigned& a, unsigned& b) { asm volatile("s_nop 1\n\tv_permlane32_swap_b32 %0, %1" : "+v"(a), "+v"(b)); }
__device__ __forceinline__ float sum32(float v) {
  unsigned a = __builtin_bit_cast(unsigned, v), b = a; swap32(a, b);
  return __builtin_bit_cast(float, a) + __builtin_bit_cast(float, b);
}
__device__ __forceinline__ float sum_groups(float v) {
  unsigned a = __builtin_bit_cast(unsigned, v), b = a; swap16(a, b);
  return sum32(__builtin_bit_cast(float, a) + __builtin_bit_cast(float, b));
}
__device__ __forceinline__ u32x4 pair_rows(u32x2 a, u32x2 b) {
  unsigned a0 = a[0], a1 = a[1], b0 = b[0], b1 = b[1]; swap16(a0, b0); swap16(a1, b1);
  return (u32x4){a0, a1, b0, b1};
}
__device__ __forceinline__ void unpair_rows(u32x4 v, u32x2& a, u32x2& b) {
  unsigned a0 = v[0], a1 = v[1], b0 = v[2], b1 = v[3]; swap16(a0, b0); swap16(a1, b1);
  a = (u32x2){a0, a1}; b = (u32x2){b0, b1};
}
__device__ __forceinline__ u32x4 pair_cols(u32x2 a, u32x2 b) {
  unsigned a0 = a[0], a1 = a[1], b0 = b[0], b1 = b[1]; swap32(a0, b0); swap32(a1, b1); swap16(a0, b0); swap16(a1, b1);
  return (u32x4){a0, a1, b0, b1};
}
__device__ __forceinline__ void unpair_cols(u32x4 v, u32x2& a, u32x2& b) {
  unsigned a0 = v[0], a1 = v[1], b0 = v[2], b1 = v[3]; swap16(a0, b0); swap16(a1, b1); swap32(a0, b0); swap32(a1, b1);
  a = (u32x2){a0, a1}; b = (u32x2){b0, b1};
}
__device__ __forceinline__ u32x2 pack4(f32x4 v) { return (u32x2){pack_bf16(v[0], v[1]), pack_bf16(v[2], v[3])}; }
__device__ __forceinline__ f32x4 unpack4(u32x2 y) { return (f32x4){bf_lo(y[0]), bf_hi(y[0]), bf_lo(y[1]), bf_hi(y[1])}; }
constexpr float GC1 = -2.0f * 0.7978845608028654f * 1.4426950408889634f, GC2 = GC1 * 0.044715f;
__device__ __forceinline__ f32x4 gelu_tanh4(f32x4 a, f32x4 k) {
  const f32x4 t = a * (a * a * k[2] + k[1]);
  f32x4 e;
#pragma unroll
  for (int i = 0; i < 4; ++i) e[i] = __builtin_amdgcn_exp2f(t[i]);
  e = e * k[3] + k[3];
#pragma unroll
  for (int i = 0; i < 4; ++i) e[i] = __builtin_amdgcn_rcpf(e[i]);
  return a * e;
}
#define STAUX 16
typedef __amdgpu_buffer_rsrc_t rsrc_t;
__device__ __forceinline__ rsrc_t mk_rsrc(const void* p, unsigned bytes) { return __builtin_amdgcn_make_buffer_rsrc((void*)p, 0, bytes, 0x00020000); }
__device__ __forceinline__ void st16(rsrc_t r, unsigned off, u32x4 v) { __builtin_amdgcn_raw_buffer_store_b128(v, r, off, 0, STAUX); }
template <int AUX> __device__ __forceinline__ u32x4 ld16(rsrc_t r, unsigned off) { return __builtin_amdgcn_raw_buffer_load_b128(r, off, 0, AUX); }
#define WAIT_V(n) asm volatile("s_waitcnt vmcnt(%0)" ::"n"(n) : "memory")
#define WAIT_L(n) __builtin_amdgcn_s_waitcnt(0xc07f | ((n) << 8))
#define WAIT_VL(n) asm volatile("s_waitcnt vmcnt(%0) lgkmcnt(0)" ::"n"(n) : "memory")
#define WAIT_L0() asm volatile("s_waitcnt lgkmcnt(0)" ::: "memory")
#define BARRIER() do { asm volatile("" ::: "memory"); __builtin_amdgcn_s_barrier(); asm volatile("" ::: "memory"); } while (0)
#define PIN_S(x) asm volatile("" : "+s"(x))

#define RLX_AGENT __ATOMIC_RELAXED, __HIP_MEMORY_SCOPE_AGENT
#define PF_THR 8
#define POLL_SLEEP 1
#define NORM_S 8
#define SIGA_S 4
#define FIRST3 1
#define LAT_S 1
constexpr unsigned SPIN_MAX = 1u << 18;
typedef unsigned long long u64;
template <int CTRL> __device__ __forceinline__ float dpp_add(float v) {
  return v + __builtin_bit_cast(float, __builtin_amdgcn_update_dpp(0, __builtin_bit_cast(int, v), CTRL, 0xf, 0xf, true));
}
__device__ __forceinline__ float sum8(float v) { return dpp_add<0x141>(dpp_add<0x4e>(dpp_add<0xb1>(v))); }

constexpr size_t MB = 1 << 20, LSTRIDE = 40 * MB;
constexpr size_t O_X = 0, O_X1 = 4 * MB, O_AB = 8 * MB, O_Z2 = 16 * MB, O_H = 24 * MB, O_XB = 32 * MB;
constexpr size_t O_STX = 36 * MB, O_ST1 = O_STX + 256 * 1024;
constexpr size_t O_XCH = O_STX + 512 * 1024;
constexpr size_t O_SF = O_STX + 768 * 1024;
constexpr size_t O_SD = 2 * LSTRIDE, O_SI = O_SD + 128 * 1024;
constexpr size_t O_EP = O_SD + 512 * 1024;
enum { F_X0, F_WD, F_WI, F_GA, F_GB, F_G2, F_Z, F_K4, F_K5, F_XC };
constexpr unsigned W_RING = 96 * 1024, A_RING = W_RING, A_BYTES = 48 * 1024, CS1_OFF = 144 * 1024, CS3_OFF = CS1_OFF + 512, ST_OFF = CS3_OFF + 1024, PS_OFF = ST_OFF + 2048,
                   FS_OFF = PS_OFF + 2048, BS_OFF = FS_OFF + 1024, CNT_OFF = BS_OFF + 512, EP_OFF = CNT_OFF + 128;
constexpr unsigned LDS_BYTES = EP_OFF + 256;

enum { GA = 0, G2 = 1, ZZ = 2, K4 = 3, K5 = 4 };

struct Params {
  const char *x, *wsre, *wsim, *wdre, *wdim, *win, *wsp, *wout;
  const u16* bsp;
  char* out;
  char* ws;
};

template <int PH_, int BM_, int BN_, int WM_, int WN_, int AM_, int BMODE_, int T_, int PARTS_, int TN_, int XN_, int PA_, int PB_, int WSIDE_>
struct Cfg {
  static constexpr int PH = PH_, BM = BM_, BN = BN_, WM = WM_, WN = WN_, AM = AM_, BMODE = BMODE_, T = T_, PARTS = PARTS_;
  static constexpr int TN = TN_, TJ = T_ - TN_, XM = 4, XN = XN_, PA = PA_, PB = PB_, WSIDE = WSIDE_;
  static constexpr int MT = BM / WM / 16, NT = BN / WN / 16;
};
#define GA_TN 6
#define GA_FS 2
#define GA_HK 6
using CGA = Cfg<GA, 128, 128, 2, 2, 1, 0, 16, 1, GA_TN, 8, D_ / 32, 2048,    1>;
using CG2 = Cfg<G2, 128, 128, 2, 2, 0, 2, 32, 1, 0,     8, 4096,    D_ / 16, 0>;
using CZZ = Cfg<ZZ, 128, 128, 2, 2, 1, 1, 32, 2, 0,     7, D_ / 32, F_ * 2,  1>;
using CK4 = Cfg<K4, 128, 128, 2, 2, 0, 2, 32, 1, 0,     7, 4096,    FH / 16, 0>;
using CK5 = Cfg<K5, 128, 64,  4, 1, 1, 1, 28, 1, 0,     8, FH / 32, D_ * 2,  1>;
template <int V> struct IC { static constexpr int v = V; };
struct PairRing { static constexpr unsigned SLOT = 32768, D = 3; };

template <class C> struct Tile {
  int bm, bn, kh, cls, rot;
  __device__ __forceinline__ Tile() {
    int bid = blockIdx.x; PIN_S(bid);
    const int xcd = bid & 7, idx = bid >> 3, lm = idx / C::XN, ln = idx % C::XN;
    bm = (xcd >> 1) * C::XM + lm; bn = C::PH == G2 ? ln : (xcd & 1) * C::XN + ln; kh = C::PH == G2 ? xcd & 1 : 0; cls = (lm + ln) & 3;
#define ROTMASK 16
    rot = !(ROTMASK >> C::PH & 1) ? 0 : (C::PH == GA || C::PH == K5 ? (xcd & 1) * (C::T / 2) : (xcd >> 1) * (C::PH == K4 ? 8 : 4));
  }
};
template <class C, int SIDE> struct Ring {
  static constexpr bool ISW = C::WSIDE == SIDE;
  static constexpr unsigned SLOT = (SIDE ? C::BN : C::BM) * 128, BASE = ISW ? 0 : A_RING, D = (ISW ? W_RING : A_BYTES) / SLOT;
};

#define FW 32
struct Flag { u64 v[FW]; };
__device__ __forceinline__ Flag* flags(char* wl, int id, int panel) { return (Flag*)(wl + O_SF) + (id * 16 + panel) * 16; }
__device__ __forceinline__ u64 tag_of(LAS char* smem) { return *(const volatile LAS u64*)(smem + EP_OFF); }
__device__ __forceinline__ void signal_done(LAS char* smem, int slot, Flag* flag, int nf, int lane) {
  const u64 tag = tag_of(smem);
  WAIT_V(0);
  unsigned old = 0;
  if (lane == 0) old = __hip_atomic_fetch_add((LAS unsigned*)(smem + CNT_OFF) + slot, 1u, __ATOMIC_RELAXED, __HIP_MEMORY_SCOPE_WORKGROUP);
  if (__builtin_amdgcn_readfirstlane(old) == 3 && lane < nf) __hip_atomic_store(flag->v, tag, RLX_AGENT);
}
__device__ __forceinline__ u64 peek(u64* f) { return __hip_atomic_load(f, RLX_AGENT); }
__device__ __forceinline__ void ld8_async(u64& d, const void* sbase, unsigned voff) { asm volatile("s_nop 4\n\tglobal_load_dwordx2 %0, %1, %2 sc1" : "=v"(d) : "v"(voff), "s"(sbase) : "memory"); }
#define LCNT(n) (n)
#define LANDED1(n, a) asm volatile("s_waitcnt vmcnt(%1)" : "+v"(a) : "n"(LCNT(n)) : "memory")
__device__ __forceinline__ void ld16_async(f32x4& d, const void* sbase, unsigned voff) { asm volatile("s_nop 4\n\tglobal_load_dwordx4 %0, %1, %2 sc1" : "=v"(d) : "v"(voff), "s"(sbase) : "memory"); }
#define LANDED8(n, g) asm volatile("s_waitcnt vmcnt(%8)" : "+v"(g[0]), "+v"(g[1]), "+v"(g[2]), "+v"(g[3]), "+v"(g[4]), "+v"(g[5]), "+v"(g[6]), "+v"(g[7]) : "n"(LCNT(n)) : "memory")
__device__ __forceinline__ u64* stat_slot(char* base, int g, int row) { return (u64*)base + ((g >> 1) * S_ + row) * 2 + (g & 1); }
__device__ __forceinline__ void wait_flags(LAS char* smem, Flag* f, u64 first) {
  const u64 tag = tag_of(smem);
  if (!__all(first == tag)) {
#pragma unroll 1
    for (unsigned spin = 0; spin < SPIN_MAX; ++spin) {
      if (__all(peek(f->v) == tag)) break;
      if (POLL_SLEEP) __builtin_amdgcn_s_sleep(POLL_SLEEP);
    }
  }
  asm volatile("" ::: "memory");
}

template <int GAP> __device__ __forceinline__ void poll_flags(u64 tag, const void* base, unsigned off, unsigned mask) {
  unsigned left = SPIN_MAX;
  u64 q;
  asm volatile("s_mov_b32 exec_lo, %5\n\ts_mov_b32 exec_hi, 0\n\ts_nop 4\n"
               ".Lpoll_next%=:\n\tglobal_load_dwordx2 %0, %2, %3 sc1\n\ts_waitcnt vmcnt(0)\n\tv_cmp_ne_u64 vcc, %4, %0\n\ts_and_b64 exec, exec, vcc\n\t"
               "s_cbranch_scc0 .Lpoll_done%=\n\ts_sleep %6\n\ts_sub_u32 %1, %1, 1\n\ts_cbranch_scc0 .Lpoll_next%=\n"
               ".Lpoll_done%=:\n\ts_mov_b64 exec, -1"
               : "=&v"(q), "+s"(left) : "v"(off), "s"(base), "v"(tag), "s"(__builtin_amdgcn_readfirstlane(mask)), "n"(GAP) : "vcc", "scc", "memory");
}
__device__ __forceinline__ void wait_count(LAS char* smem, int slot, unsigned n) {
#pragma unroll 1
  for (unsigned spin = 0; spin < 64 * SPIN_MAX; ++spin) {
    if (*((const volatile LAS unsigned*)(smem + CNT_OFF) + slot) >= n) break;
    __builtin_amdgcn_s_sleep(1);
  }
  asm volatile("" ::: "memory");
}

template <class C, int SIDE> struct Stream {
  using R = Ring<C, SIDE>;
  static constexpr int NP = R::SLOT / 2048, T = C::T, D = R::D, MODE = SIDE ? C::BMODE : (C::AM ? 3 : 0), PITCH = SIDE ? C::PB : C::PA;
  static constexpr unsigned KS = MODE == 0 ? 128 : (MODE == 1 ? 64 * PITCH : (MODE == 2 ? 2 * PITCH * 1024 : 2048));
  static_assert(T % 4 == 0 && D * NP <= 63, "");
  rsrc_t rs; unsigned base, voff, ks, slot, swp; int cls, rot, lat, w;
  __device__ __forceinline__ Stream(const char* src, unsigned bytes, const Tile<C>& t, int w_, int lane, bool raw = false) {
    rs = mk_rsrc(src, bytes); w = w_; cls = t.cls; rot = t.rot; lat = LAT_S; ks = KS; slot = 0; swp = t.kh * 32 * PITCH;
    const int b = SIDE ? t.bn : t.bm;
    const unsigned sw = ((lane & 7) ^ ((w << 2) | (lane >> 4))) << 4;
    if (MODE == 0) { base = ((C::PH == GA ? b & 7 : b) * 128 + w * 8) * PITCH; voff = (lane >> 3) * PITCH + sw; }
    else if (MODE == 1) { base = w * 8 * PITCH + b * C::BN * 2; voff = (lane >> 3) * PITCH + (((lane & 7) ^ ((((lane >> 4) & 1) << 2) | (w << 1))) << 4); }
    else if (MODE == 2) { base = (b * (C::BN / 16) + w) * 1024 + t.kh * T * KS; voff = lane * 16; }
    else if (raw) { base = (b * 128 + w * 8) * (PITCH * 64); voff = (lane >> 3) * (PITCH * 64) + sw; ks = 128; }
    else { base = (b * 8 * PITCH + w) * 1024; voff = lane * 16; }
  }
  __device__ __forceinline__ unsigned poff(int i) const {
    if (MODE == 0) return C::PH == G2 ? (i * 16 * PITCH) ^ swp : i * 16 * PITCH;
    if (MODE == 1) return (i & 3) * 16 * PITCH + (i >> 2) * 128;
    if (MODE == 2) return ((i >> 2) * PITCH + (i & 3) * 2) * 1024;
    return i * PITCH * 1024;
  }
  __device__ __forceinline__ int chunk(int s) const {
    constexpr int L = T / C::PARTS;
    const int hi = C::PARTS == 2 ? s >> 4 : 0;
    int ch = s - hi * L;
    if (C::TN && s >= L) ch = s < L + 6 ? s - 6 : s - 6 - C::TN;
#define LEADMASK 23
#define LEAD0 4
    if ((LEADMASK >> C::PH & 1) && (ch & 3) == cls && ch >= LEAD0) { ch += 4; if (ch >= L) ch -= L - LEAD0; }
    if (C::PH == GA) ch = ch == LAT_S ? lat : (ch == lat ? LAT_S : ch);
    else if (C::PH == G2) ch = ((ch + rot) & 15) * 2 + (ch >> 4);
    else { ch += rot; if (ch >= L) ch -= L; }
    return ch;
  }
  __device__ __forceinline__ unsigned writers(int s) const { return C::PH == GA ? 1u << chunk(s) : (C::PH == ZZ ? 3u << (chunk(s) & 14) : 1u << (chunk(s) >> 1)); }
  __device__ __forceinline__ unsigned source(int s) const {
    unsigned g = base + chunk(s) * ks;
    if (C::PH == ZZ && SIDE == 1) g += (1 - (s >> 4)) * (FH * 2);
    return g;
  }
  __device__ __forceinline__ void issue(LAS char* smem, int s, unsigned at, bool thr = false) const {
    constexpr bool TWO = C::TN && !R::ISW;
    const bool tail = TWO && s >= C::TJ;
    const int v = tail ? 2 * s - C::TJ : s;
    unsigned g = source(v), g1 = g;
    if (tail) { g1 = source(v + 1); if (v >= T) g += poff(NP / 2); else g1 -= poff(NP / 2); }
#pragma unroll
    for (int i = 0; i < NP; ++i) {
      __builtin_amdgcn_raw_ptr_buffer_load_lds(rs, (LAS void*)(smem + R::BASE + at + (w + 2 * i) * 1024), 16, voff, (TWO && i >= NP / 2 ? g1 : g) + poff(i), 0, R::ISW ? 0 : 16);
      if (PF_THR && thr && (i & 1)) WAIT_V(PF_THR);
    }
  }
  __device__ __forceinline__ void adv() { slot += R::SLOT; if (slot == D * R::SLOT) slot = 0; }
  __device__ __forceinline__ void fill(LAS char* smem, int s0, int s1, bool thr) {
#pragma unroll 1
    for (int s = s0; s < s1; ++s) { issue(smem, s, slot, thr); adv(); }
  }
  __device__ __forceinline__ void start() const {
    WAIT_V((D - 1) * NP);
    BARRIER();
    WAIT_V((D - 2) * NP);
  }
  __device__ __forceinline__ void ramp(LAS char* smem) {
#define WRAMP 2
    fill(smem, 0, WRAMP, false); WAIT_V((WRAMP - 1) * NP); BARRIER();
    fill(smem, WRAMP, 4, false); WAIT_V(2 * NP); BARRIER();
    fill(smem, 4, 6, false); WAIT_V(3 * NP); BARRIER();
    fill(smem, 6, 8, false); WAIT_V(4 * NP);
  }
  template <int X, class F> __device__ __forceinline__ void step(LAS char* smem, int s, F pre) {
    BARRIER();
    pre();
    issue(smem, s + D - 1, slot);
    adv();
    WAIT_V((D - 2) * NP + X > 63 ? 63 : (D - 2) * NP + X);
  }
  template <int X = 0> __device__ __forceinline__ void steps(LAS char* smem, int s0, int s1) {
#pragma unroll 1
    for (int s = s0; s < s1; ++s) step<X>(smem, s, [] {});
  }
  template <class F> __device__ __forceinline__ void finish(LAS char* smem, int s0, F last) {
    steps(smem, s0, T - D + 1);
    BARRIER();
    WAIT_V((D - 3) * NP);
#pragma unroll 1
    for (int s = T - D + 2; s < T - 1; ++s) { BARRIER(); WAIT_V(0); }
    BARRIER();
    last();
    BARRIER();
  }
  template <int X = 0> __device__ __forceinline__ void go(LAS char* smem, int b, int b1, bool last = false) {
    static_assert(C::TN == 0 || (D == 6 && C::TN >= 6 && C::TN % 2 == 0 && C::TJ % 2 == 0), "");
#pragma unroll 1
    for (;;) {
      if (C::TN) {
        const int v = b <= C::TJ ? b + 5 : 2 * b - C::TJ + 4;
        if (v < T || v >= T + 6) {
          fill(smem, v, v + 1 + (b > C::TJ), false);
          if (b < C::TJ - 1) WAIT_VL(4 * NP); else if (b < C::TJ) WAIT_VL(3 * NP); else WAIT_VL(2 * NP);
        } else { adv(); adv(); WAIT_VL(0); }
      } else {
        issue(smem, b + D - 1, slot);
        adv();
        WAIT_VL((D - 2) * NP + X);
      }
      if (++b == b1) break;
      BARRIER();
    }
    if (!last) BARRIER();
  }
  template <class NX> __device__ __forceinline__ void tail_over(LAS char* smem, NX& nx) {
    static_assert(C::TN == 0 || NX::NP == NP, "");
    nx.slot = slot;
#define TO_A 1
#define TO_B 2
#define TO_C 3
    BARRIER(); nx.fill(smem, 0, TO_A, false); if (C::TN > 6) WAIT_V(TO_A * NP);
    BARRIER(); nx.fill(smem, TO_A, TO_B, false);
    BARRIER(); nx.fill(smem, TO_B, TO_C, false); WAIT_V((TO_C - 1) * NP);
    BARRIER(); nx.fill(smem, TO_C, 6, false); WAIT_V(4 * NP);
  }
  template <class NX, class F> __device__ __forceinline__ void hand_over(LAS char* smem, NX& nx, F pre) {
    constexpr int Q = NP / NX::NP;
    nx.slot = slot;
#pragma unroll 1
    for (int k = 0; k < D; ++k) { BARRIER(); pre(T - D + 1 + k); nx.fill(smem, k * Q, k * Q + Q, false); WAIT_V((D - 2) * NP); }
  }
};

#define TS(i)
#define TSR(i)
#define TS_DECL
#define TS_OUT(ph)

__device__ __forceinline__ unsigned tr_lane(int r, int j) { return ((r >> 2) & 1) * 512 + (j & 1) * 256 + (r >> 3) * 128 + (r & 3) * 32 + (j >> 1) * 16; }
__device__ __forceinline__ void unpack8(u32x4 v, float (&e)[8]) {
#pragma unroll
  for (int i = 0; i < 4; ++i) { e[2 * i] = bf_lo(v[i]); e[2 * i + 1] = bf_hi(v[i]); }
}

__device__ __forceinline__ float dot2(float a, float b, float c) { return __builtin_amdgcn_fdot2_f32_bf16(__builtin_bit_cast(bf16x2, a), __builtin_bit_cast(bf16x2, b), c, false); }
#define FIRST_SLEEP 4
__device__ __forceinline__ void first_touch(const Params& p, int wid, int lane, u64 (&tch)[2]) {
  const Tile<CG2> u; const Tile<CZZ> z;
#define TCH0_N 4
#define TCH0_H 1
#define TCH1_N 4
  ld8_async(tch[0], (u.kh ? p.wsim : p.wsre) + (u.bm * 128 + u.bn * 16 + wid * 4) * (S_ * 2), (lane >> 4) * (S_ * 2) + ((((lane >> 1) & (TCH0_N - 1)) + u.rot) & 15) * 256 + (lane & TCH0_H) * 64);
  ld8_async(tch[1], p.win + ((z.bm & 15) * 4 + wid) * TCH1_N * (F_ * 2) + (FH + z.bn * 128) * 2, (lane >> 2) % TCH1_N * (F_ * 2) + (lane & 3) * 64);
}
__device__ __forceinline__ void first_tag(LAS char* smem, const Params& p, int wid, int lane) {
  if (wid == 2 && lane < 32) *(LAS unsigned*)(smem + CNT_OFF + lane * 4) = 0u;
  if (wid == 3) {
    u64 e0;
    asm volatile("s_load_dwordx2 %0, %1, 0x0\n\ts_waitcnt lgkmcnt(0)" : "=s"(e0) : "s"(p.ws + O_EP) : "memory");
    u64 e = (e0 ^ (e0 >> 29)) * 0x9E3779B97F4A7C15ull + 0x632BE59BD9B4E019ull;
    if (e == e0) e = ~e0;
    if (lane == 0) *(LAS u64*)(smem + EP_OFF) = e;
  }
  WAIT_L0();
}
__device__ __forceinline__ float join32(float a, float b) {
  unsigned x = __builtin_bit_cast(unsigned, a), y = __builtin_bit_cast(unsigned, b); swap32(x, y);
  x = __builtin_bit_cast(unsigned, __builtin_bit_cast(float, x) + __builtin_bit_cast(float, y)); y = x; swap16(x, y);
  return __builtin_bit_cast(float, x) + __builtin_bit_cast(float, y);
}
__device__ __forceinline__ void first_sums(const Params& p, int wid, int lane, const s16x8 (&a0)[4], const s16x8 (&a1)[4], const s16x8 (&b0)[8], const s16x8 (&b1)[8]) {
  const Tile<CGA> t;
  const int wm = wid >> 1, wn = wid & 1, g = (t.bm + t.bn) & 15;
  const float ones = __builtin_bit_cast(float, 0x3f803f80u);
  float a[2] = {}, q[2] = {}, b[2] = {};
#pragma unroll
  for (int i = 0; i < 4; ++i) {
    const f32x4 x = __builtin_bit_cast(f32x4, wn ? (i & 2 ? a1 : a0)[2 + (i & 1)] : (i & 2 ? a1 : a0)[i & 1]);
    const f32x4 w = __builtin_bit_cast(f32x4, wm ? (i & 2 ? b1 : b0)[2 + (i & 1)] : (i & 2 ? b1 : b0)[i & 1]);
#pragma unroll
    for (int k = 0; k < 4; ++k) { a[i & 1] = dot2(x[k], ones, a[i & 1]); q[i & 1] = dot2(x[k], x[k], q[i & 1]); b[i & 1] = dot2(w[k], ones, b[i & 1]); }
  }
  const f32x2 st = {join32(a[0], a[1]), join32(q[0], q[1])};
  const float sw = join32(b[0], b[1]);
  if (!(lane & 16)) {
    const int row = wn * 64 + wm * 32 + (lane >> 5) * 16 + (lane & 15);
    __hip_atomic_store(stat_slot(p.ws + O_STX, g, t.bm * 128 + row), __builtin_bit_cast(u64, st), RLX_AGENT);
    __hip_atomic_store((unsigned*)(p.ws + O_SD) + ((g >> 2) * 2048 + t.bn * 128 + row) * 4 + (g & 3), __builtin_bit_cast(unsigned, sw), RLX_AGENT);
  }
  if (wid < 2) st16(mk_rsrc(p.ws + wid * LSTRIDE + O_XCH, 256 * 1024), blockIdx.x * 1024 + lane * 16, (u32x4){~0u, ~0u, ~0u, ~0u});
}
__device__ __forceinline__ void column_sums(u64* dst, int lane, f32x4 (&v)[4], f32x4 (&w)[4]) {
  asm volatile("s_waitcnt vmcnt(0)" : "+v"(v[0]), "+v"(v[1]), "+v"(v[2]), "+v"(v[3]), "+v"(w[0]), "+v"(w[1]), "+v"(w[2]), "+v"(w[3]) :: "memory");
  float s[8], e[8];
  unpack8(__builtin_bit_cast(u32x4, v[0]), s);
#pragma unroll
  for (int i = 1; i < 8; ++i) {
    unpack8(__builtin_bit_cast(u32x4, i < 4 ? v[i & 3] : w[i & 3]), e);
#pragma unroll
    for (int k = 0; k < 8; ++k) s[k] += e[k];
  }
#pragma unroll
  for (int k = 0; k < 4; ++k) { unsigned a = __builtin_bit_cast(unsigned, s[k]), b = __builtin_bit_cast(unsigned, s[k + 4]); swap32(a, b); s[k] = __builtin_bit_cast(float, a) + __builtin_bit_cast(float, b); }
#pragma unroll
  for (int k = 0; k < 2; ++k) { unsigned a = __builtin_bit_cast(unsigned, s[k]), b = __builtin_bit_cast(unsigned, s[k + 2]); swap16(a, b); s[k] = dpp_add<0x128>(__builtin_bit_cast(float, a) + __builtin_bit_cast(float, b)); }
  if (!(lane & 8)) __hip_atomic_store(dst + (lane & 7) * 8 + (lane >> 4) * 2, __builtin_bit_cast(u64, (f32x2){s[0], s[1]}), RLX_AGENT);
}

template <class C>
__device__ __forceinline__ void compute_phase(LAS char* smem, const Params& p, int l, int wid, int lane, u32x2 (&zk)[4][4], unsigned& wpos) {
  constexpr int BN = C::BN, MT = C::MT, NT = C::NT, PH = C::PH, TN = C::TN, TP = C::T / C::PARTS - TN;
  using RX = Ring<C, 0>; using RY = Ring<C, 1>;
  TS_DECL; TSR(4); TS(0);
#define TR(i)
  asm volatile("" : "+v"(lane));
  const Tile<C> t;
  char* wl = p.ws + l * LSTRIDE;
  const int wm = wid / C::WN, wn = wid % C::WN, n0 = t.bn * BN + wn * (NT * 16);
  const int ml = wm * (TN ? 32 : MT * 16) + t.kh * 32, m0 = t.bm * 128 + ml;
  constexpr int OW = PH == K4 ? FH : D_;
  f32x4 acc[MT][NT];
  u64 tch[2] = {};
  u32x4 rv[4];
  f32x4 zf[MT][4];
  float ini[MT];
  constexpr int NA = C::AM == 0 || PH == GA ? 2 : 1, NB = C::BMODE == 0 ? 2 : (C::BMODE == 1 ? 4 : 1);
  unsigned ad[NA + NB];
  const unsigned xs = C::WSIDE ? 0 : wpos, ys = C::WSIDE ? wpos : (PH == G2 ? RY::SLOT : 0);
  {
    const int r = lane & 15, j = lane >> 4;
    constexpr int MW = TN ? 2 : MT;
    if (C::AM == 0 || (PH == GA && l == 0)) { ad[0] = RX::BASE + xs + (wm * MW * 16 + r) * 128 + ((j ^ (r >> 1)) << 4); ad[NA - 1] = ad[0] ^ 64; }
    else { ad[0] = RX::BASE + xs + wm * MW * 2048 + lane * 16; if (NA == 2) ad[1] = ad[0] + 1024; }
    if (C::BMODE == 0) { ad[NA] = RY::BASE + ys + (wn * NT * 16 + r) * 128 + ((j ^ (r >> 1)) << 4); ad[NA + NB - 1] = ad[NA] ^ 64; }
    else if (C::BMODE == 1) {
      const int q = (lane >> 2) & 3, pp = lane & 3, f = ((q >> 1) << 1) | (j & 1);
#pragma unroll
      for (int c = 0; c < NB; ++c) ad[NA + c] = RY::BASE + ys + wn * 8192 + (j * 8 + q) * 128 + ((c ^ f) << 5) + pp * 8;
    } else ad[NA] = RY::BASE + ys + wn * NT * 1024 + lane * 8;
#pragma unroll
    for (int u = 0; u < NA + NB; ++u) ad[u] += (unsigned)(size_t)smem;
#pragma unroll
    for (int mt = 0; mt < MT; ++mt) ini[mt] = PH == K4 ? *(const LAS float*)(smem + BS_OFF + (ml + mt * 16 + r) * 4) : 0.f;
  }
  unsigned cx = RX::D - 1 - xs / RX::SLOT, cy = RY::D - 1 - ys / RY::SLOT, dx, dy;
  wpos += (C::T + TN) * Ring<C, C::WSIDE>::SLOT % W_RING; if (wpos >= W_RING) wpos -= W_RING;
#define RING_NEXT(c, d, R) asm volatile("s_sub_u32 %0, %0, 1\n\ts_cselect_b32 %1, %2, %3\n\ts_cselect_b32 %0, %4, %0" : "+s"(c), "=&s"(d) : "n"(-(int)((R::D - 1) * R::SLOT)), "s"(R::SLOT), "n"(R::D - 1) : "scc")
  static_assert(NT == 4, "");
  constexpr int BR = C::BMODE == 0 ? 1 : 2, NM = MT * NT, NF = TN ? 2 * NT : NT;
  auto rd = [&](s16x8 (&fa)[MT], s16x8 (&fb)[NF], int kk, int i, bool two) {
    const int ma = two ? MT / 2 : MT, q = i / (ma + NT * BR);
    i -= q * (ma + NT * BR);
    if (i >= BR && i < BR + ma) {
      const int mt = i - BR + q * ma;
      fa[mt] = *(const LAS s16x8*)((LAS char*)(size_t)ad[NA == 2 ? kk : 0] + (NA == 2 ? 0 : kk * 1024) + (TN ? (mt >> 1) * 8192 + (mt & 1) * 2048 : mt * 2048));
      return;
    }
    const int ib = i < BR ? i : i - ma, nt = ib / BR, h = ib % BR;
    s16x8& f = fb[q * NT + nt];
    if (C::BMODE == 0) { f = *(const LAS s16x8*)((LAS char*)(size_t)ad[NA + kk] + nt * 2048 + q * RY::SLOT); return; }
    const s16x4 v = __builtin_amdgcn_ds_read_tr16_b64_v4i16((LAS s16x4*)((LAS char*)(size_t)ad[NA + nt % NB] + (C::BMODE == 1 ? kk * 4096 : (kk * (BN / 16) + nt) * 1024) + h * 512 + q * RY::SLOT));
    if (h == 0) f = __builtin_shufflevector(v, v, 0, 1, 2, 3, -1, -1, -1, -1);
    else { const s16x4 lo = __builtin_shufflevector(f, f, 0, 1, 2, 3); f = __builtin_shufflevector(lo, v, 0, 1, 2, 3, 4, 5, 6, 7); }
  };
  auto half = [&](auto cm, auto nm, const s16x8 (&ca)[MT], const s16x8 (&cb)[NF], s16x8 (&na)[MT], s16x8 (&nb)[NF], int kk, bool zero = false) {
    constexpr int NR = MT + NT * BR * (nm.v ? nm.v : 1);
#define TGAPS 12
    constexpr int GAPS = nm.v == 2 ? TGAPS : (NR <= NM - 2 ? NR : NM - 2), FREE = NM - GAPS;
#pragma unroll
    for (int i = 0; i < NM; ++i) {
      constexpr int MC = MT / cm.v;
      const int ib = i / MC, m = (ib & 1) ? MC - 1 - i % MC : i % MC, ia = m + ib / NT * MC;
      f32x4& d = acc[m][ib % NT];
      if (MT == 4 && cm.v == 1 && kk == 1 && i == 0) WAIT_L(NR - BR - MT);
      if (MT == 4 && cm.v == 1 && kk == 1 && i == MT) WAIT_L(MT);
      if (cm.v == 2 && kk == 1 && i == 0) WAIT_L(NR / 2);
      if (cm.v == 2 && kk == 1 && i == NM / 2) WAIT_L((NM / 2 * NR + GAPS - 1) / GAPS);
      if (zero) asm volatile("v_mfma_f32_16x16x32_bf16 %0, %1, %2, 0" : "=&v"(d) : "v"(cb[ib]), "v"(ca[ia]));
      else asm volatile("v_mfma_f32_16x16x32_bf16 %0, %1, %2, %0" : "+v"(d) : "v"(cb[ib]), "v"(ca[ia]));
      __builtin_amdgcn_sched_barrier(0);
      if (nm.v) {
#pragma unroll
        for (int u = (i * NR + GAPS - 1) / GAPS; u < ((i + 1) * NR + GAPS - 1) / GAPS; ++u) if (u < NR) rd(na, nb, kk, u, nm.v == 2);
        if (kk == 0 && i == GAPS) RING_NEXT(cx, dx, RX);
        if (kk == 0 && i == GAPS + 1) { if (nm.v == 2) RING_NEXT(cy, dy, PairRing); else RING_NEXT(cy, dy, RY); }
        if (kk == 1 && i >= GAPS) {
#pragma unroll
          for (int u = i - GAPS; u < NA + NB; u += FREE) asm volatile("v_add_u32 %0, %1, %0" : "+v"(ad[u]) : "s"(u < NA ? dx : dy));
        }
      }
      __builtin_amdgcn_sched_barrier(0);
    }
  };

  s16x8 fa0[MT], fb0[NF], fa1[MT], fb1[NF];
  RING_NEXT(cx, dx, RX); RING_NEXT(cy, dy, RY);
  auto residual = [&]() {
    int ln = lane; asm volatile("" : "+v"(ln));
    const int r = ln & 15, j = ln >> 4;
    const bool raw = PH == G2 && l == 0;
    const rsrc_t rr = mk_rsrc(raw ? p.x : wl + (PH == G2 ? O_X : O_X1), S_ * D_ * 2);
    const unsigned o = (unsigned)((m0 >> 4) + (j & 1)) * (D_ * 32) + (raw ? r * (D_ * 2) + n0 * 2 + (j >> 1) * 16 : (n0 >> 4) * 512 + (j >> 1) * 256 + r * 16);
#pragma unroll
    for (int nt = 0; nt < 4; ++nt) rv[nt] = __builtin_amdgcn_raw_buffer_load_b128(rr, o, nt * (raw ? 32 : 512), 16);
  };
  auto add_residual = [&]() {
#pragma unroll
    for (int nt = 0; nt < 4; ++nt) {
      u32x2 x0, x1; unpair_rows(rv[nt], x0, x1);
      if (PH == K5) { acc[0][nt] = unpack4(x0); acc[1][nt] = unpack4(x1); } else { acc[0][nt] += unpack4(x0); acc[1][nt] += unpack4(x1); }
    }
  };
  if (PH == K5) {
    if ((blockIdx.x >> 3) >= 28) wait_flags(smem, flags(wl, F_G2, t.bm) + (lane & 15), ~tag_of(smem));
    residual();
    add_residual();
  }
  constexpr bool ZERO = PH == GA || PH == G2;
  if (!ZERO) {
#pragma unroll
    for (int mt = 0; mt < MT; ++mt) {
#pragma unroll
      for (int nt = 0; nt < NT; ++nt) if (PH != K5) acc[mt][nt] = (f32x4){ini[mt], ini[mt], ini[mt], ini[mt]};
      asm volatile("" : "+v"(acc[mt][0]), "+v"(acc[mt][1]), "+v"(acc[mt][2]), "+v"(acc[mt][3]));
    }
  }
#define TOUCH_AT 1
  if (PH == GA && l == 0) {
    first_tag(smem, p, wid, lane);
    if (TOUCH_AT == 0) { __builtin_amdgcn_s_sleep(FIRST_SLEEP); first_touch(p, wid, lane, tch); }
  }
  const int wcol = (wid >> 1) * FH + t.bn * 128 + (wid & 1) * 64;
  if (PH == ZZ && l == 0) {
#pragma unroll
    for (int i = 0; i < 8; ++i) ld16_async(zf[i >> 2][i & 3], p.win + (t.bm * 64 + i * 8) * (F_ * 2) + wcol * 2, (lane >> 3) * (F_ * 2) + (lane & 7) * 16);
  }
  BARRIER();
  TS(1); TSR(7); TR(0);
#pragma unroll
  for (int i = 0; i < MT + NT * BR; ++i) rd(fa0, fb0, 0, i, false);
  constexpr IC<1> ONE{}; constexpr IC<2> TWO{};
  auto stepm = [&](auto m, auto pre) {
    BARRIER();
    pre();
    __builtin_amdgcn_sched_barrier(0);
    half(m, m, fa1, fb1, fa0, fb0, 0);
    half(m, m, fa0, fb0, fa1, fb1, 1);
    WAIT_L(0);
  };
  auto step = [&]() { stepm(ONE, [] {}); };
#pragma unroll 1
  for (int part = 0; part < C::PARTS; ++part) {
    if (!ZERO) {
#pragma unroll
      for (int mt = 0; mt < MT; ++mt) asm volatile("s_nop 1" : "+v"(acc[mt][0]), "+v"(acc[mt][1]), "+v"(acc[mt][2]), "+v"(acc[mt][3]));
    }
    __builtin_amdgcn_sched_barrier(0);
    half(ONE, ONE, fa0, fb0, fa1, fb1, 1, ZERO);
    WAIT_L(0);
    if (PH == ZZ && l + part == 0) {
      int ln = lane; asm volatile("" : "+v"(ln));
      column_sums((u64*)(p.ws + O_SI) + (t.bm >> 1) * F_ + wcol + (t.bm & 1), ln, zf[0], zf[1]);
    }
    auto sums = [&](int at) {
      if constexpr (PH == GA) {
        if (l == 0) {
          int ln = lane; asm volatile("" : "+v"(ln));
          if (at == LAT_S) first_sums(p, wid, ln, fa0, fa1, fb0, fb1);
          if (at + 1 == TOUCH_AT) first_touch(p, wid, ln, tch);
        }
      }
    };
    sums(0);
    TR(part * TP + 1);
    step();
    sums(1);
    int s = 2;
#pragma unroll 1
    for (int seg = PH != ZZ ? 2 : (part ? 0 : 1 + l); seg < 3; ++seg) {
      const int e = seg == 2 ? TP : (part ? NORM_S + seg * 4 : 4);
#pragma unroll 1
      for (; s < e; s += 2) { TR(part * TP + s); step(); TR(part * TP + s + 1); step(); if (PH == GA || PH == G2) WAIT_V(0); }
      if (PH == ZZ) {
        WAIT_V(0);
        if (seg == 0) {
          int ln = lane; asm volatile("" : "+v"(ln));
          const int r = ln & 15, j = ln >> 4;
          const rsrc_t ro = mk_rsrc(wl + O_Z2, S_ * FH * 2);
          const unsigned o = (unsigned)((m0 >> 5) * (FH / 16) + (n0 >> 4)) * 1024u + tr_lane(r, j);
          TS(10);
#pragma unroll
          for (int mp = 0; mp < 2; ++mp) {
            const f32x2 f0 = *(const LAS f32x2*)(smem + FS_OFF + (ml + mp * 32 + r) * 8), f1 = *(const LAS f32x2*)(smem + FS_OFF + (ml + mp * 32 + 16 + r) * 8);
#pragma unroll
            for (int nt = 0; nt < 4; ++nt)
              st16(ro, o + mp * (FH / 16) * 1024 + nt * 1024, pair_rows(pack4(zf[(mp * 2) % MT][nt] * f0[0] + f0[1]), pack4(zf[(mp * 2 + 1) % MT][nt] * f1[0] + f1[1])));
          }
          TS(11);
        }
      }
    }
    TR(part * TP + TP);
    BARRIER();
    __builtin_amdgcn_sched_barrier(0);
    if (PH == GA) asm volatile("" :: "v"(tch[0]), "v"(tch[1]));
    if (TN) cy >>= 1;
    half(ONE, IC<TN ? 2 : (C::PARTS > 1)>{}, fa1, fb1, fa0, fb0, 0);
    if constexpr (TN > 0) {
      static_assert(GA_FS >= 2 && GA_FS < TN / 2, "");
      u64 tag = 0;
      int sig = -1;
#pragma unroll 1
      for (int hp = 0; hp < 2; ++hp) {
        half(TWO, TWO, fa0, fb0, fa1, fb1, 1);
        WAIT_L(0);
#pragma unroll
        for (int s = 1; s < TN / 2; ++s) {
          TR(TP + hp * (TN / 2) + s);
          stepm(TWO, [&] { if (s == GA_FS) TSR(12); if (s == GA_FS && lane == sig) __hip_atomic_store(flags(wl, F_GA, t.bn)[t.bm].v, tag, RLX_AGENT); });
          if (s + 1 == GA_FS) WAIT_V(0);
        }
        TR(TP + (hp + 1) * (TN / 2));
        BARRIER();
        __builtin_amdgcn_sched_barrier(0);
        half(TWO, TWO, fa1, fb1, fa0, fb0, 0);
#pragma unroll
        for (int mt = 0; mt < 2; ++mt) asm volatile("s_nop %4" : "+v"(acc[mt][0]), "+v"(acc[mt][1]), "+v"(acc[mt][2]), "+v"(acc[mt][3]) : "n"(mt ? 0 : 11));
        if (hp) { TS(2); TSR(14); } else TS(8);
        int ln = lane; asm volatile("" : "+v"(ln));
        const int r = ln & 15, j = ln >> 4;
        const int hi = n0 >= D_, nn = n0 - hi * D_;
        const rsrc_t ro = mk_rsrc(wl + O_AB, 2 * S_ * D_ * 2);
        const unsigned o = (unsigned)(((hi * S_ + m0 + hp * 64) >> 5) * (D_ / 16) + (nn >> 4)) * 1024u + tr_lane(r, j);
        f32x2 st0 = *(const LAS f32x2*)(smem + ST_OFF + (hp * 64 + ml + r) * 16), st1 = *(const LAS f32x2*)(smem + ST_OFF + (hp * 64 + ml + 16 + r) * 16);
        f32x4 cs[NT];
#pragma unroll
        for (int nt = 0; nt < NT; ++nt) cs[nt] = *(const LAS f32x4*)(smem + CS1_OFF + (wn * 64 + nt * 16 + j * 4) * 4);
        tag = tag_of(smem); sig = -wid;
        if (hi) { st0 = -st0; st1 = -st1; }
#pragma unroll
        for (int nt = 0; nt < NT; ++nt) {
          st16(ro, o + nt * 1024, pair_rows(pack4(acc[0][nt] * st0[0] + cs[nt] * st0[1]), pack4(acc[1][nt] * st1[0] + cs[nt] * st1[1])));
          acc[0][nt] = acc[2][nt]; acc[1][nt] = acc[3][nt];
        }
#pragma unroll
        for (int mt = 0; mt < 2; ++mt) asm volatile("s_nop 1" : "+v"(acc[mt][0]), "+v"(acc[mt][1]), "+v"(acc[mt][2]), "+v"(acc[mt][3]));
        __builtin_amdgcn_sched_barrier(0);
        if (!hp) TS(9);
      }
      break;
    }
#pragma unroll
    for (int mt = 0; mt < MT; ++mt) asm volatile("s_nop %4" : "+v"(acc[mt][0]), "+v"(acc[mt][1]), "+v"(acc[mt][2]), "+v"(acc[mt][3]) : "n"(mt ? 0 : 11));
    if (part == C::PARTS - 1) { TS(2); TSR(14); } else TS(8);

    int ln = lane; asm volatile("" : "+v"(ln));
    const int r = ln & 15, j = ln >> 4;
    if (PH == ZZ) {
      f32x4 k[MT], cs[4];
#pragma unroll
      for (int mt = 0; mt < MT; ++mt) k[mt] = *(const LAS f32x4*)(smem + ST_OFF + (ml + mt * 16 + r) * 16);
      if (part == 0) {
#pragma unroll
        for (int nt = 0; nt < 4; ++nt) cs[nt] = *(const LAS f32x4*)(smem + CS3_OFF + (128 + wn * 64 + nt * 16 + j * 4) * 4);
#pragma unroll
        for (int mt = 0; mt < MT; ++mt)
#pragma unroll
          for (int nt = 0; nt < 4; ++nt) acc[mt][nt] += cs[nt] * k[mt][0];
      }
      f32x2 ps = {0.f, 0.f};
#pragma unroll
      for (int mt = 0; mt < MT; ++mt) {
        f32x4 (&z)[4] = zf[mt];
#pragma unroll
        for (int nt = 0; nt < 4; ++nt) z[nt] = gelu_tanh4(acc[mt][nt], k[mt]);
        if (part == 0) {
          const f32x4 s4 = (z[0] + z[1]) + (z[2] + z[3]), q4 = z[0] * z[0] + z[1] * z[1] + z[2] * z[2] + z[3] * z[3];
          const float sm = sum_groups((s4[0] + s4[1]) + (s4[2] + s4[3])), q = sum_groups((q4[0] + q4[1]) + (q4[2] + q4[3]));
          if (j == mt) ps = (f32x2){sm, q};
        }
      }
      if (part == 0) {
        *(LAS f32x2*)(smem + PS_OFF + (wn * 128 + ml + ln) * 8) = ps;
#pragma unroll
        for (int nt = 0; nt < 4; ++nt) cs[nt] = *(const LAS f32x4*)(smem + CS3_OFF + (wn * 64 + nt * 16 + j * 4) * 4);
#pragma unroll
        for (int mt = 0; mt < MT; ++mt)
#pragma unroll
          for (int nt = 0; nt < 4; ++nt) acc[mt][nt] = cs[nt] * k[mt][0];
      }
    }
    if (part < C::PARTS - 1) TS(9);
  }

  asm volatile("" : "+v"(lane));
  const int r = lane & 15, j = lane >> 4;
  const unsigned imgl = (j & 1) * (OW / 32) * 1024u + (j >> 1) * 256 + r * 16;
  if (PH == G2 || PH == K5) {
    if (PH == G2) {
      const rsrc_t rx = mk_rsrc(wl + O_XB, 256 * 16384);
      const unsigned so = (blockIdx.x * 4 + wid) * 4096 + lane * 16, go = ((blockIdx.x ^ 1) * 4 + wid) * 4096 + lane * 16;
      const u64 tag = tag_of(smem);
#pragma unroll
      for (int i = 0; i < 4; ++i) { const u32x2 lo = pack4(acc[MT - 2][i]), hi = pack4(acc[MT - 1][i]); st16(rx, so + i * 1024, (u32x4){lo[0], lo[1], hi[0], hi[1]}); }
      residual();
      WAIT_V(4);
      Flag* fl = flags(wl, F_XC, 0);
      if (lane == 0) __hip_atomic_store(fl[blockIdx.x * 4 + wid].v, tag, RLX_AGENT);
      TS(8);
      const u64 seen = peek(fl[(blockIdx.x ^ 1) * 4 + wid].v);
      add_residual();
      wait_flags(smem, fl + (blockIdx.x ^ 1) * 4 + wid, seen);
      u32x4 pv[4];
#pragma unroll
      for (int i = 0; i < 4; ++i) pv[i] = ld16<16>(rx, go + i * 1024);
      TSR(15);
      TS(9);
#pragma unroll
      for (int i = 0; i < 4; ++i) { acc[0][i] += unpack4((u32x2){pv[i][0], pv[i][1]}); acc[1][i] += unpack4((u32x2){pv[i][2], pv[i][3]}); }
    }
    u32x2 y[2][4];
#pragma unroll
    for (int nt = 0; nt < 4; ++nt) { y[0][nt] = pack4(acc[0][nt]); y[1][nt] = pack4(acc[1][nt]); }
    if (PH == K5 && l == 1) {
      const rsrc_t ro = mk_rsrc(p.out, S_ * D_ * 2);
#pragma unroll
      for (int i = 0; i < 4; ++i)
        __builtin_amdgcn_raw_buffer_store_b128(pair_cols(y[i >> 1][(i & 1) * 2], y[i >> 1][(i & 1) * 2 + 1]), ro, ((m0 + (i >> 1) * 16 + r) * D_ + n0 + (i & 1) * 32 + j * 8) * 2, 0, 0);
      if (blockIdx.x == 0 && wid == 0 && lane == 0) __hip_atomic_store((u64*)(p.ws + O_EP), tag_of(smem), RLX_AGENT);
      TS(3);
    } else {
      char* wo = PH == G2 ? wl : wl + LSTRIDE;
      const rsrc_t ro = mk_rsrc(wo + (PH == G2 ? O_X1 : O_X), S_ * D_ * 2);
#pragma unroll
      for (int i = 0; i < 4; ++i) st16(ro, (unsigned)((m0 >> 4) * (OW / 32)) * 1024u + ((n0 >> 4) + i) * 512 + imgl, pair_rows(y[0][i], y[1][i]));
      f32x2 ps[2];
#pragma unroll
      for (int mt = 0; mt < 2; ++mt) {
        f32x4 z[4];
#pragma unroll
        for (int nt = 0; nt < 4; ++nt) z[nt] = unpack4(y[mt][nt]);
        const f32x4 s4 = (z[0] + z[1]) + (z[2] + z[3]), q4 = z[0] * z[0] + z[1] * z[1] + z[2] * z[2] + z[3] * z[3];
        ps[mt] = (f32x2){sum_groups((s4[0] + s4[1]) + (s4[2] + s4[3])), sum_groups((q4[0] + q4[1]) + (q4[2] + q4[3]))};
      }
      if (j < 2) __hip_atomic_store(stat_slot(wo + (PH == G2 ? O_ST1 : O_STX), n0 >> 6, m0 + j * 16 + r), __builtin_bit_cast(u64, j ? ps[1] : ps[0]), RLX_AGENT);
      TS(3);
      if (PH == G2) signal_done(smem, l * 8 + 2, flags(wl, F_G2, t.bm) + t.bn * 2 + t.kh, 1, lane);
      else signal_done(smem, l * 8 + 5, flags(wl, F_K5, t.bm) + t.bn, 1, lane);
    }
  } else if (PH == K4) {
    const rsrc_t ro = mk_rsrc(wl + O_H, S_ * FH * 2);
#pragma unroll
    for (int mp = 0; mp < 2; ++mp)
#pragma unroll
      for (int nt = 0; nt < NT; ++nt)
        st16(ro, (unsigned)(((m0 >> 4) + mp * 2) * (OW / 32)) * 1024u + ((n0 >> 4) + nt) * 512 + imgl,
             pair_rows(pack4(acc[(mp * 2) % MT][nt] * unpack4(zk[mp * 2][nt])), pack4(acc[(mp * 2 + 1) % MT][nt] * unpack4(zk[mp * 2 + 1][nt]))));
    TS(3);
    signal_done(smem, l * 8 + 4, flags(wl, F_K4, t.bm) + t.bn, 1, lane);
  } else {
    if (PH == ZZ) {
#pragma unroll
      for (int mt = 0; mt < MT; ++mt)
#pragma unroll
        for (int nt = 0; nt < 4; ++nt) zk[mt % 4][nt] = pack4(zf[mt][nt]);
    }
    TS(3);
  }
  TS(6); TSR(5); TS_OUT(PH);
}

template <int N, int W> __device__ __forceinline__ f32x2 row_stat(const f32x4 (&g)[8]) {
  f32x4 a = g[0];
#pragma unroll
  for (int i = 1; i < N; ++i) a += g[i];
  const float mu = (a[0] + a[2]) * (1.0f / W), v = __builtin_fmaf(-mu, mu, (a[1] + a[3]) * (1.0f / W));
  return (f32x2){mu, (v < 0.f ? 0.f : v) + LN_EPS};
}

template <class C> __device__ __forceinline__ Flag* act_flag(char* wl) {
  const Tile<C> t;
  if (C::PH == GA) return flags(wl - LSTRIDE, F_K5, t.bm);
  if (C::PH == G2) return flags(wl, F_GA, t.kh * 8 + t.bn);
  if (C::PH == ZZ) return flags(wl, F_G2, t.bm);
  if (C::PH == K4) return flags(wl, F_Z, t.bn);
  return flags(wl, F_K4, t.bm);
}
template <class C> __device__ __forceinline__ Stream<C, 1 - C::WSIDE> act_stream(const Params& p, int l, int w, int lane) {
  constexpr int PH = C::PH;
  const Tile<C> t;
  const bool raw = PH == GA && l == 0;
  return Stream<C, 1 - C::WSIDE>(raw ? p.x : p.ws + l * LSTRIDE + (PH == GA ? O_X : (PH == G2 ? O_AB : (PH == ZZ ? O_X1 : (PH == K4 ? O_Z2 : O_H)))),
                                 S_ * 2 * (PH == G2 ? 2 * D_ : (PH == K4 || PH == K5 ? FH : D_)), t, w, lane, raw);
}
template <class C>
__device__ __forceinline__ void act_phase(LAS char* smem, const Params& p, int l, int w, int lane, u64& early) {
  constexpr int PH = C::PH;
  const int wid = 6 + w;
  TS_DECL; TS(0);
  asm volatile("" : "+v"(lane));
  char* wl = p.ws + l * LSTRIDE;
  auto st = act_stream<C>(p, l, w, lane);
  if (PH == GA && l == 0) { const Tile<C> t; st.lat = (t.bm + t.bn) & 15; }
  const int fi = PH == K5 && (lane & 15) >= 14 ? 0 : lane & 15;
#define POLL_WAIT 6
#define IDLE_GAP 8
  const u64 tag = PH != GA || l ? tag_of(smem) : 0;
  if (PH == G2) st.slot = 16384;
  if ((PH != GA || l) && (PH != K4 || !__all(early == tag))) {
    const unsigned first = FIRST3 ? st.writers(0) | st.writers(1) | st.writers(2) | st.writers(3) : (PH == K5 ? 0x3fffu : 0xffffu);
    if (PH == GA || PH == ZZ || (PH == K5 && (blockIdx.x >> 3) < 28)) { wait_count(smem, PH == GA ? 5 : l * 8 + (PH == ZZ ? 2 : 4), 4); __builtin_amdgcn_s_sleep(POLL_WAIT); }
    else if (PH == K5) poll_flags<IDLE_GAP>(tag, act_flag<C>(wl), lane * sizeof(Flag), 1u << (blockIdx.x & 1) * 7);
    poll_flags<0>(tag, act_flag<C>(wl), lane * sizeof(Flag), first);
  }
  TS(1); TSR(9);
#define ARAMP 1
  st.fill(smem, 0, PH != GA || l ? st.D : ARAMP, false);
  TSR(11);
  if (PH != GA || l) {
    u64 seen;
    ld8_async(seen, act_flag<C>(wl), fi * sizeof(Flag));
    WAIT_V(2 * st.NP + 1);
    BARRIER();
    WAIT_V(st.NP + 1);
    st.template steps<1>(smem, 1, 2);
    st.template step<0>(smem, 2, [&] {
      LANDED1(st.NP, seen);
      const unsigned miss = (unsigned)__builtin_amdgcn_ballot_w64(seen != tag) & 0xffff;
      if (miss) poll_flags<0>(tag, act_flag<C>(wl), fi * sizeof(Flag), miss);
    });
  } else {
    WAIT_V((ARAMP - 1) * st.NP);
    TSR(12);
    BARRIER();
    st.fill(smem, ARAMP, st.D, false);
    TSR(13);
    WAIT_V(st.NP);
    TSR(14);
    st.steps(smem, 1, 3);
  }
  TS(2);
  if (PH == GA) {
    st.steps(smem, 3, C::T - 2);
    BARRIER(); WAIT_V(0);
    BARRIER();
    BARRIER();
    TSR(8);
  } else {
    st.finish(smem, 3, [&] { if (PH == ZZ) early = peek(act_flag<CK4>(wl)[fi].v); });
  }
  TS(3); TS_OUT(PH);
}

template <class C> __device__ __forceinline__ Stream<C, C::WSIDE> wt_stream(const Params& p, int w, int lane) {
  constexpr int PH = C::PH;
  const Tile<C> t;
  return Stream<C, C::WSIDE>(PH == GA ? (t.bn >= 8 ? p.wdim : p.wdre) : (PH == G2 ? (t.kh ? p.wsim : p.wsre) : (PH == ZZ ? p.win : (PH == K4 ? p.wsp : p.wout))),
                             2 * (PH == GA ? D_ * D_ : (PH == ZZ ? D_ * F_ : (PH == K5 ? FH * D_ : S_ * S_))), t, w, lane);
}
template <class C, class NEXT>
__device__ __forceinline__ void wt_phase(LAS char* smem, const Params& p, int l, int w, int lane, unsigned& wpos, bool mid) {
  constexpr int PH = C::PH;
  asm volatile("" : "+v"(lane));
  const Tile<C> t;
  char* wl = p.ws + l * LSTRIDE;
  auto st = wt_stream<C>(p, w, lane);
  st.slot = wpos;
  if (PH == GA && l == 0) st.lat = (t.bm + t.bn) & 15;
  const int row = w * 64 + lane;
  int s1 = 1;
  if (PH == GA && l == 0) {
    st.ramp(smem); s1 = 3;
  }
  else {
    if (PH == GA) st.fill(smem, 0, st.D, true);
    if (PH == ZZ && l == 0) *(LAS unsigned*)(smem + BS_OFF + row * 4) = (unsigned)p.bsp[t.bm * 128 + row] << 16;
    if (PH != G2) st.start();
  }
  if (PH != K5) BARRIER();
  if (PH == ZZ) {
    f32x4 g[8];
    Flag* fl = flags(wl, F_WI, t.bn);
    st.go(smem, 1, 3);
#pragma unroll
    for (int i = 0; i < 8; ++i) ld16_async(g[i], wl + O_ST1 + (i * S_ + t.bm * 128) * 16, row * 16);
    st.template go<8>(smem, 3, 4);
    if (l == 0 && w == 0 && lane == 0) __hip_atomic_store(fl[t.bm].v, tag_of(smem), RLX_AGENT);
    st.template go<8>(smem, 4, 6);
    LANDED8(24, g);
    g[0] = ((g[0] + g[1]) + (g[2] + g[3])) + ((g[4] + g[5]) + (g[6] + g[7]));
    asm volatile("" : "+v"(g[0]));
    st.go(smem, 6, 7);
    asm volatile("" : "+v"(g[0]));
    {
      const f32x2 m = row_stat<1, D_>(g);
      const float rs = __builtin_amdgcn_rsqf(m[1]);
      *(LAS f32x4*)(smem + ST_OFF + row * 16) = (f32x4){-m[0], GC1 * rs, GC2 * rs * rs * rs, m[1] * rs};
    }
    int s2 = 7;
    if (l == 0) {
      u64 seen;
      st.go(smem, 7, 8);
      ld8_async(seen, fl, (lane & 15) * sizeof(Flag));
      st.go(smem, 8, 10);
      LANDED1(16, seen);
      wait_flags(smem, fl + (lane & 15), seen);
#pragma unroll
      for (int i = 0; i < 8; ++i) ld16_async(g[i], p.ws + O_SI + (i * F_ + w * FH + t.bn * 128) * 8, lane * 16);
      st.template go<8>(smem, 10, 13);
      LANDED8(24, g);
      const f32x4 a = ((g[0] + g[1]) + (g[2] + g[3])) + ((g[4] + g[5]) + (g[6] + g[7]));
      *(LAS f32x2*)(smem + CS3_OFF + row * 8) = (f32x2){a[0] + a[2], a[1] + a[3]};
      s2 = 13;
    }
    st.go(smem, s2, 17);
    {
      const u32x2 q = __builtin_bit_cast(u32x2, *(const LAS f32x2*)(smem + PS_OFF + row * 8) + *(const LAS f32x2*)(smem + PS_OFF + (128 + row) * 8));
      const u32x2 v = {q[0] < 0xfffffffeu ? q[0] : 0xfffffffeu, q[1] < 0xfffffffeu ? q[1] : 0xfffffffeu};
      __hip_atomic_store(stat_slot(wl + O_XCH, t.bn, t.bm * 128 + row), __builtin_bit_cast(u64, v), RLX_AGENT);
    }
    st.go(smem, 17, 12 + NORM_S);
#pragma unroll
    for (int i = 0; i < 7; ++i) ld16_async(g[i], wl + O_XCH + (i * S_ + t.bm * 128) * 16, row * 16);
    st.template go<7>(smem, 12 + NORM_S, 14 + NORM_S);
    LANDED8(16, g);
#pragma unroll 1
    for (unsigned spin = 0; spin < SPIN_MAX; ++spin) {
      unsigned mx = 0;
#pragma unroll
      for (int i = 0; i < 7; ++i) { const u32x4 u = __builtin_bit_cast(u32x4, g[i]); mx = max(max(mx, max(u[0], u[1])), max(u[2], u[3])); }
      if (__all(mx != 0xffffffffu)) break;
#pragma unroll
      for (int i = 0; i < 7; ++i) ld16_async(g[i], wl + O_XCH + (i * S_ + t.bm * 128) * 16, row * 16);
      LANDED8(0, g);
    }
    const f32x2 m = row_stat<7, FH>(g);
    const float rs = __builtin_amdgcn_rsqf(m[1]);
    *(LAS f32x2*)(smem + FS_OFF + row * 8) = (f32x2){rs, -m[0] * rs};
    s1 = 14 + NORM_S;
    static_assert(NORM_S + 20 > C::T - 6, "");
  } else if (PH == G2) {
    Flag* fl = flags(wl, F_GB, t.kh * 8 + t.bn);
    const int fi = lane & 15;
    u64 seen;
    st.go(smem, 1, 4);
    const Tile<CGA> ta;
    if (w == 0 && lane == 0) __hip_atomic_store(flags(wl, F_GB, ta.bn)[ta.bm].v, tag_of(smem), RLX_AGENT);
    st.go(smem, 4, 7);
    ld8_async(seen, fl, fi * sizeof(Flag));
    st.go(smem, 7, 9);
    LANDED1(16, seen);
    wait_flags(smem, fl + fi, seen);
    s1 = 9;
  }
  if (PH == K5) st.finish(smem, s1, [] {});
  else if (C::TN) {
    constexpr int E = C::TJ + C::TN / 2;
    static_assert(C::TN == 0 || (C::TJ >= 6 && E - GA_HK >= 5 && GA_HK >= 6), "");
    f32x4 g[8], c[4];
    if (l == 0) {
      u64 seen;
      st.go(smem, 3, 4);
      const int own = (F_X0 * 16 + t.bm) * 16 + t.bn + w * ((F_WD - F_X0) * 256 + (t.bn - t.bm) * 15);
      if (lane == 0) __hip_atomic_store(flags(wl, 0, 0)[own].v, tag_of(smem), RLX_AGENT);
      const unsigned fo = ((lane & 16 ? F_WD * 16 + t.bn : F_X0 * 16 + t.bm) * 16 + (lane & 15)) * sizeof(Flag);
      st.go(smem, 4, E - GA_HK);
      ld8_async(seen, p.ws + O_SF, fo);
      st.go(smem, E - GA_HK, E - GA_HK + 2);
      LANDED1(16, seen);
      wait_flags(smem, (Flag*)(p.ws + O_SF + fo), seen);
      s1 = E - GA_HK + 2;
#pragma unroll
      for (int i = 0; i < 4; ++i) ld16_async(c[i], p.ws + O_SD + (i * 2048 + t.bn * 128) * 16, row * 16);
    } else { st.go(smem, 1, 3); s1 = 3; }
#pragma unroll
    for (int i = 0; i < 8; ++i) ld16_async(g[i], wl + O_STX + (i * S_ + t.bm * 128) * 16, row * 16);
    st.go(smem, s1, E - 2);
    LANDED8(16, g);
    if (l == 0) {
      asm volatile("" : "+v"(c[0]), "+v"(c[1]), "+v"(c[2]), "+v"(c[3]));
      const f32x4 a = (c[0] + c[1]) + (c[2] + c[3]);
      *(LAS float*)(smem + CS1_OFF + row * 4) = (a[0] + a[1]) + (a[2] + a[3]);
    }
    g[0] = ((g[0] + g[1]) + (g[2] + g[3])) + ((g[4] + g[5]) + (g[6] + g[7]));
    asm volatile("" : "+v"(g[0]));
    st.go(smem, E - 2, E - 1);
    asm volatile("" : "+v"(g[0]));
    {
      const f32x2 m = row_stat<1, D_>(g);
      const float rs = __builtin_amdgcn_rsqf(m[1]);
      *(LAS f32x2*)(smem + ST_OFF + row * 16) = (f32x2){rs, -m[0] * rs};
    }
    st.go(smem, E - 1, C::T - 2, true);
    auto nx = wt_stream<NEXT>(p, w, lane);
    st.tail_over(smem, nx);
  } else {
    st.go(smem, s1, C::T - st.D + 1, true);
    auto sig = [&](int s) { if (PH == ZZ && s == NORM_S + 20 && w == 0 && lane == 0) __hip_atomic_store(flags(wl, F_Z, t.bn)[t.bm].v, tag_of(smem), RLX_AGENT); };
    if (PH != G2 || mid) { auto nx = wt_stream<NEXT>(p, w, lane); st.hand_over(smem, nx, sig); }
    else { auto nx = wt_stream<CK5>(p, w, lane); st.hand_over(smem, nx, sig); }
  }
  wpos = st.slot;
}

__device__ __forceinline__ void touch(LAS char* smem, const Params& p, int wid, int lane) {
  const int u = ((blockIdx.x >> 3) - 28 + (blockIdx.x & 7) * 4) * 8 + wid;
  BARRIER();
#pragma unroll 1
  for (int i = 0; i < 46; i += 2) {
    const char* g = (i < 32 ? p.wsp + (size_t)(u * 32 + i) * 1024 : p.wout + (size_t)(u * 14 + i - 32) * 1024) + lane * 16;
    __builtin_amdgcn_global_load_lds((const unsigned*)g, (LAS unsigned*)(smem + A_RING + wid * 1024), 16, 0, 0);
    __builtin_amdgcn_global_load_lds((const unsigned*)(g + 1024), (LAS unsigned*)(smem + A_RING + wid * 1024), 16, 0, 0);
    WAIT_V(8);
  }
  __builtin_amdgcn_s_waitcnt(0x0f70);
  BARRIER();
}

__global__ __launch_bounds__(512, 2) void mega_k(Params p) {
  extern __shared__ __attribute__((aligned(16))) char smem_[];
  LAS char* smem = (LAS char*)smem_;
  const int lane = threadIdx.x & 63, wid = __builtin_amdgcn_readfirstlane(threadIdx.x >> 6);
  const bool mid = (blockIdx.x >> 3) < 28;
  unsigned wpos = 0;
  if (wid < 4) {
    __builtin_amdgcn_s_waitcnt(0x0f70);
#pragma unroll 1
    for (int l = 0; l < 2; ++l) {
      u32x2 zk[4][4];
      compute_phase<CGA>(smem, p, l, wid, lane, zk, wpos);
      compute_phase<CG2>(smem, p, l, wid, lane, zk, wpos);
      if (mid) { compute_phase<CZZ>(smem, p, l, wid, lane, zk, wpos); compute_phase<CK4>(smem, p, l, wid, lane, zk, wpos); }
      else if (l == 0) touch(smem, p, wid, lane);
      compute_phase<CK5>(smem, p, l, wid, lane, zk, wpos);
    }
  } else if (wid < 6) {
#pragma unroll 1
    for (int l = 0; l < 2; ++l) {
      wt_phase<CGA, CG2>(smem, p, l, wid - 4, lane, wpos, mid);
      wt_phase<CG2, CZZ>(smem, p, l, wid - 4, lane, wpos, mid);
      if (mid) { wt_phase<CZZ, CK4>(smem, p, l, wid - 4, lane, wpos, mid); wt_phase<CK4, CK5>(smem, p, l, wid - 4, lane, wpos, mid); }
      else if (l == 0) touch(smem, p, wid, lane);
      wt_phase<CK5, CK5>(smem, p, l, wid - 4, lane, wpos, mid);
    }
  } else {
#pragma unroll 1
    for (int l = 0; l < 2; ++l) {
      u64 early = 0;
      act_phase<CGA>(smem, p, l, wid - 6, lane, early);
      act_phase<CG2>(smem, p, l, wid - 6, lane, early);
      if (mid) { act_phase<CZZ>(smem, p, l, wid - 6, lane, early); act_phase<CK4>(smem, p, l, wid - 6, lane, early); }
      else if (l == 0) touch(smem, p, wid, lane);
      act_phase<CK5>(smem, p, l, wid - 6, lane, early);
    }
  }
}

extern "C" void kernel_launch(void* const* d_in, const int* in_sizes, int n_in, void* d_out, int out_size, void* d_ws, size_t ws_size, hipStream_t stream) {
  Params p;
  p.x = (const char*)d_in[0];
  p.wsre = (const char*)d_in[1]; p.wsim = (const char*)d_in[2]; p.wdre = (const char*)d_in[3]; p.wdim = (const char*)d_in[4];
  p.win = (const char*)d_in[6]; p.wsp = (const char*)d_in[7]; p.wout = (const char*)d_in[9]; p.bsp = (const u16*)d_in[8];
  p.out = (char*)d_out; p.ws = (char*)d_ws;
  (void)hipFuncSetAttribute((const void*)mega_k, hipFuncAttributeMaxDynamicSharedMemorySize, LDS_BYTES);
  hipLaunchKernelGGL(mega_k, dim3(256), dim3(512), LDS_BYTES, stream, p);
}
```

```cpp
#include <hip/hip_runtime.h>
#include <hip/hip_bf16.h>
#include <stdint.h>

#define LAS __attribute__((address_space(3)))
typedef unsigned short u16;
typedef short s16x4 __attribute__((ext_vector_type(4)));
typedef short s16x8 __attribute__((ext_vector_type(8)));
typedef __bf16 bf16x8 __attribute__((ext_vector_type(8)));
typedef __bf16 bf16x2 __attribute__((ext_vector_type(2)));
typedef float f32x2 __attribute__((ext_vector_type(2)));
typedef float f32x4 __attribute__((ext_vector_type(4)));
typedef unsigned u32x2 __attribute__((ext_vector_type(2)));
typedef unsigned u32x4 __attribute__((ext_vector_type(4)));

constexpr int S_ = 2048, D_ = 1024, F_ = 3584, FH = 1792;
constexpr float LN_EPS = 1e-5f;

__device__ __forceinline__ unsigned pack_bf16(float lo, float hi) {
  f32x2 v = {lo, hi};
  return __builtin_bit_cast(unsigned, __builtin_convertvector(v, bf16x2));
}
__device__ __forceinline__ float bf_lo(unsigned u) { return __builtin_bit_cast(float, u << 16); }
__device__ __forceinline__ float bf_hi(unsigned u) { return __builtin_bit_cast(float, u & 0xffff0000u); }
__device__ __forceinline__ void swap16(unsigned& a, unsigned& b) { asm volatile("s_nop 1\n\tv_permlane16_swap_b32 %0, %1" : "+v"(a), "+v"(b)); }
__device__ __forceinline__ void swap32(unsigned& a, unsigned& b) { asm volatile("s_nop 1\n\tv_permlane32_swap_b32 %0, %1" : "+v"(a), "+v"(b)); }
__device__ __forceinline__ float sum32(float v) {
  unsigned a = __builtin_bit_cast(unsigned, v), b = a; swap32(a, b);
  return __builtin_bit_cast(float, a) + __builtin_bit_cast(float, b);
}
__device__ __forceinline__ float sum_groups(float v) {
  unsigned a = __builtin_bit_cast(unsigned, v), b = a; swap16(a, b);
  return sum32(__builtin_bit_cast(float, a) + __builtin_bit_cast(float, b));
}
__device__ __forceinline__ u32x4 pair_rows(u32x2 a, u32x2 b) {
  unsigned a0 = a[0], a1 = a[1], b0 = b[0], b1 = b[1]; swap16(a0, b0); swap16(a1, b1);
  return (u32x4){a0, a1, b0, b1};
}
__device__ __forceinline__ void unpair_rows(u32x4 v, u32x2& a, u32x2& b) {
  unsigned a0 = v[0], a1 = v[1], b0 = v[2], b1 = v[3]; swap16(a0, b0); swap16(a1, b1);
  a = (u32x2){a0, a1}; b = (u32x2){b0, b1};
}
__device__ __forceinline__ u32x4 pair_cols(u32x2 a, u32x2 b) {
  unsigned a0 = a[0], a1 = a[1], b0 = b[0], b1 = b[1]; swap32(a0, b0); swap32(a1, b1); swap16(a0, b0); swap16(a1, b1);
  return (u32x4){a0, a1, b0, b1};
}
__device__ __forceinline__ void unpair_cols(u32x4 v, u32x2& a, u32x2& b) {
  unsigned a0 = v[0], a1 = v[1], b0 = v[2], b1 = v[3]; swap16(a0, b0); swap16(a1, b1); swap32(a0, b0); swap32(a1, b1);
  a = (u32x2){a0, a1}; b = (u32x2){b0, b1};
}
__device__ __forceinline__ u32x2 pack4(f32x4 v) { return (u32x2){pack_bf16(v[0], v[1]), pack_bf16(v[2], v[3])}; }
__device__ __forceinline__ f32x4 unpack4(u32x2 y) { return (f32x4){bf_lo(y[0]), bf_hi(y[0]), bf_lo(y[1]), bf_hi(y[1])}; }
constexpr float GC1 = -2.0f * 0.7978845608028654f * 1.4426950408889634f, GC2 = GC1 * 0.044715f;
__device__ __forceinline__ f32x4 gelu_tanh4(f32x4 a, f32x4 k) {
  const f32x4 t = a * (a * a * k[2] + k[1]);
  f32x4 e;
#pragma unroll
  for (int i = 0; i < 4; ++i) e[i] = __builtin_amdgcn_exp2f(t[i]);
  e = e * k[3] + k[3];
#pragma unroll
  for (int i = 0; i < 4; ++i) e[i] = __builtin_amdgcn_rcpf(e[i]);
  return a * e;
}
#define STAUX 16
typedef __amdgpu_buffer_rsrc_t rsrc_t;
__device__ __forceinline__ rsrc_t mk_rsrc(const void* p, unsigned bytes) { return __builtin_amdgcn_make_buffer_rsrc((void*)p, 0, bytes, 0x00020000); }
__device__ __forceinline__ void st16(rsrc_t r, unsigned off, u32x4 v) { __builtin_amdgcn_raw_buffer_store_b128(v, r, off, 0, STAUX); }
template <int AUX> __device__ __forceinline__ u32x4 ld16(rsrc_t r, unsigned off) { return __builtin_amdgcn_raw_buffer_load_b128(r, off, 0, AUX); }
#define WAIT_V(n) asm volatile("s_waitcnt vmcnt(%0)" ::"n"(n) : "memory")
#define WAIT_L(n) __builtin_amdgcn_s_waitcnt(0xc07f | ((n) << 8))
#define WAIT_VL(n) asm volatile("s_waitcnt vmcnt(%0) lgkmcnt(0)" ::"n"(n) : "memory")
#define WAIT_L0() asm volatile("s_waitcnt lgkmcnt(0)" ::: "memory")
#define BARRIER() do { asm volatile("" ::: "memory"); __builtin_amdgcn_s_barrier(); asm volatile("" ::: "memory"); } while (0)
#define PIN_S(x) asm volatile("" : "+s"(x))

#define RLX_AGENT __ATOMIC_RELAXED, __HIP_MEMORY_SCOPE_AGENT
#define PF_THR 8
#define POLL_SLEEP 1
#define NORM_S 8
#define SIGA_S 4
#define LAT_S 1
#define ZLAG 1
constexpr unsigned SPIN_MAX = 1u << 18;
typedef unsigned long long u64;
template <int CTRL> __device__ __forceinline__ float dpp_add(float v) {
  return v + __builtin_bit_cast(float, __builtin_amdgcn_update_dpp(0, __builtin_bit_cast(int, v), CTRL, 0xf, 0xf, true));
}
__device__ __forceinline__ float sum8(float v) { return dpp_add<0x141>(dpp_add<0x4e>(dpp_add<0xb1>(v))); }

constexpr size_t MB = 1 << 20, LSTRIDE = 40 * MB;
constexpr size_t O_X = 0, O_X1 = 4 * MB, O_AB = 8 * MB, O_Z2 = 16 * MB, O_H = 24 * MB;
constexpr size_t O_XB = LSTRIDE + 32 * MB;
constexpr size_t O_STX = 36 * MB, O_ST1 = O_STX + 256 * 1024;
constexpr size_t O_XCH = O_STX + 512 * 1024;
constexpr size_t O_SF = O_STX + 768 * 1024;
constexpr size_t O_SD = 2 * LSTRIDE, O_SI = O_SD + 128 * 1024;
constexpr size_t O_EP = O_SD + 512 * 1024;
enum { F_X0, F_WD, F_WI, F_GA, F_GB, F_G2, F_S1, F_S1B, F_Z, F_K4, F_KB, F_RD, F_K5 };
constexpr unsigned W_RING = 96 * 1024, A_RING = W_RING, A_BYTES = 48 * 1024, CS1_OFF = 144 * 1024, CS3_OFF = CS1_OFF + 512, ST_OFF = CS3_OFF + 1024, PS_OFF = ST_OFF + 2048,
                   FS_OFF = PS_OFF + 2048, BS_OFF = FS_OFF + 1024, CNT_OFF = BS_OFF + 512, EP_OFF = CNT_OFF + 128;
constexpr unsigned LDS_BYTES = EP_OFF + 256;

enum { GA = 0, G2 = 1, ZZ = 2, K4 = 3, K5 = 4 };

struct Params {
  const char *x, *wsre, *wsim, *wdre, *wdim, *win, *wsp, *wout;
  const u16* bsp;
  char* out;
  char* ws;
};

template <int PH_, int BM_, int BN_, int WM_, int WN_, int AM_, int BMODE_, int T_, int PARTS_, int TN_, int XN_, int PA_, int PB_, int WSIDE_>
struct Cfg {
  static constexpr int PH = PH_, BM = BM_, BN = BN_, WM = WM_, WN = WN_, AM = AM_, BMODE = BMODE_, T = T_, PARTS = PARTS_;
  static constexpr int TN = TN_, TJ = T_ - TN_, XM = 4, XN = XN_, PA = PA_, PB = PB_, WSIDE = WSIDE_;
  static constexpr int MT = BM / WM / 16, NT = BN / WN / 16;
  static constexpr bool SPLIT = PH_ == G2 || PH_ == K5;
};
#define GA_TN 6
#define G2_TN 6
#define K4_TN 6
#define K5_TN 6
#define GA_FS 2
#define GA_HK 6
using CGA = Cfg<GA, 128, 128, 2, 2, 1, 0, 16, 1, GA_TN, 8, D_ / 32, 2048,    1>;
using CG2 = Cfg<G2, 128, 128, 2, 2, 0, 2, 32, 1, G2_TN, 8, 4096,    D_ / 16, 0>;
using CZZ = Cfg<ZZ, 128, 128, 2, 2, 1, 1, 32, 2, 0,     7, D_ / 32, F_ * 2,  1>;
using CK4 = Cfg<K4, 128, 128, 2, 2, 0, 2, 32, 1, K4_TN, 7, 4096,    FH / 16, 0>;
using CK5 = Cfg<K5, 128, 128, 2, 2, 1, 1, 14, 1, K5_TN, 8, FH / 32, D_ * 2,  1>;
template <int V> struct IC { static constexpr int v = V; };
struct PairRing { static constexpr unsigned SLOT = 32768, D = 3; };

template <class C> struct Tile {
  int bm, bn, kh, cls;
  __device__ __forceinline__ Tile() {
    int bid = blockIdx.x; PIN_S(bid);
    const int xcd = bid & 7, idx = bid >> 3, lm = idx / C::XN, ln = idx % C::XN;
    bm = (xcd >> 1) * C::XM + lm; bn = C::SPLIT ? ln : (xcd & 1) * C::XN + ln; kh = C::SPLIT ? xcd & 1 : 0; cls = (lm + ln) & 3;
  }
};
template <class C, int SIDE> struct Ring {
  static constexpr bool ISW = C::WSIDE == SIDE;
  static constexpr unsigned SLOT = (SIDE ? C::BN : C::BM) * 128, BASE = ISW ? 0 : A_RING, D = (ISW ? W_RING : A_BYTES) / SLOT;
};

#define FW 32
struct Flag { u64 v[FW]; };
__device__ __forceinline__ Flag* flags(char* wl, int id, int panel) { return (Flag*)(wl + O_SF) + (id * 16 + panel) * 16; }
__device__ __forceinline__ u64 tag_of(LAS char* smem) { return *(const volatile LAS u64*)(smem + EP_OFF); }
template <int N, bool LATE> __device__ __forceinline__ void signal_done(LAS char* smem, int slot, Flag* flag, Flag* late) {
  const u64 tag = tag_of(smem);
  unsigned old;
#define COUNT_IN(off) "s_mov_b64 exec, 1\n\tds_add_rtn_u32 %0, %1, %2 offset:" #off "\n\ts_waitcnt lgkmcnt(0)\n\tv_cmp_eq_u32 vcc, 3, %0\n\ts_and_b64 exec, exec, vcc\n\t"
#define FIRST_FLAG "s_waitcnt vmcnt(%7)\n\t" COUNT_IN(0) "s_cbranch_scc0 .Lsig_a%=\n\tglobal_store_dwordx2 %3, %4, %5 sc1\n\ts_waitcnt vmcnt(1)\n\ts_branch .Lsig_b%=\n" \
                   ".Lsig_a%=:\n\ts_waitcnt vmcnt(0)\n.Lsig_b%=:\n\t" COUNT_IN(4)
  if (LATE)
    asm volatile(FIRST_FLAG "s_cbranch_scc0 .Lsig_c%=\n\tglobal_store_dwordx2 %3, %4, %6 sc1\n.Lsig_c%=:\n\ts_mov_b64 exec, -1"
                 : "=&v"(old) : "v"((unsigned)(size_t)(smem + CNT_OFF) + slot * 4), "v"(1), "v"(0), "v"(tag), "s"(flag), "s"(late), "n"(N) : "vcc", "scc", "memory");
  else
    asm volatile(FIRST_FLAG "s_mov_b64 exec, -1"
                 : "=&v"(old) : "v"((unsigned)(size_t)(smem + CNT_OFF) + slot * 4), "v"(1), "v"(0), "v"(tag), "s"(flag), "n"(0), "n"(N) : "vcc", "scc", "memory");
}
__device__ __forceinline__ u64 peek(u64* f) { return __hip_atomic_load(f, RLX_AGENT); }
__device__ __forceinline__ void ld8_async(u64& d, const void* sbase, unsigned voff) { asm volatile("s_nop 4\n\tglobal_load_dwordx2 %0, %1, %2 sc1" : "=v"(d) : "v"(voff), "s"(sbase) : "memory"); }
#define LCNT(n) (n)
#define LANDED1(n, a) asm volatile("s_waitcnt vmcnt(%1)" : "+v"(a) : "n"(LCNT(n)) : "memory")
__device__ __forceinline__ void ld16_async(f32x4& d, const void* sbase, unsigned voff) { asm volatile("s_nop 4\n\tglobal_load_dwordx4 %0, %1, %2 sc1" : "=v"(d) : "v"(voff), "s"(sbase) : "memory"); }
#define LANDED8(n, g) asm volatile("s_waitcnt vmcnt(%8)" : "+v"(g[0]), "+v"(g[1]), "+v"(g[2]), "+v"(g[3]), "+v"(g[4]), "+v"(g[5]), "+v"(g[6]), "+v"(g[7]) : "n"(LCNT(n)) : "memory")
__device__ __forceinline__ u64* stat_slot(char* base, int g, int row) { return (u64*)base + ((g >> 1) * S_ + row) * 2 + (g & 1); }
__device__ __forceinline__ void wait_flags(LAS char* smem, Flag* f, u64 first) {
  const u64 tag = tag_of(smem);
  if (!__all(first == tag)) {
#pragma unroll 1
    for (unsigned spin = 0; spin < SPIN_MAX; ++spin) {
      if (__all(peek(f->v) == tag)) break;
      if (POLL_SLEEP) __builtin_amdgcn_s_sleep(POLL_SLEEP);
    }
  }
  asm volatile("" ::: "memory");
}

template <int GAP> __device__ __forceinline__ void poll_flags(u64 tag, const void* base, unsigned off, unsigned mask) {
  unsigned left = SPIN_MAX;
  u64 q;
  asm volatile("s_mov_b32 exec_lo, %5\n\ts_mov_b32 exec_hi, 0\n\ts_nop 4\n"
               ".Lpoll_next%=:\n\tglobal_load_dwordx2 %0, %2, %3 sc1\n\ts_waitcnt vmcnt(0)\n\tv_cmp_ne_u64 vcc, %4, %0\n\ts_and_b64 exec, exec, vcc\n\t"
               "s_cbranch_scc0 .Lpoll_done%=\n\ts_sleep %6\n\ts_sub_u32 %1, %1, 1\n\ts_cbranch_scc0 .Lpoll_next%=\n"
               ".Lpoll_done%=:\n\ts_mov_b64 exec, -1"
               : "=&v"(q), "+s"(left) : "v"(off), "s"(base), "v"(tag), "s"(__builtin_amdgcn_readfirstlane(mask)), "n"(GAP) : "vcc", "scc", "memory");
}
__device__ __forceinline__ void wait_count(LAS char* smem, int slot, unsigned n) {
#pragma unroll 1
  for (unsigned spin = 0; spin < 64 * SPIN_MAX; ++spin) {
    if (*((const volatile LAS unsigned*)(smem + CNT_OFF) + slot) >= n) break;
    __builtin_amdgcn_s_sleep(1);
  }
  asm volatile("" ::: "memory");
}

template <class C, int SIDE> struct Stream {
  using R = Ring<C, SIDE>;
  static constexpr int NP = 8, T = C::T, TJ = C::TJ, D = R::D, MODE = SIDE ? C::BMODE : (C::AM ? 3 : 0), PITCH = SIDE ? C::PB : C::PA;
  static constexpr unsigned KS = MODE == 0 ? 128 : (MODE == 1 ? 64 * PITCH : (MODE == 2 ? 2 * PITCH * 1024 : 2048));
  static constexpr unsigned HALF = MODE == 0 ? 64 * PITCH : (MODE == 1 ? 128 : (MODE == 2 ? 2048 : 4 * PITCH * 1024));
  static constexpr unsigned P4 = MODE == 0 ? 32 * PITCH : (MODE == 1 ? (C::PH == K5 ? 64 : 32) * PITCH : (MODE == 2 ? PITCH * 1024 : 2 * PITCH * 1024));
  static constexpr bool TWO = C::TN && !R::ISW;
  static_assert(R::SLOT == 16384 && (C::TN == 0 || (C::TN == 6 && TJ % 2 == 0)), "");
#define AH_GA 235
#define AH_G2 234
#define AH_K5 234
#define AH_ZZ 234
  static constexpr int AHEAD = C::PH == GA ? AH_GA : (C::PH == G2 ? AH_G2 : (C::PH == ZZ ? AH_ZZ : (C::PH == K5 ? AH_K5 : 6)));
  rsrc_t rs; unsigned base, vo[4], ks, slot, g, first; int cls, k0, lat, w, n;
  __device__ __forceinline__ Stream(const char* src, unsigned bytes, int l, int w_, int lane, unsigned slot0, int n0) {
    const Tile<C> t;
    const bool raw = MODE == 3 && C::PH == GA && l == 0;
    rs = mk_rsrc(src, bytes); w = w_; cls = t.cls; k0 = t.kh * T; ks = raw ? 128 : KS;
    lat = l ? LAT_S : (t.bm + t.bn) & 15;
    first = C::SPLIT && TWO ? (1 - t.kh) * HALF : 0;
    const int b = SIDE ? t.bn : t.bm;
    base = MODE == 0 ? (C::PH == GA ? b & 7 : b) * 128 * PITCH : (MODE == 1 ? b * 256 : (MODE == 2 ? b * 8192 + t.kh * T * KS : b * 8 * PITCH * 1024));
#pragma unroll
    for (int i = 0; i < 4; ++i) {
      const unsigned row = i * 8 + (lane >> 3), sw = ((lane & 7) ^ (((i & 1) << 2) | (lane >> 4))) << 4;
      if (MODE == 0) vo[i] = row * PITCH + sw;
      else if (MODE == 1) vo[i] = row * PITCH + (((lane & 7) ^ ((((lane >> 4) & 1) << 2) | ((i & 1) << 1))) << 4);
      else if (MODE == 2) vo[i] = (i >> 1) * 4096 + (i & 1) * 1024 + lane * 16;
      else vo[i] = raw ? row * (PITCH * 64) + sw : (i >> 1) * PITCH * 1024 + (i & 1) * (C::PH == K5 ? 2048 : 1024) + lane * 16;
      vo[i] -= i * 1024;
    }
    slot = slot0 + n0 * R::SLOT; if (slot >= D * R::SLOT) slot -= D * R::SLOT;
    n = n0; g = source<true>(n);
  }
  __device__ __forceinline__ int chunk(int ch) const {
    constexpr int L = T / C::PARTS;
    if (C::PARTS == 2) ch &= 15;
#define LEADMASK 7
    if ((LEADMASK >> C::PH & 1) && (ch & 3) == cls && ch >= 4) { ch += 4; if (ch >= L) ch = cls + 4; }
    if (C::PH == GA) ch = ch == LAT_S ? lat : (ch == lat ? LAT_S : ch);
    if (C::PH == G2) ch = (ch & 15) * 2 + (ch >> 4);
    if (C::PH == K5) ch = k0 + (ch < 7 ? 2 * ch : 2 * ch - 13);
    return ch;
  }
  __device__ __forceinline__ unsigned writers(int s) const { return C::PH == GA || C::PH == ZZ ? 3u << (chunk(s) & 14) : 1u << (chunk(s) >> 1); }
  template <bool JOINT> __device__ __forceinline__ unsigned source(int s) const {
    const bool tail = TWO && !JOINT && s >= TJ, second = tail ? s >= TJ + 3 : w;
    const int ch = chunk(tail ? 2 * s - TJ + w - (second ? 6 : 0) : s);
    unsigned a = base + (C::PH == K5 ? (2 * ch - (ch & 1)) * (KS / 2) : ch * ks) + (second ? HALF - first : first);
    if (C::PH == ZZ && SIDE == 1) a += (s & 16 ? 0 : FH * 2);
    return a;
  }
  __device__ __forceinline__ void put(LAS char* smem, bool joint = false) {
    {
#define PIECE(h, i) __builtin_amdgcn_raw_ptr_buffer_load_lds(rs, (LAS void*)(smem + R::BASE + slot + w * 8192 + h * 4096), 16, vo[i], g + h * P4, i * 1024, R::ISW ? 0 : 16)
#ifdef __HIP_DEVICE_COMPILE__
      PIECE(0, 0); PIECE(0, 1); PIECE(0, 2); PIECE(0, 3); PIECE(1, 0); PIECE(1, 1); PIECE(1, 2); PIECE(1, 3);
#endif
    }
    slot += R::SLOT; if (slot == D * R::SLOT) slot = 0;
    ++n; g = joint ? source<true>(n) : source<false>(n);
    PIN_S(g);
  }
  __device__ __forceinline__ void fill(LAS char* smem, int n1) {
#pragma unroll 1
    while (n < n1) put(smem, true);
  }
  template <int X, class F> __device__ __forceinline__ void step(LAS char* smem, F pre, bool joint) {
    BARRIER();
    pre();
    put(smem, joint);
    WAIT_V(NP + X);
  }
  template <class F> __device__ __forceinline__ void finish(LAS char* smem, F last) {
#pragma unroll 1
    while (n < TJ - (C::TN > 0)) step<0>(smem, [] {}, true);
    if (C::TN) {
#pragma unroll 1
      while (n < T) step<0>(smem, [] {}, false);
    }
    BARRIER();
    WAIT_V(0);
    BARRIER();
    last();
    BARRIER();
  }
  template <int X, int LAG> __device__ __forceinline__ void skip() const { WAIT_VL((4 - LAG) * NP + X); BARRIER(); }
  template <int X = 0, int LAG = 0> __device__ __forceinline__ void go(LAS char* smem, int b) {
#pragma unroll 1
    while (n < b + 5 - LAG) { put(smem); skip<X, LAG>(); }
  }
  __device__ __forceinline__ void close(LAS char* smem) {
    go(smem, TJ - 1);
#pragma unroll 1
    while (n < T) { put(smem); if (n < T) WAIT_VL(3 * NP); else WAIT_VL(2 * NP); BARRIER(); }
  }
  __device__ __forceinline__ void open(LAS char* smem) {
#pragma unroll 1
    for (int k = 8; k >= 0; k -= 4) { BARRIER(); fill(smem, (AHEAD / 100 * 256 + AHEAD / 10 % 10 * 16 + AHEAD % 10) >> k & 15); }
  }
  __device__ __forceinline__ void resume(LAS char* smem) {
    WAIT_V((AHEAD % 10 - 1) * NP);
    BARRIER();
    fill(smem, 6);
    skip<0, 0>();
  }
};

#define TS(i)
#define TSR(i)
#define TS_DECL
#define TS_OUT(ph)

__device__ __forceinline__ unsigned tr_lane(int r, int j) { return ((r >> 2) & 1) * 512 + (j & 1) * 256 + (r >> 3) * 128 + (r & 3) * 32 + (j >> 1) * 16; }
__device__ __forceinline__ void unpack8(u32x4 v, float (&e)[8]) {
#pragma unroll
  for (int i = 0; i < 4; ++i) { e[2 * i] = bf_lo(v[i]); e[2 * i + 1] = bf_hi(v[i]); }
}

__device__ __forceinline__ float dot2(float a, float b, float c) { return __builtin_amdgcn_fdot2_f32_bf16(__builtin_bit_cast(bf16x2, a), __builtin_bit_cast(bf16x2, b), c, false); }
#define FIRST_SLEEP 4
__device__ __forceinline__ void first_touch(const Params& p, int wid, int lane, u64 (&tch)[2]) {
  const Tile<CG2> u; const Tile<CZZ> z;
#define TCH0_N 4
#define TCH0_H 1
#define TCH1_N 4
  ld8_async(tch[0], (u.kh ? p.wsim : p.wsre) + (u.bm * 128 + u.bn * 16 + wid * 4) * (S_ * 2), (lane >> 4) * (S_ * 2) + ((lane >> 1) & (TCH0_N - 1)) * 256 + (lane & TCH0_H) * 64);
  ld8_async(tch[1], p.win + ((z.bm & 15) * 4 + wid) * TCH1_N * (F_ * 2) + (FH + z.bn * 128) * 2, (lane >> 2) % TCH1_N * (F_ * 2) + (lane & 3) * 64);
}
__device__ __forceinline__ void first_tag(LAS char* smem, const Params& p, int wid, int lane) {
  if (wid == 2 && lane < 32) *(LAS unsigned*)(smem + CNT_OFF + lane * 4) = 0u;
  if (wid == 3) {
    u64 e0;
    asm volatile("s_load_dwordx2 %0, %1, 0x0\n\ts_waitcnt lgkmcnt(0)" : "=s"(e0) : "s"(p.ws + O_EP) : "memory");
    u64 e = (e0 ^ (e0 >> 29)) * 0x9E3779B97F4A7C15ull + 0x632BE59BD9B4E019ull;
    if (e == e0) e = ~e0;
    if (lane == 0) *(LAS u64*)(smem + EP_OFF) = e;
  }
  WAIT_L0();
}
__device__ __forceinline__ float join4(float a, float b, float c, float d) {
  unsigned x = __builtin_bit_cast(unsigned, a), y = __builtin_bit_cast(unsigned, c), z = __builtin_bit_cast(unsigned, b), w = __builtin_bit_cast(unsigned, d);
  swap32(x, y); swap32(z, w);
  x = __builtin_bit_cast(unsigned, __builtin_bit_cast(float, x) + __builtin_bit_cast(float, y));
  z = __builtin_bit_cast(unsigned, __builtin_bit_cast(float, z) + __builtin_bit_cast(float, w));
  swap16(x, z);
  return __builtin_bit_cast(float, x) + __builtin_bit_cast(float, z);
}
__device__ __forceinline__ float join2(float a, float b) {
  unsigned x = __builtin_bit_cast(unsigned, a), y = __builtin_bit_cast(unsigned, b); swap32(x, y);
  return __builtin_bit_cast(float, x) + __builtin_bit_cast(float, y);
}
__device__ __forceinline__ void add_stat(u32x4 y, float& s, float& q) {
#pragma unroll
  for (int k = 0; k < 4; ++k) { const unsigned u = y[k]; const float v = __builtin_bit_cast(float, u); s = dot2(v, __builtin_bit_cast(float, 0x3f803f80u), s); q = dot2(v, v, q); }
}
__device__ __forceinline__ float join32(float a, float b) {
  unsigned x = __builtin_bit_cast(unsigned, a), y = __builtin_bit_cast(unsigned, b); swap32(x, y);
  x = __builtin_bit_cast(unsigned, __builtin_bit_cast(float, x) + __builtin_bit_cast(float, y)); y = x; swap16(x, y);
  return __builtin_bit_cast(float, x) + __builtin_bit_cast(float, y);
}
__device__ __forceinline__ void first_sums(const Params& p, int wid, int lane, const s16x8 (&a0)[4], const s16x8 (&a1)[4], const s16x8 (&b0)[8], const s16x8 (&b1)[8]) {
  const Tile<CGA> t;
  const int wm = wid >> 1, wn = wid & 1, g = (t.bm + t.bn) & 15;
  const float ones = __builtin_bit_cast(float, 0x3f803f80u);
  float a[2] = {}, q[2] = {}, b[2] = {};
#pragma unroll
  for (int i = 0; i < 4; ++i) {
    const f32x4 x = __builtin_bit_cast(f32x4, wn ? (i & 2 ? a1 : a0)[2 + (i & 1)] : (i & 2 ? a1 : a0)[i & 1]);
    const f32x4 w = __builtin_bit_cast(f32x4, wm ? (i & 2 ? b1 : b0)[2 + (i & 1)] : (i & 2 ? b1 : b0)[i & 1]);
#pragma unroll
    for (int k = 0; k < 4; ++k) { a[i & 1] = dot2(x[k], ones, a[i & 1]); q[i & 1] = dot2(x[k], x[k], q[i & 1]); b[i & 1] = dot2(w[k], ones, b[i & 1]); }
  }
  const f32x2 st = {join32(a[0], a[1]), join32(q[0], q[1])};
  const float sw = join32(b[0], b[1]);
  if (!(lane & 16)) {
    const int row = wn * 64 + wm * 32 + (lane >> 5) * 16 + (lane & 15);
    __hip_atomic_store(stat_slot(p.ws + O_STX, g, t.bm * 128 + row), __builtin_bit_cast(u64, st), RLX_AGENT);
    __hip_atomic_store((unsigned*)(p.ws + O_SD) + ((g >> 2) * 2048 + t.bn * 128 + row) * 4 + (g & 3), __builtin_bit_cast(unsigned, sw), RLX_AGENT);
  }
  if (wid < 2) st16(mk_rsrc(p.ws + wid * LSTRIDE + O_XCH, 256 * 1024), blockIdx.x * 1024 + lane * 16, (u32x4){~0u, ~0u, ~0u, ~0u});
}
__device__ __forceinline__ void column_sums(u64* dst, int lane, f32x4 (&v)[4], f32x4 (&w)[4]) {
  asm volatile("s_waitcnt vmcnt(0)" : "+v"(v[0]), "+v"(v[1]), "+v"(v[2]), "+v"(v[3]), "+v"(w[0]), "+v"(w[1]), "+v"(w[2]), "+v"(w[3]) :: "memory");
  float s[8], e[8];
  unpack8(__builtin_bit_cast(u32x4, v[0]), s);
#pragma unroll
  for (int i = 1; i < 8; ++i) {
    unpack8(__builtin_bit_cast(u32x4, i < 4 ? v[i & 3] : w[i & 3]), e);
#pragma unroll
    for (int k = 0; k < 8; ++k) s[k] += e[k];
  }
#pragma unroll
  for (int k = 0; k < 4; ++k) { unsigned a = __builtin_bit_cast(unsigned, s[k]), b = __builtin_bit_cast(unsigned, s[k + 4]); swap32(a, b); s[k] = __builtin_bit_cast(float, a) + __builtin_bit_cast(float, b); }
#pragma unroll
  for (int k = 0; k < 2; ++k) { unsigned a = __builtin_bit_cast(unsigned, s[k]), b = __builtin_bit_cast(unsigned, s[k + 2]); swap16(a, b); s[k] = dpp_add<0x128>(__builtin_bit_cast(float, a) + __builtin_bit_cast(float, b)); }
  if (!(lane & 8)) __hip_atomic_store(dst + (lane & 7) * 8 + (lane >> 4) * 2, __builtin_bit_cast(u64, (f32x2){s[0], s[1]}), RLX_AGENT);
}

template <class C>
__device__ __forceinline__ void compute_phase(LAS char* smem, const Params& p, int l, int wid, int lane, u32x2 (&zk)[4][4], u32x4 (&rv)[4], unsigned& wpos) {
  constexpr int BN = C::BN, MT = C::MT, NT = C::NT, PH = C::PH, TN = C::TN, TP = C::T / C::PARTS - TN;
  using RX = Ring<C, 0>; using RY = Ring<C, 1>;
  TS_DECL; TSR(4); TS(0);
#define TR(i)
  asm volatile("" : "+v"(lane));
  const Tile<C> t;
  char* wl = p.ws + l * LSTRIDE;
  constexpr bool ROWS = TN && C::WSIDE, COLS = TN && !C::WSIDE;
  const int wm = wid / C::WN, wn = wid % C::WN, n0 = t.bn * BN + wn * (NT * 16) + (COLS ? t.kh * 32 : 0);
  const int ml = ROWS ? wm * 32 + t.kh * 64 : wm * 64, m0 = t.bm * 128 + ml;
  constexpr int OW = PH == K4 ? FH : D_;
  f32x4 acc[MT][NT];
  u64 tch[2] = {};
  u32x4 pv[4];
  f32x4 zf[MT][4];
  float ini[MT];
  constexpr int NA = C::AM == 0 || PH == GA ? 2 : 1, NB = C::BMODE == 0 ? 2 : (C::BMODE == 1 ? 4 : 1);
  unsigned ad[NA + NB];
  const unsigned xs = C::WSIDE ? 0 : wpos, ys = C::WSIDE ? wpos : 0;
  {
    const int r = lane & 15, j = lane >> 4;
    constexpr int MW = ROWS ? 2 : MT;
    if (C::AM == 0 || (PH == GA && l == 0)) { ad[0] = RX::BASE + xs + (wm * MW * 16 + r) * 128 + ((j ^ (r >> 1)) << 4); ad[NA - 1] = ad[0] ^ 64; }
    else { ad[0] = RX::BASE + xs + wm * MW * 2048 + lane * 16; if (NA == 2) ad[1] = ad[0] + 1024; }
    if (C::BMODE == 0) { ad[NA] = RY::BASE + ys + (wn * NT * 16 + r) * 128 + ((j ^ (r >> 1)) << 4); ad[NA + NB - 1] = ad[NA] ^ 64; }
    else if (C::BMODE == 1) {
      const int q = (lane >> 2) & 3, pp = lane & 3, f = ((q >> 1) << 1) | (j & 1);
#pragma unroll
      for (int c = 0; c < NB; ++c) ad[NA + c] = RY::BASE + ys + wn * 8192 + (j * 8 + q) * 128 + ((c ^ f) << 5) + pp * 8;
    } else ad[NA] = RY::BASE + ys + wn * 2048 + lane * 8;
#pragma unroll
    for (int u = 0; u < NA + NB; ++u) ad[u] += (unsigned)(size_t)smem;
#pragma unroll
    for (int mt = 0; mt < MT; ++mt) ini[mt] = PH == K4 ? *(const LAS float*)(smem + BS_OFF + (ml + mt * 16 + r) * 4) : 0.f;
  }
  unsigned cx = RX::D - 1 - xs / RX::SLOT, cy = RY::D - 1 - ys / RY::SLOT, dx, dy;
  wpos += (C::T + TN) * Ring<C, C::WSIDE>::SLOT % W_RING; if (wpos >= W_RING) wpos -= W_RING;
#define RING_NEXT(c, d, R) asm volatile("s_sub_u32 %0, %0, 1\n\ts_cselect_b32 %1, %2, %3\n\ts_cselect_b32 %0, %4, %0" : "+s"(c), "=&s"(d) : "n"(-(int)((R::D - 1) * R::SLOT)), "s"(R::SLOT), "n"(R::D - 1) : "scc")
  static_assert(NT == 4, "");
  constexpr int BR = C::BMODE == 0 ? 1 : 2, NM = MT * NT, MF = COLS ? 2 * MT : MT, NF = ROWS ? 2 * NT : NT;
  auto rd = [&](s16x8 (&fa)[MF], s16x8 (&fb)[NF], int kk, int i, bool two) {
    const int ma = two ? MF / 2 : MT, nb = two ? NF / 2 : NT, q = i / (ma + nb * BR);
    i -= q * (ma + nb * BR);
    if (i >= BR && i < BR + ma) {
      const int mt = i - BR + q * ma;
      fa[mt] = *(const LAS s16x8*)((LAS char*)(size_t)ad[NA == 2 ? kk : 0] + (NA == 2 ? 0 : kk * 1024) + (ROWS ? (mt >> 1) * 8192 + (mt & 1) * 2048 : mt / MT * RX::SLOT + mt % MT * 2048));
      return;
    }
    const int ib = i < BR ? i : i - ma, n = q * nb + ib / BR, nt = n % NT, h = ib % BR;
    s16x8& f = fb[n];
    if (C::BMODE == 0) { f = *(const LAS s16x8*)((LAS char*)(size_t)ad[NA + kk] + nt * 2048 + n / NT * RY::SLOT); return; }
    const s16x4 v = __builtin_amdgcn_ds_read_tr16_b64_v4i16((LAS s16x4*)((LAS char*)(size_t)ad[NA + nt % NB] + kk * 4096 + (C::BMODE == 1 ? 0 : (nt >> 1) * 8192 + (nt & 1) * 1024) + h * 512 + n / NT * RY::SLOT));
    if (h == 0) f = __builtin_shufflevector(v, v, 0, 1, 2, 3, -1, -1, -1, -1);
    else { const s16x4 lo = __builtin_shufflevector(f, f, 0, 1, 2, 3); f = __builtin_shufflevector(lo, v, 0, 1, 2, 3, 4, 5, 6, 7); }
  };
  auto half = [&](auto cm, auto nm, const s16x8 (&ca)[MF], const s16x8 (&cb)[NF], s16x8 (&na)[MF], s16x8 (&nb)[NF], int kk, int hs, auto extra) {

    constexpr int NR = nm.v == 2 ? MF + NF * BR : MT + NT * BR;
    constexpr int GAPS = NR <= NM - 2 ? NR : NM - 2, FREE = NM - GAPS;
#pragma unroll
    for (int i = 0; i < NM; ++i) {
      constexpr int MC = cm.v == 2 ? MF / 2 : MT, NC = cm.v == 2 ? NF / 2 : NT;
      const int ib = i / MC, m = (ib & 1) ? MC - 1 - i % MC : i % MC, ia = m + ib / NC * MC;
      f32x4& d = acc[m + (ROWS && hs > 0 ? 2 : 0)][ib % NC + (COLS && hs > 0 ? 2 : 0)];
      const bool zero = hs < 0 && (!C::SPLIT || (ROWS ? m : ib) < 2);
      if (MT == 4 && cm.v == 1 && kk == 1 && i == 0) WAIT_L(NR - BR - MT);
      if (MT == 4 && cm.v == 1 && kk == 1 && i == MT) WAIT_L(MT);
      if (cm.v == 2 && kk == 1 && i == 0) WAIT_L(NR / 2);
      if (cm.v == 2 && kk == 1 && i == NM / 2) WAIT_L((NM / 2 * NR + GAPS - 1) / GAPS);
      if (zero) asm volatile("v_mfma_f32_16x16x32_bf16 %0, %1, %2, 0" : "=&v"(d) : "v"(cb[ib]), "v"(ca[ia]));
      else asm volatile("v_mfma_f32_16x16x32_bf16 %0, %1, %2, %0" : "+v"(d) : "v"(cb[ib]), "v"(ca[ia]));
      __builtin_amdgcn_sched_barrier(0);
      if (nm.v) {
#pragma unroll
        for (int u = (i * NR + GAPS - 1) / GAPS; u < ((i + 1) * NR + GAPS - 1) / GAPS; ++u) if (u < NR) rd(na, nb, kk, u, nm.v == 2);
        if (kk == 0 && i == GAPS) { if (nm.v == 2 && COLS) RING_NEXT(cx, dx, PairRing); else RING_NEXT(cx, dx, RX); }
        if (kk == 0 && i == GAPS + 1) { if (nm.v == 2 && ROWS) RING_NEXT(cy, dy, PairRing); else RING_NEXT(cy, dy, RY); }
        if (kk == 1 && i >= GAPS) {
#pragma unroll
          for (int u = i - GAPS; u < NA + NB; u += FREE) asm volatile("v_add_u32 %0, %1, %0" : "+v"(ad[u]) : "s"(u < NA ? dx : dy));
        }
      }
      extra(i);
      __builtin_amdgcn_sched_barrier(0);
    }
  };
  auto none = [](int) {};

  s16x8 fa0[MF], fb0[NF], fa1[MF], fb1[NF];
  auto ht = [&](int u, int e, int h = 1) -> f32x4& { return COLS ? acc[(u & 2) + e][(u & 1) + 2 * h] : acc[e + 2 * h][u]; };
  auto pair_at = [&](int u) { return COLS ? (u & 2) * (OW / 32) * 1024 + (u & 1) * 512 : u * 512; };
#define HALF_NOP(h) do { asm volatile("s_nop 11" : "+v"(ht(0, 0, h)), "+v"(ht(1, 0, h)), "+v"(ht(2, 0, h)), "+v"(ht(3, 0, h))); \
                         asm volatile("s_nop 0" : "+v"(ht(0, 1, h)), "+v"(ht(1, 1, h)), "+v"(ht(2, 1, h)), "+v"(ht(3, 1, h))); } while (0)
  const rsrc_t rw = mk_rsrc(wl, 2 * LSTRIDE);
  unsigned out_at = 0, flag_at = 0, x_at = 0;
  if (TN) {
    const int r = lane & 15, j = lane >> 4, hi = n0 >= D_;
    if (PH == GA) out_at = O_AB + (unsigned)(((hi * S_ + m0) >> 5) * (D_ / 16) + ((n0 - hi * D_) >> 4)) * 1024u + tr_lane(r, j);
    else if (PH == K4) out_at = O_H + (unsigned)(((m0 >> 4) + (j & 1)) * (FH / 32)) * 1024u + (n0 >> 4) * 512 + (j >> 1) * 256 + r * 16;
    else out_at = O_XB - l * LSTRIDE + (blockIdx.x * 4 + wid) * 4096 + lane * 16;
    if (C::SPLIT) flag_at = out_at ^ 16384;
    else flag_at = lane != -wid ? 2 * LSTRIDE : O_SF + (PH == GA ? (F_GA * 16 + t.bn) * 16 + t.bm : (F_K4 * 16 + t.bm) * 16 + t.bn) * sizeof(Flag);
    if (C::SPLIT) x_at = (PH == G2 ? O_X1 : LSTRIDE + O_X) + (unsigned)(((m0 >> 4) + (j & 1)) * (D_ / 32)) * 1024u + (n0 >> 4) * 512 + (j >> 1) * 256 + r * 16;
    asm volatile("" : "+v"(out_at), "+v"(flag_at), "+v"(x_at));
  }
  RING_NEXT(cx, dx, RX); RING_NEXT(cy, dy, RY);
  unsigned res_at = 0;
  if (PH == GA || PH == K4 || PH == K5) {
    const int r = lane & 15, j = lane >> 4;
    const Tile<CG2> u;
    const int um = u.bm * 128 + (PH == GA ? wm * 64 : wm * 32 + u.kh * 64), un = u.bn * 128 + wn * 64 + (PH == GA ? u.kh * 32 : 0);
    res_at = (unsigned)((um >> 4) + (j & 1)) * (D_ * 32) + (PH == GA && l == 0 ? r * (D_ * 2) + un * 2 + (j >> 1) * 16 : (un >> 4) * 512 + (j >> 1) * 256 + r * 16);
    asm volatile("" : "+v"(res_at));
  }
  auto empty = [&](int u) { __builtin_amdgcn_raw_buffer_store_b128((u32x4){~0u, ~0u, ~0u, ~0u}, rw, lane * 16 + u * 1024, O_XB - l * LSTRIDE + ((blockIdx.x ^ 1) * 4 + wid) * 4096, STAUX); };
  auto residual = [&](int u) {
    const bool raw = PH == GA && l == 0;
    const rsrc_t rr = mk_rsrc(raw ? p.x : wl + (PH == GA ? O_X : O_X1), S_ * D_ * 2);
    rv[u] = __builtin_amdgcn_raw_buffer_load_b128(rr, res_at, PH == GA ? (u & 2) * (D_ * 32) + (u & 1) * (raw ? 32 : 512) : u * 512, 16);
  };
  if (PH == K5 && (blockIdx.x >> 3) >= 28) {
    wait_flags(smem, flags(wl, F_G2, t.bm) + (lane & 15), ~tag_of(smem));
#pragma unroll
    for (int u = 0; u < 4; ++u) { residual(u); empty(u); }
    __builtin_amdgcn_s_waitcnt(0x0f70);
  }
  if (C::SPLIT) {
#pragma unroll
    for (int u = 0; u < 4; ++u) {
      u32x2 x0, x1; unpair_rows(rv[u], x0, x1); ht(u, 0) = unpack4(x0); ht(u, 1) = unpack4(x1);
      asm volatile("" : "+v"(ht(u, 0)), "+v"(ht(u, 1)));
    }
  }
  constexpr bool ZERO = PH != ZZ && PH != K4;
  if (!ZERO) {
#pragma unroll
    for (int mt = 0; mt < MT; ++mt) {
#pragma unroll
      for (int nt = 0; nt < NT; ++nt) acc[mt][nt] = (f32x4){ini[mt], ini[mt], ini[mt], ini[mt]};
      asm volatile("" : "+v"(acc[mt][0]), "+v"(acc[mt][1]), "+v"(acc[mt][2]), "+v"(acc[mt][3]));
    }
  }
#define TOUCH_AT 1
  if (PH == GA && l == 0) {
    first_tag(smem, p, wid, lane);
    if (TOUCH_AT == 0) { __builtin_amdgcn_s_sleep(FIRST_SLEEP); first_touch(p, wid, lane, tch); }
  }
  const int wcol = (wid >> 1) * FH + t.bn * 128 + (wid & 1) * 64;
  if (PH == ZZ && l == 0) {
#pragma unroll
    for (int i = 0; i < 8; ++i) ld16_async(zf[i >> 2][i & 3], p.win + (t.bm * 64 + i * 8) * (F_ * 2) + wcol * 2, (lane >> 3) * (F_ * 2) + (lane & 7) * 16);
  }
  BARRIER();
  TS(1); TSR(7); TR(0);
#pragma unroll
  for (int i = 0; i < MT + NT * BR; ++i) rd(fa0, fb0, 0, i, false);
  constexpr IC<1> ONE{}; constexpr IC<2> TWO{};
  auto stepm = [&](auto m, int hs, auto extra) {
    BARRIER();
    __builtin_amdgcn_sched_barrier(0);
    half(m, m, fa1, fb1, fa0, fb0, 0, hs, extra);
    half(m, m, fa0, fb0, fa1, fb1, 1, hs, [&](int i) { extra(i + 16); });
    WAIT_L(0);
  };
  auto step = [&]() { stepm(ONE, 0, none); };
#pragma unroll 1
  for (int part = 0; part < C::PARTS; ++part) {
    if (!ZERO) {
#pragma unroll
      for (int mt = 0; mt < MT; ++mt) asm volatile("s_nop 1" : "+v"(acc[mt][0]), "+v"(acc[mt][1]), "+v"(acc[mt][2]), "+v"(acc[mt][3]));
    }
    __builtin_amdgcn_sched_barrier(0);
    half(ONE, ONE, fa0, fb0, fa1, fb1, 1, ZERO ? -1 : 0, none);
    WAIT_L(0);
    if (PH == ZZ && l + part == 0) {
      int ln = lane; asm volatile("" : "+v"(ln));
      column_sums((u64*)(p.ws + O_SI) + (t.bm >> 1) * F_ + wcol + (t.bm & 1), ln, zf[0], zf[1]);
    }
    auto sums = [&](int at) {
      if constexpr (PH == GA) {
        if (l == 0) {
          int ln = lane; asm volatile("" : "+v"(ln));
          if (at == LAT_S) first_sums(p, wid, ln, fa0, fa1, fb0, fb1);
          if (at + 1 == TOUCH_AT) first_touch(p, wid, ln, tch);
        }
      }
    };
    sums(0);
    TR(part * TP + 1);
    step();
    sums(1);
    if (PH == K5) WAIT_V(0);
    int s = 2;
#pragma unroll 1
    for (int seg = PH != ZZ ? 2 : (part ? 0 : 1 + l); seg < 3; ++seg) {
      const int e = seg == 2 ? TP : (part ? NORM_S + seg * 4 : 4);
#pragma unroll 1
      for (; s < e; s += 2) { TR(part * TP + s); step(); TR(part * TP + s + 1); step(); if (PH == GA || PH == G2) WAIT_V(0); }
      if (PH == ZZ) {
        WAIT_V(0);
        if (seg == 0) {
          int ln = lane; asm volatile("" : "+v"(ln));
          const int r = ln & 15, j = ln >> 4;
          const rsrc_t ro = mk_rsrc(wl + O_Z2, S_ * FH * 2);
          const unsigned o = (unsigned)((m0 >> 5) * (FH / 16) + (n0 >> 4)) * 1024u + tr_lane(r, j);
          TS(10);
#pragma unroll
          for (int mp = 0; mp < 2; ++mp) {
            const f32x2 f0 = *(const LAS f32x2*)(smem + FS_OFF + (ml + mp * 32 + r) * 8), f1 = *(const LAS f32x2*)(smem + FS_OFF + (ml + mp * 32 + 16 + r) * 8);
#pragma unroll
            for (int nt = 0; nt < 4; ++nt)
              st16(ro, o + mp * (FH / 16) * 1024 + nt * 1024, pair_rows(pack4(zf[(mp * 2) % MT][nt] * f0[0] + f0[1]), pack4(zf[(mp * 2 + 1) % MT][nt] * f1[0] + f1[1])));
          }
          TS(11);
        }
      }
    }
    TR(part * TP + TP);
    if (PH == G2 || PH == K5) TS(12);
    BARRIER();
    if (PH == G2 || PH == K5) TS(13);
    __builtin_amdgcn_sched_barrier(0);
    if (PH == GA) asm volatile("" :: "v"(tch[0]), "v"(tch[1]));
    if (ROWS) cy >>= 1;
    if (COLS) cx >>= 1;
    half(ONE, IC<TN ? 2 : (C::PARTS > 1)>{}, fa1, fb1, fa0, fb0, 0, 0, none);
    if constexpr (TN > 0) {
      u64 tag;
      f32x2 st[2];
      f32x4 cs[4];
      u32x4 y;
      auto operands = [&](int h, int i) {
        if (PH != GA || i < 12) return;
        if (i < 14) st[i - 12] = *(const LAS f32x2*)(smem + ST_OFF + (h * 64 + ml + (i - 12) * 16) * 16 + (lane & 15) * 16);
        if (!h) cs[i - 12] = *(const LAS f32x4*)(smem + CS1_OFF + (wn * 64 + (i - 12) * 16) * 4 + (lane & 48));
      };
      auto finish = [&](int h) {
        HALF_NOP(h);
        if (h) { TS(2); TSR(14); } else TS(8);
#pragma unroll
        for (int u = 0; u < 4; ++u) {
          if (PH == GA) st16(rw, out_at + h * (2 * D_ / 16 * 1024) + u * 1024, pair_rows(pack4(ht(u, 0, h) * st[0][0] + cs[u] * st[0][1]), pack4(ht(u, 1, h) * st[1][0] + cs[u] * st[1][1])));
          if (PH == K4) st16(rw, out_at + h * 1024 + pair_at(u), pair_rows(pack4(ht(u, 0, h) * unpack4(zk[u & 2][(u & 1) + 2 * h])), pack4(ht(u, 1, h) * unpack4(zk[(u & 2) + 1][(u & 1) + 2 * h]))));
        }
        __builtin_amdgcn_sched_barrier(0);
        if (!h) TS(9);
      };
      auto send = [&](int i) {
        if (!C::SPLIT) return;
        const int u = COLS ? 0x1320 >> (i & 12) & 3 : i >> 2, e = (i & 3) - 1;
        if (e == 0 || e == 1) {
          f32x4& a = ht(u, e, 0);
          asm volatile("" : "+v"(a));
          const u32x2 q = pack4(a); y[2 * e] = min(q[0], 0xfffffffeu); y[2 * e + 1] = min(q[1], 0xfffffffeu);
          asm volatile("" : "+v"(y));
        }
        if (e == 2) st16(rw, out_at + u * 1024, y);
      };
      auto flag = [&] { __builtin_amdgcn_raw_buffer_store_b64(__builtin_bit_cast(u32x2, tag), rw, flag_at, 0, 16); };
      half(TWO, TWO, fa0, fb0, fa1, fb1, 1, 0, none);
      WAIT_L(0);
      if (PH == G2 || PH == K5) TS(15);
      TR(TP + 1);
      stepm(TWO, 0, [&](int i) {
        if (i == 15 && !C::SPLIT) tag = tag_of(smem);
        if (i >= 16 && i % 4 == 2 && !C::SPLIT) empty(i / 4 - 4);
      });
      TR(TP + 2); stepm(TWO, 0, [&](int i) { if (i >= 16 && i % 4 == 2 && !C::SPLIT) residual(i / 4 - 4); });
      TR(TP + 3);
      BARRIER();
      __builtin_amdgcn_sched_barrier(0);
      half(TWO, TWO, fa1, fb1, fa0, fb0, 0, 0, [&](int i) { operands(0, i); });
      if (!C::SPLIT) finish(0);
      half(TWO, TWO, fa0, fb0, fa1, fb1, 1, 1, send);
      WAIT_L(0);
      TR(TP + 4); stepm(TWO, 1, none);
      if (!C::SPLIT) __builtin_amdgcn_s_waitcnt(0x0f70);
#define PV_GAP 8
      TR(TP + 5);
      stepm(TWO, 1, [&](int i) {
        if (i == 0 && !C::SPLIT) { if (PH == GA) TSR(12); flag(); }
        if (C::SPLIT && i >= PV_GAP && i < PV_GAP + 8 && (i - PV_GAP) % 2 == 0) pv[(i - PV_GAP) / 2] = ld16<16>(rw, flag_at + (i - PV_GAP) * 512);
      });
      TR(TP + 6);
      BARRIER();
      __builtin_amdgcn_sched_barrier(0);
      half(TWO, IC<0>{}, fa1, fb1, fa0, fb0, 0, 1, [&](int i) { operands(1, i + 12); });
      finish(1);
      break;
    }
#pragma unroll
    for (int mt = 0; mt < MT; ++mt) asm volatile("s_nop %4" : "+v"(acc[mt][0]), "+v"(acc[mt][1]), "+v"(acc[mt][2]), "+v"(acc[mt][3]) : "n"(mt ? 0 : 11));
    if (part == C::PARTS - 1) { TS(2); TSR(14); } else TS(8);

    int ln = lane; asm volatile("" : "+v"(ln));
    const int r = ln & 15, j = ln >> 4;
    if (PH == ZZ) {
      f32x4 k[MT], cs[4];
#pragma unroll
      for (int mt = 0; mt < MT; ++mt) k[mt] = *(const LAS f32x4*)(smem + ST_OFF + (ml + mt * 16 + r) * 16);
      if (part == 0) {
#pragma unroll
        for (int nt = 0; nt < 4; ++nt) cs[nt] = *(const LAS f32x4*)(smem + CS3_OFF + (128 + wn * 64 + nt * 16 + j * 4) * 4);
#pragma unroll
        for (int mt = 0; mt < MT; ++mt)
#pragma unroll
          for (int nt = 0; nt < 4; ++nt) acc[mt][nt] += cs[nt] * k[mt][0];
      }
      float sm[4], q[4];
#pragma unroll
      for (int mt = 0; mt < MT; ++mt) {
        f32x4 (&z)[4] = zf[mt];
#pragma unroll
        for (int nt = 0; nt < 4; ++nt) z[nt] = gelu_tanh4(acc[mt][nt], k[mt]);
        if (part == 0) {
          const f32x4 s4 = (z[0] + z[1]) + (z[2] + z[3]), q4 = z[0] * z[0] + z[1] * z[1] + z[2] * z[2] + z[3] * z[3];
          sm[mt] = (s4[0] + s4[1]) + (s4[2] + s4[3]); q[mt] = (q4[0] + q4[1]) + (q4[2] + q4[3]);
        }
      }
      if (part == 0) {
        *(LAS f32x2*)(smem + PS_OFF + (wn * 128 + ml + ln) * 8) = (f32x2){join4(sm[0], sm[1], sm[2], sm[3]), join4(q[0], q[1], q[2], q[3])};
#pragma unroll
        for (int nt = 0; nt < 4; ++nt) cs[nt] = *(const LAS f32x4*)(smem + CS3_OFF + (wn * 64 + nt * 16 + j * 4) * 4);
#pragma unroll
        for (int mt = 0; mt < MT; ++mt)
#pragma unroll
          for (int nt = 0; nt < 4; ++nt) acc[mt][nt] = cs[nt] * k[mt][0];
      }
    }
    if (part < C::PARTS - 1) TS(9);
  }

  asm volatile("" : "+v"(lane));
  const int r = lane & 15, j = lane >> 4;
  if (PH == G2 || PH == K5) {
    {
      TS(10);
      auto there = [&] {
        unsigned mx = 0;
#pragma unroll
        for (int u = 0; u < 4; ++u) mx = max(max(mx, max(pv[u][0], pv[u][1])), max(pv[u][2], pv[u][3]));
        return __all(mx != 0xffffffffu);
      };
      if (__builtin_expect(!there(), 0)) {
#pragma unroll 1
        for (unsigned spin = 0; spin < SPIN_MAX; ++spin) {
          asm volatile("" ::: "memory");
#pragma unroll
          for (int u = 0; u < 4; ++u) pv[u] = ld16<16>(rw, flag_at + u * 1024);
          if (there()) break;
        }
      }
      TS(11);
#pragma unroll
      for (int u = 0; u < 4; ++u) { ht(u, 0) += unpack4((u32x2){pv[u][0], pv[u][1]}); ht(u, 1) += unpack4((u32x2){pv[u][2], pv[u][3]}); }
    }
    if (PH == K5 && l == 1) {
      const rsrc_t ro = mk_rsrc(p.out, S_ * D_ * 2);
#pragma unroll
      for (int i = 0; i < 4; ++i)
        __builtin_amdgcn_raw_buffer_store_b128(pair_cols(pack4(ht((i & 1) * 2, i >> 1)), pack4(ht((i & 1) * 2 + 1, i >> 1))), ro, ((m0 + (i >> 1) * 16 + r) * D_ + n0 + (i & 1) * 32 + j * 8) * 2, 0, 0);
      if (blockIdx.x == 0 && wid == 0 && lane == 0) __hip_atomic_store((u64*)(p.ws + O_EP), tag_of(smem), RLX_AGENT);
      TS(3);
    } else {
      u32x4 y[4];
#pragma unroll
      for (int u = 0; u < 4; ++u) { y[u] = pair_rows(pack4(ht(u, 0)), pack4(ht(u, 1))); st16(rw, x_at + pair_at(u), y[u]); }
      asm volatile("" ::: "memory");
      float sm[2] = {}, q[2] = {};
#pragma unroll
      for (int u = 0; u < 4; ++u) add_stat(y[u], sm[u >> 1], q[u >> 1]);
      if (PH == G2) {
        *(LAS f32x2*)(smem + PS_OFF + (wn * 128 + ml + lane) * 8) = (f32x2){join2(sm[0], sm[1]), join2(q[0], q[1])};
        asm volatile("" ::: "memory");
        if (lane == 0) __hip_atomic_fetch_add((LAS unsigned*)(smem + CNT_OFF) + l * 8 + 4, 1u, __ATOMIC_RELAXED, __HIP_MEMORY_SCOPE_WORKGROUP);
        TS(3);
        signal_done<0, false>(smem, l * 8 + 2, flags(wl, F_G2, t.bm) + t.bn * 2 + t.kh, nullptr);
      } else {
        const f32x2 ps = {sum32(sm[0] + sm[1]), sum32(q[0] + q[1])};
        if (lane < 32) __hip_atomic_store(stat_slot(wl + LSTRIDE + O_STX, n0 >> 6, m0 + lane), __builtin_bit_cast(u64, ps), RLX_AGENT);
        TS(3);
        signal_done<1, true>(smem, l * 8 + 5, flags(wl, F_K5, t.bm) + t.bn * 2 + t.kh, flags(wl + LSTRIDE, F_X0, t.bm) + t.bn * 2 + t.kh);
      }
    }
  } else {
    if (PH == ZZ) {
#pragma unroll
      for (int mt = 0; mt < MT; ++mt)
#pragma unroll
        for (int nt = 0; nt < 4; ++nt) zk[mt % 4][nt] = pack4(zf[mt][nt]);
    }
    TS(3);
  }
  TS(6); TSR(5); TS_OUT(PH);
}

template <int N> __device__ __forceinline__ f32x4 sum_of(const f32x4* g) {
  f32x4 a = g[0];
#pragma unroll
  for (int i = 1; i < N; ++i) {
    a += g[i];
  }
  return a;
}
template <int N, int W> __device__ __forceinline__ f32x2 row_stat(const f32x4 (&g)[8]) {
  const f32x4 a = sum_of<N>(g);
  const float mu = (a[0] + a[2]) * (1.0f / W), v = __builtin_fmaf(-mu, mu, (a[1] + a[3]) * (1.0f / W));
  return (f32x2){mu, (v < 0.f ? 0.f : v) + LN_EPS};
}

template <class C> __device__ __forceinline__ Flag* act_flag(char* wl) {
  const Tile<C> t;
  if (C::PH == GA) return flags(wl - LSTRIDE, F_K5, t.bm);
  if (C::PH == G2) return flags(wl, F_GA, t.kh * 8 + t.bn);
  if (C::PH == ZZ) return flags(wl, F_G2, t.bm);
  if (C::PH == K4) return flags(wl, F_Z, t.bn);
  return flags(wl, F_K4, t.bm);
}
template <class C>
__device__ __forceinline__ void act_phase(LAS char* smem, const Params& p, int l, int w, int lane, u64& early) {
  constexpr int PH = C::PH;
  const int wid = 6 + w;
  TS_DECL; TS(0);
  asm volatile("" : "+v"(lane));
  char* wl = p.ws + l * LSTRIDE;
  Stream<C, 1 - C::WSIDE> st(PH == GA && l == 0 ? p.x : wl + (PH == GA ? O_X : (PH == G2 ? O_AB : (PH == ZZ ? O_X1 : (PH == K4 ? O_Z2 : O_H)))),
                             S_ * 2 * (PH == G2 ? 2 * D_ : (PH == K4 || PH == K5 ? FH : D_)), l, w, lane, 0, 0);
  const unsigned fo = (lane & 15) * sizeof(Flag), all = PH == K5 ? 0x7fu << (blockIdx.x & 1) * 7 : 0xffffu;
#define POLL_WAIT 6
#define IDLE_GAP 8
  if (PH != GA || l) {
    const u64 tag = tag_of(smem);
    Flag* fl = act_flag<C>(wl);
    if (PH != K4 || !__all(early == tag)) {
      if (PH == GA || PH == ZZ) { wait_count(smem, PH == GA ? 5 : l * 8 + 2, 4); __builtin_amdgcn_s_sleep(POLL_WAIT); }
      else if (PH == K5 && (blockIdx.x >> 3) >= 28) poll_flags<IDLE_GAP>(tag, fl, fo, st.writers(0));
      poll_flags<0>(tag, fl, fo, st.writers(0) | st.writers(1) | st.writers(2) | st.writers(3));
    }
    TS(1); TSR(9);
    st.fill(smem, 3);
    TSR(11);
    u64 seen;
    auto check = [&] {
      LANDED1(st.NP, seen);
      const unsigned miss = (unsigned)__builtin_amdgcn_ballot_w64(seen != tag) & all;
      if (miss) poll_flags<0>(tag, fl, fo, miss);
    };
    ld8_async(seen, fl, fo);
    WAIT_V(2 * st.NP + 1);
    BARRIER();
    WAIT_V(st.NP + 1);
    st.template step<1>(smem, [] {}, true);
    st.template step<0>(smem, check, true);
    if (PH == K5) {
      fl += (F_KB - F_K4) * 256;
      st.template step<0>(smem, [] {}, true);
      st.template step<1>(smem, [&] { ld8_async(seen, fl, fo); }, true);
      st.template step<0>(smem, check, false);
    }
  } else {
    TS(1); TSR(9);
    st.fill(smem, 1);
    WAIT_V(0);
    BARRIER();
    st.fill(smem, 3);
    WAIT_V(st.NP);
  }
  TS(2);
  st.finish(smem, [&] { if (PH == ZZ) early = peek(act_flag<CK4>(wl)[lane & 15].v); });
  TSR(8); TS(3); TS_OUT(PH);
}

template <class C> __device__ __forceinline__ Stream<C, C::WSIDE> wt_stream(const Params& p, int l, int w, int lane, unsigned slot, int n) {
  constexpr int PH = C::PH;
  const Tile<C> t;
  return Stream<C, C::WSIDE>(PH == GA ? (t.bn >= 8 ? p.wdim : p.wdre) : (PH == G2 ? (t.kh ? p.wsim : p.wsre) : (PH == ZZ ? p.win : (PH == K4 ? p.wsp : p.wout))),
                             2 * (PH == GA ? D_ * D_ : (PH == ZZ ? D_ * F_ : (PH == K5 ? FH * D_ : S_ * S_))), l, w, lane, slot, n);
}
template <class C, class NEXT>
__device__ __forceinline__ void wt_phase(LAS char* smem, const Params& p, int l, int w, int lane, unsigned& wpos, bool mid) {
  constexpr int PH = C::PH, AH = Stream<C, C::WSIDE>::AHEAD % 10;
  asm volatile("" : "+v"(lane));
  const Tile<C> t;
  char* wl = p.ws + l * LSTRIDE;
  auto st = wt_stream<C>(p, l, w, lane, wpos, (PH == GA && l == 0) || (PH == K5 && !mid) ? 0 : AH);
  const int row = w * 64 + lane;
  const unsigned fo = (lane & 15) * sizeof(Flag);
  u64 seen;
  f32x4 g[8];
  if (PH == GA && l == 0) {
    st.fill(smem, 2); WAIT_V(8); BARRIER();
    st.fill(smem, 4); WAIT_V(16); BARRIER();
#pragma unroll 1
    for (int k = 6; k <= 8; k += 2) { st.fill(smem, k); WAIT_V(24); BARRIER(); }
  } else {
    if (PH == ZZ && l == 0) *(LAS unsigned*)(smem + BS_OFF + row * 4) = (unsigned)p.bsp[t.bm * 128 + row] << 16;
    if (PH == K5) st.fill(smem, AH);
    st.resume(smem);
  }
  if (PH == ZZ) {
    Flag* fl = flags(wl, F_WI, t.bn);
    st.go(smem, 2);
    ld8_async(seen, flags(wl, F_S1 + w, t.bm), fo);
    st.go(smem, 4);
    LANDED1(16, seen);
    if (l == 0 && w == 0 && lane == 0) __hip_atomic_store(fl[t.bm].v, tag_of(smem), RLX_AGENT);
    wait_flags(smem, flags(wl, F_S1 + w, t.bm) + (lane & 15), seen);
#pragma unroll
    for (int i = 0; i < 8; ++i) ld16_async(g[i], wl + O_ST1 + (i * S_ + t.bm * 128) * 16, row * 16);
    st.template skip<8, 1>();
    st.template go<8, 1>(smem, 7);
    LANDED8(16, g);
    {
      const f32x2 m = row_stat<8, D_>(g);
      const float rs = __builtin_amdgcn_rsqf(m[1]);
      *(LAS f32x4*)(smem + ST_OFF + row * 16) = (f32x4){-m[0], GC1 * rs, GC2 * rs * rs * rs, m[1] * rs};
    }
    st.template skip<0, 2>();
    if (l == 0) {
      ld8_async(seen, fl, fo);
      st.template go<0, 2>(smem, 10);
      LANDED1(16, seen);
      wait_flags(smem, fl + (lane & 15), seen);
#pragma unroll
      for (int i = 0; i < 8; ++i) ld16_async(g[i], p.ws + O_SI + (i * F_ + w * FH + t.bn * 128) * 8, lane * 16);
      st.template skip<8, 3>();
      st.template go<8, 3>(smem, 12);
      st.template go<0, 3>(smem, 13);
      LANDED8(16, g);
      const f32x4 a = sum_of<8>(g);
      *(LAS f32x2*)(smem + CS3_OFF + row * 8) = (f32x2){a[0] + a[2], a[1] + a[3]};
      st.template go<0, 3>(smem, 16);
    } else st.template go<0, 2>(smem, 16);
    st.fill(smem, 22);
    auto nx = wt_stream<NEXT>(p, l, w, lane, wpos + 32768, 0);
    asm volatile("" : "+v"(nx.vo[0]), "+v"(nx.vo[1]), "+v"(nx.vo[2]), "+v"(nx.vo[3]));
    st.template skip<0, 0>();
    {
      const u32x2 q = __builtin_bit_cast(u32x2, *(const LAS f32x2*)(smem + PS_OFF + row * 8) + *(const LAS f32x2*)(smem + PS_OFF + (128 + row) * 8));
      const u32x2 v = {q[0] < 0xfffffffeu ? q[0] : 0xfffffffeu, q[1] < 0xfffffffeu ? q[1] : 0xfffffffeu};
      __hip_atomic_store(stat_slot(wl + O_XCH, t.bn, t.bm * 128 + row), __builtin_bit_cast(u64, v), RLX_AGENT);
    }
    static_assert(NORM_S == 8, "");
    st.template skip<1, 1>();
    st.template go<1, 1>(smem, 20);
#pragma unroll
    for (int i = 0; i < 7; ++i) ld16_async(g[i], wl + O_XCH + (i * S_ + t.bm * 128) * 16, row * 16);
    st.template skip<7, 2>();
    st.template go<7, 2>(smem, 22);
    LANDED8(8, g);
    f32x2 m = row_stat<7, FH>(g);
    if (!__all(m[1] == m[1])) {
#pragma unroll 1
      for (unsigned spin = 0; spin < SPIN_MAX; ++spin) {
#pragma unroll
        for (int i = 0; i < 7; ++i) ld16_async(g[i], wl + O_XCH + (i * S_ + t.bm * 128) * 16, row * 16);
        LANDED8(0, g);
        unsigned mx = 0;
#pragma unroll
        for (int i = 0; i < 7; ++i) { const u32x4 u = __builtin_bit_cast(u32x4, g[i]); mx = max(max(mx, max(u[0], u[1])), max(u[2], u[3])); }
        if (__all(mx != 0xffffffffu)) break;
      }
      m = row_stat<7, FH>(g);
    }
    const float rs = __builtin_amdgcn_rsqf(m[1]);
    *(LAS f32x2*)(smem + FS_OFF + row * 8) = (f32x2){rs, -m[0] * rs};
    st.template skip<0, 3>();
    st.fill(smem, 27);
    st.template go<0, 1>(smem, 28);
    if (w == 0 && lane == 0) __hip_atomic_store(flags(wl, F_Z, t.bn)[t.bm].v, tag_of(smem), RLX_AGENT);
    nx.template go<0, 1>(smem, 0);
    nx.fill(smem, 6);
  } else {
    f32x4 c[4];
    if (PH == G2) {
      Flag* fl = flags(wl, F_GB, 0);
      const int fi = lane == 16 ? ((t.kh ^ 1) * 8 + t.bn) * 16 + t.bm : (t.kh * 8 + t.bn) * 16 + (lane & 15);
      st.go(smem, 4);
      const Tile<CGA> ta;
      if (w == 0 && lane == 0) __hip_atomic_store(flags(wl, F_GB, ta.bn)[ta.bm].v, tag_of(smem), RLX_AGENT);
      st.go(smem, 7);
      ld8_async(seen, fl, fi * sizeof(Flag));
      st.go(smem, 9);
      LANDED1(16, seen);
      wait_flags(smem, fl + fi, seen);
    } else if (PH == K5) {
      st.go(smem, 2);
      const Tile<CK4> tk;
      const int own = w ? F_RD * 256 + blockIdx.x : (F_KB * 16 + tk.bm) * 16 + tk.bn;
      if ((mid || w) && lane == 0) __hip_atomic_store(flags(wl, 0, 0)[own].v, tag_of(smem), RLX_AGENT);
      st.go(smem, 5);
      ld8_async(seen, flags(wl, F_RD, 0) + (blockIdx.x ^ 1), 0);
      st.go(smem, 7);
      LANDED1(16, seen);
      wait_flags(smem, flags(wl, F_RD, 0) + (blockIdx.x ^ 1), seen);
    } else if (PH == GA) {
      if (l == 0) {
        st.go(smem, 4);
        const int own = (F_X0 * 16 + t.bm) * 16 + t.bn + w * ((F_WD - F_X0) * 256 + (t.bn - t.bm) * 15);
        if (lane == 0) __hip_atomic_store(flags(wl, 0, 0)[own].v, tag_of(smem), RLX_AGENT);
      }
      const unsigned fa = ((lane & 16) && l == 0 ? F_WD * 16 + t.bn : F_X0 * 16 + t.bm) * 16 * sizeof(Flag) + fo;
      st.go(smem, l ? 2 : 7);
      ld8_async(seen, wl + O_SF, fa);
      st.go(smem, l ? 4 : 9);
      LANDED1(16, seen);
      wait_flags(smem, (Flag*)(wl + O_SF + fa), seen);
      if (l == 0) {
#pragma unroll
        for (int i = 0; i < 4; ++i) ld16_async(c[i], p.ws + O_SD + (i * 2048 + t.bn * 128) * 16, row * 16);
      }
#pragma unroll
      for (int i = 0; i < 8; ++i) ld16_async(g[i], wl + O_STX + (i * S_ + t.bm * 128) * 16, row * 16);
    }
    st.close(smem);
    if (PH == GA) {
      LANDED8(16, g);
      if (l == 0) {
        asm volatile("" : "+v"(c[0]), "+v"(c[1]), "+v"(c[2]), "+v"(c[3]));
        const f32x4 a = sum_of<4>(c);
        *(LAS float*)(smem + CS1_OFF + row * 4) = (a[0] + a[1]) + (a[2] + a[3]);
      }
      g[0] = sum_of<8>(g);
      asm volatile("" : "+v"(g[0]));
    }
    st.template skip<0, 4>();
    if (PH == GA) {
      asm volatile("" : "+v"(g[0]));
      const f32x2 m = row_stat<1, D_>(g);
      const float rs = __builtin_amdgcn_rsqf(m[1]);
      *(LAS f32x2*)(smem + ST_OFF + row * 16) = (f32x2){rs, -m[0] * rs} * (t.bn >= 8 ? -1.0f : 1.0f);
    }
    st.template skip<0, 4>();
    if (PH == G2 ? mid : PH != K5 || l == 0) wt_stream<NEXT>(p, 1, w, lane, st.slot, 0).open(smem);
    else { BARRIER(); BARRIER(); BARRIER(); }
    if (PH == G2) {
      wait_count(smem, l * 8 + 4, 4);
      const f32x2 q = *(const LAS f32x2*)(smem + PS_OFF + row * 8) + *(const LAS f32x2*)(smem + PS_OFF + (128 + row) * 8);
      __hip_atomic_store(stat_slot(wl + O_ST1, t.bn * 2 + t.kh, t.bm * 128 + row), __builtin_bit_cast(u64, q), RLX_AGENT);
      const u64 tag = tag_of(smem);
      WAIT_V(0);
      if (lane == 0) __hip_atomic_store(flags(wl, F_S1 + w, t.bm)[t.bn * 2 + t.kh].v, tag, RLX_AGENT);
    }
  }
  wpos = st.slot;
}

__device__ __forceinline__ void touch(LAS char* smem, const Params& p, int wid, int lane) {
  const int u = ((blockIdx.x >> 3) - 28 + (blockIdx.x & 7) * 4) * 8 + wid;
  BARRIER();
#pragma unroll 1
  for (int i = 0; i < 46; i += 2) {
    const char* g = (i < 32 ? p.wsp + (size_t)(u * 32 + i) * 1024 : p.wout + (size_t)(u * 14 + i - 32) * 1024) + lane * 16;
    __builtin_amdgcn_global_load_lds((const unsigned*)g, (LAS unsigned*)(smem + A_RING + wid * 1024), 16, 0, 0);
    __builtin_amdgcn_global_load_lds((const unsigned*)(g + 1024), (LAS unsigned*)(smem + A_RING + wid * 1024), 16, 0, 0);
    WAIT_V(8);
  }
  __builtin_amdgcn_s_waitcnt(0x0f70);
  BARRIER();
}

__global__ __launch_bounds__(512, 2) void mega_k(Params p) {
  extern __shared__ __attribute__((aligned(16))) char smem_[];
  LAS char* smem = (LAS char*)smem_;
  const int lane = threadIdx.x & 63, wid = __builtin_amdgcn_readfirstlane(threadIdx.x >> 6);
  const bool mid = (blockIdx.x >> 3) < 28;
  unsigned wpos = 0;
  if (wid < 4) {
    __builtin_amdgcn_s_waitcnt(0x0f70);
#pragma unroll 1
    for (int l = 0; l < 2; ++l) {
      u32x2 zk[4][4];
      u32x4 rv[4];
      compute_phase<CGA>(smem, p, l, wid, lane, zk, rv, wpos);
      compute_phase<CG2>(smem, p, l, wid, lane, zk, rv, wpos);
      if (mid) { compute_phase<CZZ>(smem, p, l, wid, lane, zk, rv, wpos); compute_phase<CK4>(smem, p, l, wid, lane, zk, rv, wpos); }
      else if (l == 0) touch(smem, p, wid, lane);
      compute_phase<CK5>(smem, p, l, wid, lane, zk, rv, wpos);
    }
  } else if (wid < 6) {
#pragma unroll 1
    for (int l = 0; l < 2; ++l) {
      wt_phase<CGA, CG2>(smem, p, l, wid - 4, lane, wpos, mid);
      wt_phase<CG2, CZZ>(smem, p, l, wid - 4, lane, wpos, mid);
      if (mid) { wt_phase<CZZ, CK4>(smem, p, l, wid - 4, lane, wpos, mid); wt_phase<CK4, CK5>(smem, p, l, wid - 4, lane, wpos, mid); }
      else if (l == 0) touch(smem, p, wid, lane);
      wt_phase<CK5, CGA>(smem, p, l, wid - 4, lane, wpos, mid);
    }
  } else {
#pragma unroll 1
    for (int l = 0; l < 2; ++l) {
      u64 early = 0;
      act_phase<CGA>(smem, p, l, wid - 6, lane, early);
      act_phase<CG2>(smem, p, l, wid - 6, lane, early);
      if (mid) { act_phase<CZZ>(smem, p, l, wid - 6, lane, early); act_phase<CK4>(smem, p, l, wid - 6, lane, early); }
      else if (l == 0) touch(smem, p, wid, lane);
      act_phase<CK5>(smem, p, l, wid - 6, lane, early);
    }
  }
}

extern "C" void kernel_launch(void* const* d_in, const int* in_sizes, int n_in, void* d_out, int out_size, void* d_ws, size_t ws_size, hipStream_t stream) {
  Params p;
  p.x = (const char*)d_in[0];
  p.wsre = (const char*)d_in[1]; p.wsim = (const char*)d_in[2]; p.wdre = (const char*)d_in[3]; p.wdim = (const char*)d_in[4];
  p.win = (const char*)d_in[6]; p.wsp = (const char*)d_in[7]; p.wout = (const char*)d_in[9]; p.bsp = (const u16*)d_in[8];
  p.out = (char*)d_out; p.ws = (char*)d_ws;
  (void)hipFuncSetAttribute((const void*)mega_k, hipFuncAttributeMaxDynamicSharedMemorySize, LDS_BYTES);
  hipLaunchKernelGGL(mega_k, dim3(256), dim3(512), LDS_BYTES, stream, p);
}
```

```cpp
#include <hip/hip_runtime.h>
#include <hip/hip_bf16.h>
#include <stdint.h>

#define LAS __attribute__((address_space(3)))
typedef unsigned short u16;
typedef short s16x4 __attribute__((ext_vector_type(4)));
typedef short s16x8 __attribute__((ext_vector_type(8)));
typedef __bf16 bf16x8 __attribute__((ext_vector_type(8)));
typedef __bf16 bf16x2 __attribute__((ext_vector_type(2)));
typedef float f32x2 __attribute__((ext_vector_type(2)));
typedef float f32x4 __attribute__((ext_vector_type(4)));
typedef unsigned u32x2 __attribute__((ext_vector_type(2)));
typedef unsigned u32x4 __attribute__((ext_vector_type(4)));

constexpr int S_ = 2048, D_ = 1024, F_ = 3584, FH = 1792;
constexpr float LN_EPS = 1e-5f;

__device__ __forceinline__ unsigned pack_bf16(float lo, float hi) {
  f32x2 v = {lo, hi};
  return __builtin_bit_cast(unsigned, __builtin_convertvector(v, bf16x2));
}
__device__ __forceinline__ float bf_lo(unsigned u) { return __builtin_bit_cast(float, u << 16); }
__device__ __forceinline__ float bf_hi(unsigned u) { return __builtin_bit_cast(float, u & 0xffff0000u); }
__device__ __forceinline__ void swap16(unsigned& a, unsigned& b) { asm volatile("s_nop 1\n\tv_permlane16_swap_b32 %0, %1" : "+v"(a), "+v"(b)); }
__device__ __forceinline__ void swap32(unsigned& a, unsigned& b) { asm volatile("s_nop 1\n\tv_permlane32_swap_b32 %0, %1" : "+v"(a), "+v"(b)); }
__device__ __forceinline__ float sum32(float v) {
  unsigned a = __builtin_bit_cast(unsigned, v), b = a; swap32(a, b);
  return __builtin_bit_cast(float, a) + __builtin_bit_cast(float, b);
}
__device__ __forceinline__ float sum_groups(float v) {
  unsigned a = __builtin_bit_cast(unsigned, v), b = a; swap16(a, b);
  return sum32(__builtin_bit_cast(float, a) + __builtin_bit_cast(float, b));
}
__device__ __forceinline__ u32x4 pair_rows(u32x2 a, u32x2 b) {
  unsigned a0 = a[0], a1 = a[1], b0 = b[0], b1 = b[1]; swap16(a0, b0); swap16(a1, b1);
  return (u32x4){a0, a1, b0, b1};
}
__device__ __forceinline__ void unpair_rows(u32x4 v, u32x2& a, u32x2& b) {
  unsigned a0 = v[0], a1 = v[1], b0 = v[2], b1 = v[3]; swap16(a0, b0); swap16(a1, b1);
  a = (u32x2){a0, a1}; b = (u32x2){b0, b1};
}
__device__ __forceinline__ u32x4 pair_cols(u32x2 a, u32x2 b) {
  unsigned a0 = a[0], a1 = a[1], b0 = b[0], b1 = b[1]; swap32(a0, b0); swap32(a1, b1); swap16(a0, b0); swap16(a1, b1);
  return (u32x4){a0, a1, b0, b1};
}
__device__ __forceinline__ void unpair_cols(u32x4 v, u32x2& a, u32x2& b) {
  unsigned a0 = v[0], a1 = v[1], b0 = v[2], b1 = v[3]; swap16(a0, b0); swap16(a1, b1); swap32(a0, b0); swap32(a1, b1);
  a = (u32x2){a0, a1}; b = (u32x2){b0, b1};
}
__device__ __forceinline__ u32x2 pack4(f32x4 v) { return (u32x2){pack_bf16(v[0], v[1]), pack_bf16(v[2], v[3])}; }
__device__ __forceinline__ f32x4 unpack4(u32x2 y) { return (f32x4){bf_lo(y[0]), bf_hi(y[0]), bf_lo(y[1]), bf_hi(y[1])}; }
constexpr float GC1 = -2.0f * 0.7978845608028654f * 1.4426950408889634f, GC2 = GC1 * 0.044715f;
__device__ __forceinline__ f32x4 gelu_tanh4(f32x4 a, f32x4 k) {
  const f32x4 t = a * (a * a * k[2] + k[1]);
  f32x4 e;
#pragma unroll
  for (int i = 0; i < 4; ++i) e[i] = __builtin_amdgcn_exp2f(t[i]);
  e = e * k[3] + k[3];
#pragma unroll
  for (int i = 0; i < 4; ++i) e[i] = __builtin_amdgcn_rcpf(e[i]);
  return a * e;
}
#define STAUX 16
typedef __amdgpu_buffer_rsrc_t rsrc_t;
__device__ __forceinline__ rsrc_t mk_rsrc(const void* p, unsigned bytes) { return __builtin_amdgcn_make_buffer_rsrc((void*)p, 0, bytes, 0x00020000); }
__device__ __forceinline__ void st16(rsrc_t r, unsigned off, u32x4 v) { __builtin_amdgcn_raw_buffer_store_b128(v, r, off, 0, STAUX); }
template <int AUX> __device__ __forceinline__ u32x4 ld16(rsrc_t r, unsigned off) { return __builtin_amdgcn_raw_buffer_load_b128(r, off, 0, AUX); }
#define WAIT_V(n) asm volatile("s_waitcnt vmcnt(%0)" ::"n"(n) : "memory")
#define WAIT_L(n) __builtin_amdgcn_s_waitcnt(0xc07f | ((n) << 8))
#define WAIT_VL(n) asm volatile("s_waitcnt vmcnt(%0) lgkmcnt(0)" ::"n"(n) : "memory")
#define WAIT_L0() asm volatile("s_waitcnt lgkmcnt(0)" ::: "memory")
#define BARRIER() do { asm volatile("" ::: "memory"); __builtin_amdgcn_s_barrier(); asm volatile("" ::: "memory"); } while (0)
#define PIN_S(x) asm volatile("" : "+s"(x))

#define RLX_AGENT __ATOMIC_RELAXED, __HIP_MEMORY_SCOPE_AGENT
#define PF_THR 8
#define POLL_SLEEP 1
#define NORM_S 8
#define SIGA_S 4
#define LAT_S 1
#define ZLAG 1
constexpr unsigned SPIN_MAX = 1u << 18;
typedef unsigned long long u64;
template <int CTRL> __device__ __forceinline__ float dpp_add(float v) {
  return v + __builtin_bit_cast(float, __builtin_amdgcn_update_dpp(0, __builtin_bit_cast(int, v), CTRL, 0xf, 0xf, true));
}
__device__ __forceinline__ float sum8(float v) { return dpp_add<0x141>(dpp_add<0x4e>(dpp_add<0xb1>(v))); }

constexpr size_t MB = 1 << 20, LSTRIDE = 44 * MB;
constexpr size_t O_X = 0, O_X1 = 4 * MB, O_AB = 8 * MB, O_Z2 = 16 * MB, O_H = 24 * MB, O_XB = 32 * MB, O_XB2 = 40 * MB;
constexpr size_t O_STX = 36 * MB, O_ST1 = O_STX + 256 * 1024;
constexpr size_t O_XCH = O_STX + 512 * 1024;
constexpr size_t O_SF = O_STX + 768 * 1024;
constexpr size_t O_SD = 2 * LSTRIDE, O_SI = O_SD + 128 * 1024;
constexpr size_t O_EP = O_SD + 512 * 1024;
enum { F_X0, F_WD, F_WI, F_GA, F_GB, F_G2, F_S1, F_S1B, F_Z, F_K4, F_KB, F_K5, F_XC, F_XD = F_XC + 4 };
constexpr unsigned W_RING = 96 * 1024, A_RING = W_RING, A_BYTES = 48 * 1024, CS1_OFF = 144 * 1024, CS3_OFF = CS1_OFF + 512, ST_OFF = CS3_OFF + 1024, PS_OFF = ST_OFF + 2048,
                   FS_OFF = PS_OFF + 2048, BS_OFF = FS_OFF + 1024, CNT_OFF = BS_OFF + 512, EP_OFF = CNT_OFF + 128;
constexpr unsigned LDS_BYTES = EP_OFF + 256;

enum { GA = 0, G2 = 1, ZZ = 2, K4 = 3, K5 = 4 };

struct Params {
  const char *x, *wsre, *wsim, *wdre, *wdim, *win, *wsp, *wout;
  const u16* bsp;
  char* out;
  char* ws;
};

template <int PH_, int BM_, int BN_, int WM_, int WN_, int AM_, int BMODE_, int T_, int PARTS_, int TN_, int XN_, int PA_, int PB_, int WSIDE_>
struct Cfg {
  static constexpr int PH = PH_, BM = BM_, BN = BN_, WM = WM_, WN = WN_, AM = AM_, BMODE = BMODE_, T = T_, PARTS = PARTS_;
  static constexpr int TN = TN_, TJ = T_ - TN_, XM = 4, XN = XN_, PA = PA_, PB = PB_, WSIDE = WSIDE_;
  static constexpr int MT = BM / WM / 16, NT = BN / WN / 16;
  static constexpr bool SPLIT = PH_ == G2 || PH_ == K5;
};
#define GA_TN 6
#define G2_TN 6
#define K4_TN 6
#define K5_TN 6
#define GA_FS 2
#define GA_HK 6
using CGA = Cfg<GA, 128, 128, 2, 2, 1, 0, 16, 1, GA_TN, 8, D_ / 32, 2048,    1>;
using CG2 = Cfg<G2, 128, 128, 2, 2, 0, 2, 32, 1, G2_TN, 8, 4096,    D_ / 16, 0>;
using CZZ = Cfg<ZZ, 128, 128, 2, 2, 1, 1, 32, 2, 0,     7, D_ / 32, F_ * 2,  1>;
using CK4 = Cfg<K4, 128, 128, 2, 2, 0, 2, 32, 1, K4_TN, 7, 4096,    FH / 16, 0>;
using CK5 = Cfg<K5, 128, 128, 2, 2, 1, 1, 14, 1, K5_TN, 8, FH / 32, D_ * 2,  1>;
template <int V> struct IC { static constexpr int v = V; };
struct PairRing { static constexpr unsigned SLOT = 32768, D = 3; };

template <class C> struct Tile {
  int bm, bn, kh, cls;
  __device__ __forceinline__ Tile() {
    int bid = blockIdx.x; PIN_S(bid);
    const int xcd = bid & 7, idx = bid >> 3, lm = idx / C::XN, ln = idx % C::XN;
    bm = (xcd >> 1) * C::XM + lm; bn = C::SPLIT ? ln : (xcd & 1) * C::XN + ln; kh = C::SPLIT ? xcd & 1 : 0; cls = (lm + ln) & 3;
  }
};
template <class C, int SIDE> struct Ring {
  static constexpr bool ISW = C::WSIDE == SIDE;
  static constexpr unsigned SLOT = (SIDE ? C::BN : C::BM) * 128, BASE = ISW ? 0 : A_RING, D = (ISW ? W_RING : A_BYTES) / SLOT;
};

#define FW 32
struct Flag { u64 v[FW]; };
__device__ __forceinline__ Flag* flags(char* wl, int id, int panel) { return (Flag*)(wl + O_SF) + (id * 16 + panel) * 16; }
__device__ __forceinline__ u64 tag_of(LAS char* smem) { return *(const volatile LAS u64*)(smem + EP_OFF); }
template <bool LATE> __device__ __forceinline__ void signal_done(LAS char* smem, int slot, Flag* flag, Flag* late) {
  const u64 tag = tag_of(smem);
  unsigned old;
#define COUNT_IN(off) "s_mov_b64 exec, 1\n\tds_add_rtn_u32 %0, %1, %2 offset:" #off "\n\ts_waitcnt lgkmcnt(0)\n\tv_cmp_eq_u32 vcc, 3, %0\n\ts_and_b64 exec, exec, vcc\n\t"
  if (LATE)
    asm volatile("s_waitcnt vmcnt(1)\n\t" COUNT_IN(0) "s_cbranch_scc0 .Lsig_a%=\n\tglobal_store_dwordx2 %3, %4, %5 sc1\n\ts_waitcnt vmcnt(1)\n\ts_branch .Lsig_b%=\n"
                 ".Lsig_a%=:\n\ts_waitcnt vmcnt(0)\n.Lsig_b%=:\n\t" COUNT_IN(4) "s_cbranch_scc0 .Lsig_c%=\n\tglobal_store_dwordx2 %3, %4, %6 sc1\n.Lsig_c%=:\n\ts_mov_b64 exec, -1"
                 : "=&v"(old) : "v"((unsigned)(size_t)(smem + CNT_OFF) + slot * 4), "v"(1), "v"(0), "v"(tag), "s"(flag), "s"(late) : "vcc", "scc", "memory");
  else
    asm volatile("s_waitcnt vmcnt(0)\n\t" COUNT_IN(0) "s_cbranch_scc0 .Lsig_c%=\n\tglobal_store_dwordx2 %3, %4, %5 sc1\n.Lsig_c%=:\n\ts_mov_b64 exec, -1"
                 : "=&v"(old) : "v"((unsigned)(size_t)(smem + CNT_OFF) + slot * 4), "v"(1), "v"(0), "v"(tag), "s"(flag) : "vcc", "scc", "memory");
}
__device__ __forceinline__ u64 peek(u64* f) { return __hip_atomic_load(f, RLX_AGENT); }
__device__ __forceinline__ void ld8_async(u64& d, const void* sbase, unsigned voff) { asm volatile("s_nop 4\n\tglobal_load_dwordx2 %0, %1, %2 sc1" : "=v"(d) : "v"(voff), "s"(sbase) : "memory"); }
#define LCNT(n) (n)
#define LANDED1(n, a) asm volatile("s_waitcnt vmcnt(%1)" : "+v"(a) : "n"(LCNT(n)) : "memory")
__device__ __forceinline__ void ld16_async(f32x4& d, const void* sbase, unsigned voff) { asm volatile("s_nop 4\n\tglobal_load_dwordx4 %0, %1, %2 sc1" : "=v"(d) : "v"(voff), "s"(sbase) : "memory"); }
#define LANDED8(n, g) asm volatile("s_waitcnt vmcnt(%8)" : "+v"(g[0]), "+v"(g[1]), "+v"(g[2]), "+v"(g[3]), "+v"(g[4]), "+v"(g[5]), "+v"(g[6]), "+v"(g[7]) : "n"(LCNT(n)) : "memory")
__device__ __forceinline__ u64* stat_slot(char* base, int g, int row) { return (u64*)base + ((g >> 1) * S_ + row) * 2 + (g & 1); }
__device__ __forceinline__ void wait_flags(LAS char* smem, Flag* f, u64 first) {
  const u64 tag = tag_of(smem);
  if (!__all(first == tag)) {
#pragma unroll 1
    for (unsigned spin = 0; spin < SPIN_MAX; ++spin) {
      if (__all(peek(f->v) == tag)) break;
      if (POLL_SLEEP) __builtin_amdgcn_s_sleep(POLL_SLEEP);
    }
  }
  asm volatile("" ::: "memory");
}

template <int GAP> __device__ __forceinline__ void poll_flags(u64 tag, const void* base, unsigned off, unsigned mask) {
  unsigned left = SPIN_MAX;
  u64 q;
  asm volatile("s_mov_b32 exec_lo, %5\n\ts_mov_b32 exec_hi, 0\n\ts_nop 4\n"
               ".Lpoll_next%=:\n\tglobal_load_dwordx2 %0, %2, %3 sc1\n\ts_waitcnt vmcnt(0)\n\tv_cmp_ne_u64 vcc, %4, %0\n\ts_and_b64 exec, exec, vcc\n\t"
               "s_cbranch_scc0 .Lpoll_done%=\n\ts_sleep %6\n\ts_sub_u32 %1, %1, 1\n\ts_cbranch_scc0 .Lpoll_next%=\n"
               ".Lpoll_done%=:\n\ts_mov_b64 exec, -1"
               : "=&v"(q), "+s"(left) : "v"(off), "s"(base), "v"(tag), "s"(__builtin_amdgcn_readfirstlane(mask)), "n"(GAP) : "vcc", "scc", "memory");
}
__device__ __forceinline__ void wait_count(LAS char* smem, int slot, unsigned n) {
#pragma unroll 1
  for (unsigned spin = 0; spin < 64 * SPIN_MAX; ++spin) {
    if (*((const volatile LAS unsigned*)(smem + CNT_OFF) + slot) >= n) break;
    __builtin_amdgcn_s_sleep(1);
  }
  asm volatile("" ::: "memory");
}

template <class C, int SIDE> struct Stream {
  using R = Ring<C, SIDE>;
  static constexpr int NP = 8, T = C::T, TJ = C::TJ, D = R::D, MODE = SIDE ? C::BMODE : (C::AM ? 3 : 0), PITCH = SIDE ? C::PB : C::PA;
  static constexpr unsigned KS = MODE == 0 ? 128 : (MODE == 1 ? 64 * PITCH : (MODE == 2 ? 2 * PITCH * 1024 : 2048));
  static constexpr unsigned HALF = MODE == 0 ? 64 * PITCH : (MODE == 1 ? 128 : (MODE == 2 ? 2048 : 4 * PITCH * 1024));
  static constexpr unsigned P4 = MODE == 0 ? 32 * PITCH : (MODE == 1 ? (C::PH == K5 ? 64 : 32) * PITCH : (MODE == 2 ? PITCH * 1024 : 2 * PITCH * 1024));
  static constexpr bool TWO = C::TN && !R::ISW;
  static_assert(R::SLOT == 16384 && (C::TN == 0 || (C::TN == 6 && TJ % 2 == 0)), "");
#define AH_GA 235
#define AH_G2 234
#define AH_K5 234
#define AH_ZZ 234
  static constexpr int AHEAD = C::PH == GA ? AH_GA : (C::PH == G2 ? AH_G2 : (C::PH == ZZ ? AH_ZZ : (C::PH == K5 ? AH_K5 : 6)));
  rsrc_t rs; unsigned base, vo[4], ks, slot, g, first; int cls, k0, lat, w, n;
  __device__ __forceinline__ Stream(const char* src, unsigned bytes, int l, int w_, int lane, unsigned slot0, int n0) {
    const Tile<C> t;
    const bool raw = MODE == 3 && C::PH == GA && l == 0;
    rs = mk_rsrc(src, bytes); w = w_; cls = t.cls; k0 = t.kh * T; ks = raw ? 128 : KS;
    lat = l ? LAT_S : (t.bm + t.bn) & 15;
    first = C::SPLIT && TWO ? (1 - t.kh) * HALF : 0;
    const int b = SIDE ? t.bn : t.bm;
    base = MODE == 0 ? (C::PH == GA ? b & 7 : b) * 128 * PITCH : (MODE == 1 ? b * 256 : (MODE == 2 ? b * 8192 + t.kh * T * KS : b * 8 * PITCH * 1024));
#pragma unroll
    for (int i = 0; i < 4; ++i) {
      const unsigned row = i * 8 + (lane >> 3), sw = ((lane & 7) ^ (((i & 1) << 2) | (lane >> 4))) << 4;
      if (MODE == 0) vo[i] = row * PITCH + sw;
      else if (MODE == 1) vo[i] = row * PITCH + (((lane & 7) ^ ((((lane >> 4) & 1) << 2) | ((i & 1) << 1))) << 4);
      else if (MODE == 2) vo[i] = (i >> 1) * 4096 + (i & 1) * 1024 + lane * 16;
      else vo[i] = raw ? row * (PITCH * 64) + sw : (i >> 1) * PITCH * 1024 + (i & 1) * (C::PH == K5 ? 2048 : 1024) + lane * 16;
      vo[i] -= i * 1024;
    }
    slot = slot0 + n0 * R::SLOT; if (slot >= D * R::SLOT) slot -= D * R::SLOT;
    n = n0; g = source<true>(n);
  }
  __device__ __forceinline__ int chunk(int ch) const {
    constexpr int L = T / C::PARTS;
    if (C::PARTS == 2) ch &= 15;
#define LEADMASK 7
    if ((LEADMASK >> C::PH & 1) && (ch & 3) == cls && ch >= 4) { ch += 4; if (ch >= L) ch = cls + 4; }
    if (C::PH == GA) ch = ch == LAT_S ? lat : (ch == lat ? LAT_S : ch);
    if (C::PH == G2) ch = (ch & 15) * 2 + (ch >> 4);
    if (C::PH == K5) ch = k0 + (ch < 7 ? 2 * ch : 2 * ch - 13);
    return ch;
  }
  __device__ __forceinline__ unsigned writers(int s) const { return C::PH == GA || C::PH == ZZ ? 3u << (chunk(s) & 14) : 1u << (chunk(s) >> 1); }
  template <bool JOINT> __device__ __forceinline__ unsigned source(int s) const {
    const bool tail = TWO && !JOINT && s >= TJ, second = tail ? s >= TJ + 3 : w;
    const int ch = chunk(tail ? 2 * s - TJ + w - (second ? 6 : 0) : s);
    unsigned a = base + (C::PH == K5 ? (2 * ch - (ch & 1)) * (KS / 2) : ch * ks) + (second ? HALF - first : first);
    if (C::PH == ZZ && SIDE == 1) a += (s & 16 ? 0 : FH * 2);
    return a;
  }
  __device__ __forceinline__ void put(LAS char* smem, bool joint = false) {
    {
#define PIECE(h, i) __builtin_amdgcn_raw_ptr_buffer_load_lds(rs, (LAS void*)(smem + R::BASE + slot + w * 8192 + h * 4096), 16, vo[i], g + h * P4, i * 1024, R::ISW ? 0 : 16)
#ifdef __HIP_DEVICE_COMPILE__
      PIECE(0, 0); PIECE(0, 1); PIECE(0, 2); PIECE(0, 3); PIECE(1, 0); PIECE(1, 1); PIECE(1, 2); PIECE(1, 3);
#endif
    }
    slot += R::SLOT; if (slot == D * R::SLOT) slot = 0;
    ++n; g = joint ? source<true>(n) : source<false>(n);
    PIN_S(g);
  }
  __device__ __forceinline__ void fill(LAS char* smem, int n1) {
#pragma unroll 1
    while (n < n1) put(smem, true);
  }
  template <int X, class F> __device__ __forceinline__ void step(LAS char* smem, F pre, bool joint) {
    BARRIER();
    pre();
    put(smem, joint);
    WAIT_V(NP + X);
  }
  template <class F> __device__ __forceinline__ void finish(LAS char* smem, F last) {
#pragma unroll 1
    while (n < TJ - (C::TN > 0)) step<0>(smem, [] {}, true);
    if (C::TN) {
#pragma unroll 1
      while (n < T) step<0>(smem, [] {}, false);
    }
    BARRIER();
    WAIT_V(0);
    BARRIER();
    last();
    BARRIER();
  }
  template <int X, int LAG> __device__ __forceinline__ void skip() const { WAIT_VL((4 - LAG) * NP + X); BARRIER(); }
  template <int X = 0, int LAG = 0> __device__ __forceinline__ void go(LAS char* smem, int b) {
#pragma unroll 1
    while (n < b + 5 - LAG) { put(smem); skip<X, LAG>(); }
  }
  __device__ __forceinline__ void close(LAS char* smem) {
    go(smem, TJ - 1);
#pragma unroll 1
    while (n < T) { put(smem); if (n < T) WAIT_VL(3 * NP); else WAIT_VL(2 * NP); BARRIER(); }
  }
  __device__ __forceinline__ void open(LAS char* smem) {
#pragma unroll 1
    for (int k = 8; k >= 0; k -= 4) { BARRIER(); fill(smem, (AHEAD / 100 * 256 + AHEAD / 10 % 10 * 16 + AHEAD % 10) >> k & 15); }
  }
  __device__ __forceinline__ void resume(LAS char* smem) {
    WAIT_V((AHEAD % 10 - 1) * NP);
    BARRIER();
    fill(smem, 6);
    skip<0, 0>();
  }
};

#define TS(i)
#define TSR(i)
#define TS_DECL
#define TS_OUT(ph)

__device__ __forceinline__ unsigned tr_lane(int r, int j) { return ((r >> 2) & 1) * 512 + (j & 1) * 256 + (r >> 3) * 128 + (r & 3) * 32 + (j >> 1) * 16; }
__device__ __forceinline__ void unpack8(u32x4 v, float (&e)[8]) {
#pragma unroll
  for (int i = 0; i < 4; ++i) { e[2 * i] = bf_lo(v[i]); e[2 * i + 1] = bf_hi(v[i]); }
}

__device__ __forceinline__ float dot2(float a, float b, float c) { return __builtin_amdgcn_fdot2_f32_bf16(__builtin_bit_cast(bf16x2, a), __builtin_bit_cast(bf16x2, b), c, false); }
#define FIRST_SLEEP 4
__device__ __forceinline__ void first_touch(const Params& p, int wid, int lane, u64 (&tch)[2]) {
  const Tile<CG2> u; const Tile<CZZ> z;
#define TCH0_N 4
#define TCH0_H 1
#define TCH1_N 4
  ld8_async(tch[0], (u.kh ? p.wsim : p.wsre) + (u.bm * 128 + u.bn * 16 + wid * 4) * (S_ * 2), (lane >> 4) * (S_ * 2) + ((lane >> 1) & (TCH0_N - 1)) * 256 + (lane & TCH0_H) * 64);
  ld8_async(tch[1], p.win + ((z.bm & 15) * 4 + wid) * TCH1_N * (F_ * 2) + (FH + z.bn * 128) * 2, (lane >> 2) % TCH1_N * (F_ * 2) + (lane & 3) * 64);
}
__device__ __forceinline__ void first_tag(LAS char* smem, const Params& p, int wid, int lane) {
  if (wid == 2 && lane < 32) *(LAS unsigned*)(smem + CNT_OFF + lane * 4) = 0u;
  if (wid == 3) {
    u64 e0;
    asm volatile("s_load_dwordx2 %0, %1, 0x0\n\ts_waitcnt lgkmcnt(0)" : "=s"(e0) : "s"(p.ws + O_EP) : "memory");
    u64 e = (e0 ^ (e0 >> 29)) * 0x9E3779B97F4A7C15ull + 0x632BE59BD9B4E019ull;
    if (e == e0) e = ~e0;
    if (lane == 0) *(LAS u64*)(smem + EP_OFF) = e;
  }
  WAIT_L0();
}
__device__ __forceinline__ float join4(float a, float b, float c, float d) {
  unsigned x = __builtin_bit_cast(unsigned, a), y = __builtin_bit_cast(unsigned, c), z = __builtin_bit_cast(unsigned, b), w = __builtin_bit_cast(unsigned, d);
  swap32(x, y); swap32(z, w);
  x = __builtin_bit_cast(unsigned, __builtin_bit_cast(float, x) + __builtin_bit_cast(float, y));
  z = __builtin_bit_cast(unsigned, __builtin_bit_cast(float, z) + __builtin_bit_cast(float, w));
  swap16(x, z);
  return __builtin_bit_cast(float, x) + __builtin_bit_cast(float, z);
}
__device__ __forceinline__ void add_stat(u32x2 y, float& s, float& q) {
#pragma unroll
  for (int k = 0; k < 2; ++k) { const unsigned u = y[k]; const float v = __builtin_bit_cast(float, u); s = dot2(v, __builtin_bit_cast(float, 0x3f803f80u), s); q = dot2(v, v, q); }
}
__device__ __forceinline__ float join32(float a, float b) {
  unsigned x = __builtin_bit_cast(unsigned, a), y = __builtin_bit_cast(unsigned, b); swap32(x, y);
  x = __builtin_bit_cast(unsigned, __builtin_bit_cast(float, x) + __builtin_bit_cast(float, y)); y = x; swap16(x, y);
  return __builtin_bit_cast(float, x) + __builtin_bit_cast(float, y);
}
__device__ __forceinline__ void first_sums(const Params& p, int wid, int lane, const s16x8 (&a0)[4], const s16x8 (&a1)[4], const s16x8 (&b0)[8], const s16x8 (&b1)[8]) {
  const Tile<CGA> t;
  const int wm = wid >> 1, wn = wid & 1, g = (t.bm + t.bn) & 15;
  const float ones = __builtin_bit_cast(float, 0x3f803f80u);
  float a[2] = {}, q[2] = {}, b[2] = {};
#pragma unroll
  for (int i = 0; i < 4; ++i) {
    const f32x4 x = __builtin_bit_cast(f32x4, wn ? (i & 2 ? a1 : a0)[2 + (i & 1)] : (i & 2 ? a1 : a0)[i & 1]);
    const f32x4 w = __builtin_bit_cast(f32x4, wm ? (i & 2 ? b1 : b0)[2 + (i & 1)] : (i & 2 ? b1 : b0)[i & 1]);
#pragma unroll
    for (int k = 0; k < 4; ++k) { a[i & 1] = dot2(x[k], ones, a[i & 1]); q[i & 1] = dot2(x[k], x[k], q[i & 1]); b[i & 1] = dot2(w[k], ones, b[i & 1]); }
  }
  const f32x2 st = {join32(a[0], a[1]), join32(q[0], q[1])};
  const float sw = join32(b[0], b[1]);
  if (!(lane & 16)) {
    const int row = wn * 64 + wm * 32 + (lane >> 5) * 16 + (lane & 15);
    __hip_atomic_store(stat_slot(p.ws + O_STX, g, t.bm * 128 + row), __builtin_bit_cast(u64, st), RLX_AGENT);
    __hip_atomic_store((unsigned*)(p.ws + O_SD) + ((g >> 2) * 2048 + t.bn * 128 + row) * 4 + (g & 3), __builtin_bit_cast(unsigned, sw), RLX_AGENT);
  }
  if (wid < 2) st16(mk_rsrc(p.ws + wid * LSTRIDE + O_XCH, 256 * 1024), blockIdx.x * 1024 + lane * 16, (u32x4){~0u, ~0u, ~0u, ~0u});
}
__device__ __forceinline__ void column_sums(u64* dst, int lane, f32x4 (&v)[4], f32x4 (&w)[4]) {
  asm volatile("s_waitcnt vmcnt(0)" : "+v"(v[0]), "+v"(v[1]), "+v"(v[2]), "+v"(v[3]), "+v"(w[0]), "+v"(w[1]), "+v"(w[2]), "+v"(w[3]) :: "memory");
  float s[8], e[8];
  unpack8(__builtin_bit_cast(u32x4, v[0]), s);
#pragma unroll
  for (int i = 1; i < 8; ++i) {
    unpack8(__builtin_bit_cast(u32x4, i < 4 ? v[i & 3] : w[i & 3]), e);
#pragma unroll
    for (int k = 0; k < 8; ++k) s[k] += e[k];
  }
#pragma unroll
  for (int k = 0; k < 4; ++k) { unsigned a = __builtin_bit_cast(unsigned, s[k]), b = __builtin_bit_cast(unsigned, s[k + 4]); swap32(a, b); s[k] = __builtin_bit_cast(float, a) + __builtin_bit_cast(float, b); }
#pragma unroll
  for (int k = 0; k < 2; ++k) { unsigned a = __builtin_bit_cast(unsigned, s[k]), b = __builtin_bit_cast(unsigned, s[k + 2]); swap16(a, b); s[k] = dpp_add<0x128>(__builtin_bit_cast(float, a) + __builtin_bit_cast(float, b)); }
  if (!(lane & 8)) __hip_atomic_store(dst + (lane & 7) * 8 + (lane >> 4) * 2, __builtin_bit_cast(u64, (f32x2){s[0], s[1]}), RLX_AGENT);
}

template <class C>
__device__ __forceinline__ void compute_phase(LAS char* smem, const Params& p, int l, int wid, int lane, u32x2 (&zk)[4][4], u32x4 (&rv)[4], unsigned& wpos) {
  constexpr int BN = C::BN, MT = C::MT, NT = C::NT, PH = C::PH, TN = C::TN, TP = C::T / C::PARTS - TN;
  using RX = Ring<C, 0>; using RY = Ring<C, 1>;
  TS_DECL; TSR(4); TS(0);
#define TR(i)
  asm volatile("" : "+v"(lane));
  const Tile<C> t;
  char* wl = p.ws + l * LSTRIDE;
  constexpr bool ROWS = TN && C::WSIDE, COLS = TN && !C::WSIDE;
  const int wm = wid / C::WN, wn = wid % C::WN, n0 = t.bn * BN + wn * (NT * 16) + (COLS ? t.kh * 32 : 0);
  const int ml = ROWS ? wm * 32 + t.kh * 64 : wm * 64, m0 = t.bm * 128 + ml;
  constexpr int OW = PH == K4 ? FH : D_;
  f32x4 acc[MT][NT];
  u64 tch[2] = {}, seen;
  f32x4 zf[MT][4];
  float ini[MT];
  constexpr int NA = C::AM == 0 || PH == GA ? 2 : 1, NB = C::BMODE == 0 ? 2 : (C::BMODE == 1 ? 4 : 1);
  unsigned ad[NA + NB];
  const unsigned xs = C::WSIDE ? 0 : wpos, ys = C::WSIDE ? wpos : 0;
  {
    const int r = lane & 15, j = lane >> 4;
    constexpr int MW = ROWS ? 2 : MT;
    if (C::AM == 0 || (PH == GA && l == 0)) { ad[0] = RX::BASE + xs + (wm * MW * 16 + r) * 128 + ((j ^ (r >> 1)) << 4); ad[NA - 1] = ad[0] ^ 64; }
    else { ad[0] = RX::BASE + xs + wm * MW * 2048 + lane * 16; if (NA == 2) ad[1] = ad[0] + 1024; }
    if (C::BMODE == 0) { ad[NA] = RY::BASE + ys + (wn * NT * 16 + r) * 128 + ((j ^ (r >> 1)) << 4); ad[NA + NB - 1] = ad[NA] ^ 64; }
    else if (C::BMODE == 1) {
      const int q = (lane >> 2) & 3, pp = lane & 3, f = ((q >> 1) << 1) | (j & 1);
#pragma unroll
      for (int c = 0; c < NB; ++c) ad[NA + c] = RY::BASE + ys + wn * 8192 + (j * 8 + q) * 128 + ((c ^ f) << 5) + pp * 8;
    } else ad[NA] = RY::BASE + ys + wn * 2048 + lane * 8;
#pragma unroll
    for (int u = 0; u < NA + NB; ++u) ad[u] += (unsigned)(size_t)smem;
#pragma unroll
    for (int mt = 0; mt < MT; ++mt) ini[mt] = PH == K4 ? *(const LAS float*)(smem + BS_OFF + (ml + mt * 16 + r) * 4) : 0.f;
  }
  unsigned cx = RX::D - 1 - xs / RX::SLOT, cy = RY::D - 1 - ys / RY::SLOT, dx, dy;
  wpos += (C::T + TN) * Ring<C, C::WSIDE>::SLOT % W_RING; if (wpos >= W_RING) wpos -= W_RING;
#define RING_NEXT(c, d, R) asm volatile("s_sub_u32 %0, %0, 1\n\ts_cselect_b32 %1, %2, %3\n\ts_cselect_b32 %0, %4, %0" : "+s"(c), "=&s"(d) : "n"(-(int)((R::D - 1) * R::SLOT)), "s"(R::SLOT), "n"(R::D - 1) : "scc")
  static_assert(NT == 4, "");
  constexpr int BR = C::BMODE == 0 ? 1 : 2, NM = MT * NT, MF = COLS ? 2 * MT : MT, NF = ROWS ? 2 * NT : NT;
  auto rd = [&](s16x8 (&fa)[MF], s16x8 (&fb)[NF], int kk, int i, bool two) {
    const int ma = two ? MF / 2 : MT, nb = two ? NF / 2 : NT, q = i / (ma + nb * BR);
    i -= q * (ma + nb * BR);
    if (i >= BR && i < BR + ma) {
      const int mt = i - BR + q * ma;
      fa[mt] = *(const LAS s16x8*)((LAS char*)(size_t)ad[NA == 2 ? kk : 0] + (NA == 2 ? 0 : kk * 1024) + (ROWS ? (mt >> 1) * 8192 + (mt & 1) * 2048 : mt / MT * RX::SLOT + mt % MT * 2048));
      return;
    }
    const int ib = i < BR ? i : i - ma, n = q * nb + ib / BR, nt = n % NT, h = ib % BR;
    s16x8& f = fb[n];
    if (C::BMODE == 0) { f = *(const LAS s16x8*)((LAS char*)(size_t)ad[NA + kk] + nt * 2048 + n / NT * RY::SLOT); return; }
    const s16x4 v = __builtin_amdgcn_ds_read_tr16_b64_v4i16((LAS s16x4*)((LAS char*)(size_t)ad[NA + nt % NB] + kk * 4096 + (C::BMODE == 1 ? 0 : (nt >> 1) * 8192 + (nt & 1) * 1024) + h * 512 + n / NT * RY::SLOT));
    if (h == 0) f = __builtin_shufflevector(v, v, 0, 1, 2, 3, -1, -1, -1, -1);
    else { const s16x4 lo = __builtin_shufflevector(f, f, 0, 1, 2, 3); f = __builtin_shufflevector(lo, v, 0, 1, 2, 3, 4, 5, 6, 7); }
  };
  auto half = [&](auto cm, auto nm, const s16x8 (&ca)[MF], const s16x8 (&cb)[NF], s16x8 (&na)[MF], s16x8 (&nb)[NF], int kk, int hs, auto extra) {
    const bool zero = hs < 0;
    constexpr int NR = nm.v == 2 ? MF + NF * BR : MT + NT * BR;
    constexpr int GAPS = NR <= NM - 2 ? NR : NM - 2, FREE = NM - GAPS;
#pragma unroll
    for (int i = 0; i < NM; ++i) {
      constexpr int MC = cm.v == 2 ? MF / 2 : MT, NC = cm.v == 2 ? NF / 2 : NT;
      const int ib = i / MC, m = (ib & 1) ? MC - 1 - i % MC : i % MC, ia = m + ib / NC * MC;
      f32x4& d = acc[m + (ROWS && hs > 0 ? 2 : 0)][ib % NC + (COLS && hs > 0 ? 2 : 0)];
      if (MT == 4 && cm.v == 1 && kk == 1 && i == 0) WAIT_L(NR - BR - MT);
      if (MT == 4 && cm.v == 1 && kk == 1 && i == MT) WAIT_L(MT);
      if (cm.v == 2 && kk == 1 && i == 0) WAIT_L(NR / 2);
      if (cm.v == 2 && kk == 1 && i == NM / 2) WAIT_L((NM / 2 * NR + GAPS - 1) / GAPS);
      if (zero) asm volatile("v_mfma_f32_16x16x32_bf16 %0, %1, %2, 0" : "=&v"(d) : "v"(cb[ib]), "v"(ca[ia]));
      else asm volatile("v_mfma_f32_16x16x32_bf16 %0, %1, %2, %0" : "+v"(d) : "v"(cb[ib]), "v"(ca[ia]));
      __builtin_amdgcn_sched_barrier(0);
      if (nm.v) {
#pragma unroll
        for (int u = (i * NR + GAPS - 1) / GAPS; u < ((i + 1) * NR + GAPS - 1) / GAPS; ++u) if (u < NR) rd(na, nb, kk, u, nm.v == 2);
        if (kk == 0 && i == GAPS) { if (nm.v == 2 && COLS) RING_NEXT(cx, dx, PairRing); else RING_NEXT(cx, dx, RX); }
        if (kk == 0 && i == GAPS + 1) { if (nm.v == 2 && ROWS) RING_NEXT(cy, dy, PairRing); else RING_NEXT(cy, dy, RY); }
        if (kk == 1 && i >= GAPS) {
#pragma unroll
          for (int u = i - GAPS; u < NA + NB; u += FREE) asm volatile("v_add_u32 %0, %1, %0" : "+v"(ad[u]) : "s"(u < NA ? dx : dy));
        }
      }
      extra(i);
      __builtin_amdgcn_sched_barrier(0);
    }
  };
  auto none = [](int) {};

  s16x8 fa0[MF], fb0[NF], fa1[MF], fb1[NF];
  auto ht = [&](int u, int e, int h = 1) -> f32x4& { return COLS ? acc[(u & 2) + e][(u & 1) + 2 * h] : acc[e + 2 * h][u]; };
  auto pair_at = [&](int u) { return COLS ? (u & 2) * (OW / 32) * 1024 + (u & 1) * 512 : u * 512; };
#define HALF_NOP(h) do { asm volatile("s_nop 11" : "+v"(ht(0, 0, h)), "+v"(ht(1, 0, h)), "+v"(ht(2, 0, h)), "+v"(ht(3, 0, h))); \
                         asm volatile("s_nop 0" : "+v"(ht(0, 1, h)), "+v"(ht(1, 1, h)), "+v"(ht(2, 1, h)), "+v"(ht(3, 1, h))); } while (0)
  const rsrc_t rw = mk_rsrc(wl, LSTRIDE);
  unsigned out_at = 0, flag_at = 0;
  if (TN) {
    const int r = lane & 15, j = lane >> 4, hi = n0 >= D_;
    if (PH == GA) out_at = O_AB + (unsigned)(((hi * S_ + m0) >> 5) * (D_ / 16) + ((n0 - hi * D_) >> 4)) * 1024u + tr_lane(r, j);
    else if (PH == K4) out_at = O_H + (unsigned)(((m0 >> 4) + (j & 1)) * (FH / 32)) * 1024u + (n0 >> 4) * 512 + (j >> 1) * 256 + r * 16;
    else out_at = (PH == G2 ? O_XB : O_XB2) + (blockIdx.x * 4 + wid) * 4096 + lane * 16;
    flag_at = O_SF + (PH == GA ? (F_GA * 16 + t.bn) * 16 + t.bm : PH == K4 ? (F_K4 * 16 + t.bm) * 16 + t.bn : (PH == G2 ? F_XC : F_XD) * 256 + blockIdx.x * 4 + wid) * sizeof(Flag);
    if (lane != (C::SPLIT ? 0 : -wid)) flag_at = LSTRIDE;
    asm volatile("" : "+v"(out_at), "+v"(flag_at));
  }
  RING_NEXT(cx, dx, RX); RING_NEXT(cy, dy, RY);
  unsigned res_at = 0;
  if (PH == GA || PH == K4 || PH == K5) {
    const int r = lane & 15, j = lane >> 4;
    const Tile<CG2> u;
    const int um = u.bm * 128 + (PH == GA ? wm * 64 : wm * 32 + u.kh * 64), un = u.bn * 128 + wn * 64 + (PH == GA ? u.kh * 32 : 0);
    res_at = (unsigned)((um >> 4) + (j & 1)) * (D_ * 32) + (PH == GA && l == 0 ? r * (D_ * 2) + un * 2 + (j >> 1) * 16 : (un >> 4) * 512 + (j >> 1) * 256 + r * 16);
    asm volatile("" : "+v"(res_at));
  }
  auto residual = [&]() {
    const bool raw = PH == GA && l == 0;
    const rsrc_t rr = mk_rsrc(raw ? p.x : wl + (PH == GA ? O_X : O_X1), S_ * D_ * 2);
#pragma unroll
    for (int u = 0; u < 4; ++u) rv[u] = __builtin_amdgcn_raw_buffer_load_b128(rr, res_at, PH == GA ? (u & 2) * (D_ * 32) + (u & 1) * (raw ? 32 : 512) : u * 512, 16);
  };
  if (PH == K5 && (blockIdx.x >> 3) >= 28) {
    wait_flags(smem, flags(wl, F_G2, t.bm) + (lane & 15), ~tag_of(smem));
    residual();
  }
  constexpr bool ZERO = PH != ZZ && PH != K4;
  if (!ZERO) {
#pragma unroll
    for (int mt = 0; mt < MT; ++mt) {
#pragma unroll
      for (int nt = 0; nt < NT; ++nt) acc[mt][nt] = (f32x4){ini[mt], ini[mt], ini[mt], ini[mt]};
      asm volatile("" : "+v"(acc[mt][0]), "+v"(acc[mt][1]), "+v"(acc[mt][2]), "+v"(acc[mt][3]));
    }
  }
#define TOUCH_AT 1
  if (PH == GA && l == 0) {
    first_tag(smem, p, wid, lane);
    if (TOUCH_AT == 0) { __builtin_amdgcn_s_sleep(FIRST_SLEEP); first_touch(p, wid, lane, tch); }
  }
  const int wcol = (wid >> 1) * FH + t.bn * 128 + (wid & 1) * 64;
  if (PH == ZZ && l == 0) {
#pragma unroll
    for (int i = 0; i < 8; ++i) ld16_async(zf[i >> 2][i & 3], p.win + (t.bm * 64 + i * 8) * (F_ * 2) + wcol * 2, (lane >> 3) * (F_ * 2) + (lane & 7) * 16);
  }
  BARRIER();
  TS(1); TSR(7); TR(0);
#pragma unroll
  for (int i = 0; i < MT + NT * BR; ++i) rd(fa0, fb0, 0, i, false);
  constexpr IC<1> ONE{}; constexpr IC<2> TWO{};
  auto stepm = [&](auto m, int hs, auto extra) {
    BARRIER();
    __builtin_amdgcn_sched_barrier(0);
    half(m, m, fa1, fb1, fa0, fb0, 0, hs, extra);
    half(m, m, fa0, fb0, fa1, fb1, 1, hs, none);
    WAIT_L(0);
  };
  auto step = [&]() { stepm(ONE, 0, none); };
#pragma unroll 1
  for (int part = 0; part < C::PARTS; ++part) {
    if (!ZERO) {
#pragma unroll
      for (int mt = 0; mt < MT; ++mt) asm volatile("s_nop 1" : "+v"(acc[mt][0]), "+v"(acc[mt][1]), "+v"(acc[mt][2]), "+v"(acc[mt][3]));
    }
    __builtin_amdgcn_sched_barrier(0);
    half(ONE, ONE, fa0, fb0, fa1, fb1, 1, ZERO ? -1 : 0, none);
    WAIT_L(0);
    if (PH == ZZ && l + part == 0) {
      int ln = lane; asm volatile("" : "+v"(ln));
      column_sums((u64*)(p.ws + O_SI) + (t.bm >> 1) * F_ + wcol + (t.bm & 1), ln, zf[0], zf[1]);
    }
    auto sums = [&](int at) {
      if constexpr (PH == GA) {
        if (l == 0) {
          int ln = lane; asm volatile("" : "+v"(ln));
          if (at == LAT_S) first_sums(p, wid, ln, fa0, fa1, fb0, fb1);
          if (at + 1 == TOUCH_AT) first_touch(p, wid, ln, tch);
        }
      }
    };
    sums(0);
    TR(part * TP + 1);
    step();
    sums(1);
    if (PH == K5) WAIT_V(0);
    int s = 2;
#pragma unroll 1
    for (int seg = PH != ZZ ? 2 : (part ? 0 : 1 + l); seg < 3; ++seg) {
      const int e = seg == 2 ? TP : (part ? NORM_S + seg * 4 : 4);
#pragma unroll 1
      for (; s < e; s += 2) { TR(part * TP + s); step(); TR(part * TP + s + 1); step(); if (PH == GA || PH == G2) WAIT_V(0); }
      if (PH == ZZ) {
        WAIT_V(0);
        if (seg == 0) {
          int ln = lane; asm volatile("" : "+v"(ln));
          const int r = ln & 15, j = ln >> 4;
          const rsrc_t ro = mk_rsrc(wl + O_Z2, S_ * FH * 2);
          const unsigned o = (unsigned)((m0 >> 5) * (FH / 16) + (n0 >> 4)) * 1024u + tr_lane(r, j);
          TS(10);
#pragma unroll
          for (int mp = 0; mp < 2; ++mp) {
            const f32x2 f0 = *(const LAS f32x2*)(smem + FS_OFF + (ml + mp * 32 + r) * 8), f1 = *(const LAS f32x2*)(smem + FS_OFF + (ml + mp * 32 + 16 + r) * 8);
#pragma unroll
            for (int nt = 0; nt < 4; ++nt)
              st16(ro, o + mp * (FH / 16) * 1024 + nt * 1024, pair_rows(pack4(zf[(mp * 2) % MT][nt] * f0[0] + f0[1]), pack4(zf[(mp * 2 + 1) % MT][nt] * f1[0] + f1[1])));
          }
          TS(11);
        }
      }
    }
    TR(part * TP + TP);
    if (PH == G2 || PH == K5) TS(12);
    BARRIER();
    if (PH == G2 || PH == K5) TS(13);
    __builtin_amdgcn_sched_barrier(0);
    if (PH == GA) asm volatile("" :: "v"(tch[0]), "v"(tch[1]));
    if (ROWS) cy >>= 1;
    if (COLS) cx >>= 1;
    half(ONE, IC<TN ? 2 : (C::PARTS > 1)>{}, fa1, fb1, fa0, fb0, 0, 0, none);
    if constexpr (TN > 0) {
      u64 tag;
      f32x2 st[2];
      f32x4 cs[4];
      u32x4 y;
      auto operands = [&](int h, int i) {
        if (PH != GA || i < 12) return;
        if (i < 14) st[i - 12] = *(const LAS f32x2*)(smem + ST_OFF + (h * 64 + ml + (i - 12) * 16) * 16 + (lane & 15) * 16);
        if (!h) cs[i - 12] = *(const LAS f32x4*)(smem + CS1_OFF + (wn * 64 + (i - 12) * 16) * 4 + (lane & 48));
      };
      auto finish = [&](int h) {
        HALF_NOP(h);
        if (h) { TS(2); TSR(14); } else TS(8);
#pragma unroll
        for (int u = 0; u < 4; ++u) {
          if (PH == GA) st16(rw, out_at + h * (2 * D_ / 16 * 1024) + u * 1024, pair_rows(pack4(ht(u, 0, h) * st[0][0] + cs[u] * st[0][1]), pack4(ht(u, 1, h) * st[1][0] + cs[u] * st[1][1])));
          if (PH == K4) st16(rw, out_at + h * 1024 + pair_at(u), pair_rows(pack4(ht(u, 0, h) * unpack4(zk[u & 2][(u & 1) + 2 * h])), pack4(ht(u, 1, h) * unpack4(zk[(u & 2) + 1][(u & 1) + 2 * h]))));
        }
        __builtin_amdgcn_sched_barrier(0);
        if (!h) TS(9);
      };
      auto send = [&](int i) {
        if (!C::SPLIT) return;
        const int u = COLS ? 0x1320 >> (i & 12) & 3 : i >> 2, e = (i & 3) - 1;
        if (e == 0 || e == 1) {
          f32x4& a = ht(u, e, 0);
          asm volatile("" : "+v"(a));
          const u32x2 q = pack4(a); y[2 * e] = q[0]; y[2 * e + 1] = q[1];
          asm volatile("" : "+v"(y));
        }
        if (e == 2) st16(rw, out_at + u * 1024, y);
      };
      auto flag = [&] { __builtin_amdgcn_raw_buffer_store_b64(__builtin_bit_cast(u32x2, tag), rw, flag_at, 0, 16); };
      half(TWO, TWO, fa0, fb0, fa1, fb1, 1, 0, [&](int i) { if (i == 15 && (PH == GA || PH == K4)) residual(); });
      WAIT_L(0);
      if (PH == G2 || PH == K5) TS(15);
      TR(TP + 1); stepm(TWO, 0, [&](int i) { if (i == 15) tag = tag_of(smem); });
      TR(TP + 2); stepm(TWO, 0, none);
      TR(TP + 3);
      BARRIER();
      __builtin_amdgcn_sched_barrier(0);
      half(TWO, TWO, fa1, fb1, fa0, fb0, 0, 0, [&](int i) { operands(0, i); });
      if (!C::SPLIT) finish(0);
      half(TWO, TWO, fa0, fb0, fa1, fb1, 1, 1, send);
      WAIT_L(0);
      TR(TP + 4); stepm(TWO, 1, none);
      WAIT_V(0);
      if (C::SPLIT) flag();
      TR(TP + 5); stepm(TWO, 1, [&](int i) { if (i == 0 && !C::SPLIT) { if (PH == GA) TSR(12); flag(); } });
      TR(TP + 6);
      if (C::SPLIT) seen = __builtin_bit_cast(u64, __builtin_amdgcn_raw_buffer_load_b64(rw, 0, O_SF + ((PH == G2 ? F_XC : F_XD) * 256 + (blockIdx.x ^ 1) * 4 + wid) * sizeof(Flag), 16));
      BARRIER();
      __builtin_amdgcn_sched_barrier(0);
      half(TWO, IC<0>{}, fa1, fb1, fa0, fb0, 0, 1, [&](int i) { operands(1, i + 12); });
      finish(1);
      break;
    }
#pragma unroll
    for (int mt = 0; mt < MT; ++mt) asm volatile("s_nop %4" : "+v"(acc[mt][0]), "+v"(acc[mt][1]), "+v"(acc[mt][2]), "+v"(acc[mt][3]) : "n"(mt ? 0 : 11));
    if (part == C::PARTS - 1) { TS(2); TSR(14); } else TS(8);

    int ln = lane; asm volatile("" : "+v"(ln));
    const int r = ln & 15, j = ln >> 4;
    if (PH == ZZ) {
      f32x4 k[MT], cs[4];
#pragma unroll
      for (int mt = 0; mt < MT; ++mt) k[mt] = *(const LAS f32x4*)(smem + ST_OFF + (ml + mt * 16 + r) * 16);
      if (part == 0) {
#pragma unroll
        for (int nt = 0; nt < 4; ++nt) cs[nt] = *(const LAS f32x4*)(smem + CS3_OFF + (128 + wn * 64 + nt * 16 + j * 4) * 4);
#pragma unroll
        for (int mt = 0; mt < MT; ++mt)
#pragma unroll
          for (int nt = 0; nt < 4; ++nt) acc[mt][nt] += cs[nt] * k[mt][0];
      }
      float sm[4], q[4];
#pragma unroll
      for (int mt = 0; mt < MT; ++mt) {
        f32x4 (&z)[4] = zf[mt];
#pragma unroll
        for (int nt = 0; nt < 4; ++nt) z[nt] = gelu_tanh4(acc[mt][nt], k[mt]);
        if (part == 0) {
          const f32x4 s4 = (z[0] + z[1]) + (z[2] + z[3]), q4 = z[0] * z[0] + z[1] * z[1] + z[2] * z[2] + z[3] * z[3];
          sm[mt] = (s4[0] + s4[1]) + (s4[2] + s4[3]); q[mt] = (q4[0] + q4[1]) + (q4[2] + q4[3]);
        }
      }
      if (part == 0) {
        *(LAS f32x2*)(smem + PS_OFF + (wn * 128 + ml + ln) * 8) = (f32x2){join4(sm[0], sm[1], sm[2], sm[3]), join4(q[0], q[1], q[2], q[3])};
#pragma unroll
        for (int nt = 0; nt < 4; ++nt) cs[nt] = *(const LAS f32x4*)(smem + CS3_OFF + (wn * 64 + nt * 16 + j * 4) * 4);
#pragma unroll
        for (int mt = 0; mt < MT; ++mt)
#pragma unroll
          for (int nt = 0; nt < 4; ++nt) acc[mt][nt] = cs[nt] * k[mt][0];
      }
    }
    if (part < C::PARTS - 1) TS(9);
  }

  asm volatile("" : "+v"(lane));
  const int r = lane & 15, j = lane >> 4;
  const unsigned imgl = (j & 1) * (OW / 32) * 1024u + (j >> 1) * 256 + r * 16;
  if (PH == G2 || PH == K5) {
    {
      TS(10);
      wait_flags(smem, flags(wl, PH == G2 ? F_XC : F_XD, 0) + (blockIdx.x ^ 1) * 4 + wid, seen);
      TS(11);
      u32x4 pv[4];
#pragma unroll
      for (int u = 0; u < 4; ++u) pv[u] = ld16<16>(rw, (out_at ^ 16384) + u * 1024);
#pragma unroll
      for (int u = 0; u < 4; ++u) { u32x2 x0, x1; unpair_rows(rv[u], x0, x1); ht(u, 0) += unpack4(x0); ht(u, 1) += unpack4(x1); }
#pragma unroll
      for (int u = 0; u < 4; ++u) { ht(u, 0) += unpack4((u32x2){pv[u][0], pv[u][1]}); ht(u, 1) += unpack4((u32x2){pv[u][2], pv[u][3]}); }
    }
    u32x2 y[2][4];
#pragma unroll
    for (int u = 0; u < 4; ++u) { y[0][u] = pack4(ht(u, 0)); y[1][u] = pack4(ht(u, 1)); }
    if (PH == K5 && l == 1) {
      const rsrc_t ro = mk_rsrc(p.out, S_ * D_ * 2);
#pragma unroll
      for (int i = 0; i < 4; ++i)
        __builtin_amdgcn_raw_buffer_store_b128(pair_cols(y[i >> 1][(i & 1) * 2], y[i >> 1][(i & 1) * 2 + 1]), ro, ((m0 + (i >> 1) * 16 + r) * D_ + n0 + (i & 1) * 32 + j * 8) * 2, 0, 0);
      if (blockIdx.x == 0 && wid == 0 && lane == 0) __hip_atomic_store((u64*)(p.ws + O_EP), tag_of(smem), RLX_AGENT);
      TS(3);
    } else {
      char* wo = PH == G2 ? wl : wl + LSTRIDE;
      const rsrc_t ro = mk_rsrc(wo + (PH == G2 ? O_X1 : O_X), S_ * D_ * 2);
#pragma unroll
      for (int i = 0; i < 4; ++i) st16(ro, (unsigned)((m0 >> 4) * (OW / 32)) * 1024u + (n0 >> 4) * 512 + pair_at(i) + imgl, pair_rows(y[0][i], y[1][i]));
      asm volatile("" ::: "memory");
      if (PH == G2) {
        float sm[4] = {}, q[4] = {};
#pragma unroll
        for (int i = 0; i < 8; ++i) add_stat(y[i & 1][i >> 1], sm[(i & 1) + (i >> 2) * 2], q[(i & 1) + (i >> 2) * 2]);
        *(LAS f32x2*)(smem + PS_OFF + (wn * 128 + ml + lane) * 8) = (f32x2){join4(sm[0], sm[1], sm[2], sm[3]), join4(q[0], q[1], q[2], q[3])};
        asm volatile("" ::: "memory");
        if (lane == 0) __hip_atomic_fetch_add((LAS unsigned*)(smem + CNT_OFF) + l * 8 + 3, 1u, __ATOMIC_RELAXED, __HIP_MEMORY_SCOPE_WORKGROUP);
        TS(3);
        signal_done<false>(smem, l * 8 + 2, flags(wl, F_G2, t.bm) + t.bn * 2 + t.kh, nullptr);
      } else {
      float sm[2] = {}, q[2] = {};
#pragma unroll
      for (int i = 0; i < 8; ++i) add_stat(y[i & 1][i >> 1], sm[i & 1], q[i & 1]);
      const f32x2 ps = {join32(sm[0], sm[1]), join32(q[0], q[1])};
      if (!(j & 1)) __hip_atomic_store(stat_slot(wo + O_STX, n0 >> 6, m0 + j * 8 + r), __builtin_bit_cast(u64, ps), RLX_AGENT);
      TS(3);
      signal_done<true>(smem, l * 8 + 5, flags(wl, F_K5, t.bm) + t.bn * 2 + t.kh, flags(wo, F_X0, t.bm) + t.bn * 2 + t.kh);
      }
    }
  } else {
    if (PH == ZZ) {
#pragma unroll
      for (int mt = 0; mt < MT; ++mt)
#pragma unroll
        for (int nt = 0; nt < 4; ++nt) zk[mt % 4][nt] = pack4(zf[mt][nt]);
    }
    TS(3);
  }
  TS(6); TSR(5); TS_OUT(PH);
}

template <int N> __device__ __forceinline__ f32x4 sum_of(const f32x4* g) {
  f32x4 a = g[0];
#pragma unroll
  for (int i = 1; i < N; ++i) {
    a += g[i];
  }
  return a;
}
template <int N, int W> __device__ __forceinline__ f32x2 row_stat(const f32x4 (&g)[8]) {
  const f32x4 a = sum_of<N>(g);
  const float mu = (a[0] + a[2]) * (1.0f / W), v = __builtin_fmaf(-mu, mu, (a[1] + a[3]) * (1.0f / W));
  return (f32x2){mu, (v < 0.f ? 0.f : v) + LN_EPS};
}

template <class C> __device__ __forceinline__ Flag* act_flag(char* wl) {
  const Tile<C> t;
  if (C::PH == GA) return flags(wl - LSTRIDE, F_K5, t.bm);
  if (C::PH == G2) return flags(wl, F_GA, t.kh * 8 + t.bn);
  if (C::PH == ZZ) return flags(wl, F_G2, t.bm);
  if (C::PH == K4) return flags(wl, F_Z, t.bn);
  return flags(wl, F_K4, t.bm);
}
template <class C>
__device__ __forceinline__ void act_phase(LAS char* smem, const Params& p, int l, int w, int lane, u64& early) {
  constexpr int PH = C::PH;
  const int wid = 6 + w;
  TS_DECL; TS(0);
  asm volatile("" : "+v"(lane));
  char* wl = p.ws + l * LSTRIDE;
  Stream<C, 1 - C::WSIDE> st(PH == GA && l == 0 ? p.x : wl + (PH == GA ? O_X : (PH == G2 ? O_AB : (PH == ZZ ? O_X1 : (PH == K4 ? O_Z2 : O_H)))),
                             S_ * 2 * (PH == G2 ? 2 * D_ : (PH == K4 || PH == K5 ? FH : D_)), l, w, lane, 0, 0);
  const unsigned fo = (lane & 15) * sizeof(Flag), all = PH == K5 ? 0x7fu << (blockIdx.x & 1) * 7 : 0xffffu;
#define POLL_WAIT 6
#define IDLE_GAP 8
  if (PH != GA || l) {
    const u64 tag = tag_of(smem);
    Flag* fl = act_flag<C>(wl);
    if (PH != K4 || !__all(early == tag)) {
      if (PH == GA || PH == ZZ) { wait_count(smem, PH == GA ? 5 : l * 8 + 2, 4); __builtin_amdgcn_s_sleep(POLL_WAIT); }
      else if (PH == K5 && (blockIdx.x >> 3) >= 28) poll_flags<IDLE_GAP>(tag, fl, fo, st.writers(0));
      poll_flags<0>(tag, fl, fo, st.writers(0) | st.writers(1) | st.writers(2) | st.writers(3));
    }
    TS(1); TSR(9);
    st.fill(smem, 3);
    TSR(11);
    u64 seen;
    auto check = [&] {
      LANDED1(st.NP, seen);
      const unsigned miss = (unsigned)__builtin_amdgcn_ballot_w64(seen != tag) & all;
      if (miss) poll_flags<0>(tag, fl, fo, miss);
    };
    ld8_async(seen, fl, fo);
    WAIT_V(2 * st.NP + 1);
    BARRIER();
    WAIT_V(st.NP + 1);
    st.template step<1>(smem, [] {}, true);
    st.template step<0>(smem, check, true);
    if (PH == K5) {
      fl += (F_KB - F_K4) * 256;
      st.template step<0>(smem, [] {}, true);
      st.template step<1>(smem, [&] { ld8_async(seen, fl, fo); }, true);
      st.template step<0>(smem, check, false);
    }
  } else {
    TS(1); TSR(9);
    st.fill(smem, 1);
    WAIT_V(0);
    BARRIER();
    st.fill(smem, 3);
    WAIT_V(st.NP);
  }
  TS(2);
  st.finish(smem, [&] { if (PH == ZZ) early = peek(act_flag<CK4>(wl)[lane & 15].v); });
  TSR(8); TS(3); TS_OUT(PH);
}

template <class C> __device__ __forceinline__ Stream<C, C::WSIDE> wt_stream(const Params& p, int l, int w, int lane, unsigned slot, int n) {
  constexpr int PH = C::PH;
  const Tile<C> t;
  return Stream<C, C::WSIDE>(PH == GA ? (t.bn >= 8 ? p.wdim : p.wdre) : (PH == G2 ? (t.kh ? p.wsim : p.wsre) : (PH == ZZ ? p.win : (PH == K4 ? p.wsp : p.wout))),
                             2 * (PH == GA ? D_ * D_ : (PH == ZZ ? D_ * F_ : (PH == K5 ? FH * D_ : S_ * S_))), l, w, lane, slot, n);
}
template <class C, class NEXT>
__device__ __forceinline__ void wt_phase(LAS char* smem, const Params& p, int l, int w, int lane, unsigned& wpos, bool mid) {
  constexpr int PH = C::PH, AH = Stream<C, C::WSIDE>::AHEAD % 10;
  asm volatile("" : "+v"(lane));
  const Tile<C> t;
  char* wl = p.ws + l * LSTRIDE;
  auto st = wt_stream<C>(p, l, w, lane, wpos, (PH == GA && l == 0) || (PH == K5 && !mid) ? 0 : AH);
  const int row = w * 64 + lane;
  const unsigned fo = (lane & 15) * sizeof(Flag);
  u64 seen;
  f32x4 g[8];
  if (PH == GA && l == 0) {
    st.fill(smem, 2); WAIT_V(8); BARRIER();
    st.fill(smem, 4); WAIT_V(16); BARRIER();
#pragma unroll 1
    for (int k = 6; k <= 8; k += 2) { st.fill(smem, k); WAIT_V(24); BARRIER(); }
  } else {
    if (PH == ZZ && l == 0) *(LAS unsigned*)(smem + BS_OFF + row * 4) = (unsigned)p.bsp[t.bm * 128 + row] << 16;
    if (PH == K5) st.fill(smem, AH);
    st.resume(smem);
  }
  if (PH == ZZ) {
    Flag* fl = flags(wl, F_WI, t.bn);
    st.go(smem, 2);
    ld8_async(seen, flags(wl, F_S1 + w, t.bm), fo);
    st.go(smem, 4);
    LANDED1(16, seen);
    if (l == 0 && w == 0 && lane == 0) __hip_atomic_store(fl[t.bm].v, tag_of(smem), RLX_AGENT);
    wait_flags(smem, flags(wl, F_S1 + w, t.bm) + (lane & 15), seen);
#pragma unroll
    for (int i = 0; i < 8; ++i) ld16_async(g[i], wl + O_ST1 + (i * S_ + t.bm * 128) * 16, row * 16);
    st.template skip<8, 1>();
    st.template go<8, 1>(smem, 7);
    LANDED8(16, g);
    {
      const f32x2 m = row_stat<8, D_>(g);
      const float rs = __builtin_amdgcn_rsqf(m[1]);
      *(LAS f32x4*)(smem + ST_OFF + row * 16) = (f32x4){-m[0], GC1 * rs, GC2 * rs * rs * rs, m[1] * rs};
    }
    st.template skip<0, 2>();
    if (l == 0) {
      ld8_async(seen, fl, fo);
      st.template go<0, 2>(smem, 10);
      LANDED1(16, seen);
      wait_flags(smem, fl + (lane & 15), seen);
#pragma unroll
      for (int i = 0; i < 8; ++i) ld16_async(g[i], p.ws + O_SI + (i * F_ + w * FH + t.bn * 128) * 8, lane * 16);
      st.template skip<8, 3>();
      st.template go<8, 3>(smem, 12);
      st.template go<0, 3>(smem, 13);
      LANDED8(16, g);
      const f32x4 a = sum_of<8>(g);
      *(LAS f32x2*)(smem + CS3_OFF + row * 8) = (f32x2){a[0] + a[2], a[1] + a[3]};
      st.template go<0, 3>(smem, 16);
    } else st.template go<0, 2>(smem, 16);
    st.fill(smem, 22);
    auto nx = wt_stream<NEXT>(p, l, w, lane, wpos + 32768, 0);
    asm volatile("" : "+v"(nx.vo[0]), "+v"(nx.vo[1]), "+v"(nx.vo[2]), "+v"(nx.vo[3]));
    st.template skip<0, 0>();
    {
      const u32x2 q = __builtin_bit_cast(u32x2, *(const LAS f32x2*)(smem + PS_OFF + row * 8) + *(const LAS f32x2*)(smem + PS_OFF + (128 + row) * 8));
      const u32x2 v = {q[0] < 0xfffffffeu ? q[0] : 0xfffffffeu, q[1] < 0xfffffffeu ? q[1] : 0xfffffffeu};
      __hip_atomic_store(stat_slot(wl + O_XCH, t.bn, t.bm * 128 + row), __builtin_bit_cast(u64, v), RLX_AGENT);
    }
    static_assert(NORM_S == 8, "");
    st.template skip<1, 1>();
    st.template go<1, 1>(smem, 20);
#pragma unroll
    for (int i = 0; i < 7; ++i) ld16_async(g[i], wl + O_XCH + (i * S_ + t.bm * 128) * 16, row * 16);
    st.template skip<7, 2>();
    st.template go<7, 2>(smem, 22);
    LANDED8(8, g);
    f32x2 m = row_stat<7, FH>(g);
    if (!__all(m[1] == m[1])) {
#pragma unroll 1
      for (unsigned spin = 0; spin < SPIN_MAX; ++spin) {
#pragma unroll
        for (int i = 0; i < 7; ++i) ld16_async(g[i], wl + O_XCH + (i * S_ + t.bm * 128) * 16, row * 16);
        LANDED8(0, g);
        unsigned mx = 0;
#pragma unroll
        for (int i = 0; i < 7; ++i) { const u32x4 u = __builtin_bit_cast(u32x4, g[i]); mx = max(max(mx, max(u[0], u[1])), max(u[2], u[3])); }
        if (__all(mx != 0xffffffffu)) break;
      }
      m = row_stat<7, FH>(g);
    }
    const float rs = __builtin_amdgcn_rsqf(m[1]);
    *(LAS f32x2*)(smem + FS_OFF + row * 8) = (f32x2){rs, -m[0] * rs};
    st.template skip<0, 3>();
    st.fill(smem, 27);
    st.template go<0, 1>(smem, 28);
    if (w == 0 && lane == 0) __hip_atomic_store(flags(wl, F_Z, t.bn)[t.bm].v, tag_of(smem), RLX_AGENT);
    nx.template go<0, 1>(smem, 0);
    nx.fill(smem, 6);
  } else {
    f32x4 c[4];
    if (PH == G2) {
      Flag* fl = flags(wl, F_GB, t.kh * 8 + t.bn);
      st.go(smem, 4);
      const Tile<CGA> ta;
      if (w == 0 && lane == 0) __hip_atomic_store(flags(wl, F_GB, ta.bn)[ta.bm].v, tag_of(smem), RLX_AGENT);
      st.go(smem, 7);
      ld8_async(seen, fl, fo);
      st.go(smem, 9);
      LANDED1(16, seen);
      wait_flags(smem, fl + (lane & 15), seen);
    } else if (PH == K5) {
      st.go(smem, 2);
      const Tile<CK4> tk;
      if (mid && w == 0 && lane == 0) __hip_atomic_store(flags(wl, F_KB, tk.bm)[tk.bn].v, tag_of(smem), RLX_AGENT);
    } else if (PH == GA) {
      if (l == 0) {
        st.go(smem, 4);
        const int own = (F_X0 * 16 + t.bm) * 16 + t.bn + w * ((F_WD - F_X0) * 256 + (t.bn - t.bm) * 15);
        if (lane == 0) __hip_atomic_store(flags(wl, 0, 0)[own].v, tag_of(smem), RLX_AGENT);
      }
      const unsigned fa = ((lane & 16) && l == 0 ? F_WD * 16 + t.bn : F_X0 * 16 + t.bm) * 16 * sizeof(Flag) + fo;
      st.go(smem, l ? 2 : 7);
      ld8_async(seen, wl + O_SF, fa);
      st.go(smem, l ? 4 : 9);
      LANDED1(16, seen);
      wait_flags(smem, (Flag*)(wl + O_SF + fa), seen);
      if (l == 0) {
#pragma unroll
        for (int i = 0; i < 4; ++i) ld16_async(c[i], p.ws + O_SD + (i * 2048 + t.bn * 128) * 16, row * 16);
      }
#pragma unroll
      for (int i = 0; i < 8; ++i) ld16_async(g[i], wl + O_STX + (i * S_ + t.bm * 128) * 16, row * 16);
    }
    st.close(smem);
    if (PH == GA) {
      LANDED8(16, g);
      if (l == 0) {
        asm volatile("" : "+v"(c[0]), "+v"(c[1]), "+v"(c[2]), "+v"(c[3]));
        const f32x4 a = sum_of<4>(c);
        *(LAS float*)(smem + CS1_OFF + row * 4) = (a[0] + a[1]) + (a[2] + a[3]);
      }
      g[0] = sum_of<8>(g);
      asm volatile("" : "+v"(g[0]));
    }
    st.template skip<0, 4>();
    if (PH == GA) {
      asm volatile("" : "+v"(g[0]));
      const f32x2 m = row_stat<1, D_>(g);
      const float rs = __builtin_amdgcn_rsqf(m[1]);
      *(LAS f32x2*)(smem + ST_OFF + row * 16) = (f32x2){rs, -m[0] * rs} * (t.bn >= 8 ? -1.0f : 1.0f);
    }
    st.template skip<0, 4>();
    if (PH == G2 ? mid : PH != K5 || l == 0) wt_stream<NEXT>(p, 1, w, lane, st.slot, 0).open(smem);
    else { BARRIER(); BARRIER(); BARRIER(); }
    if (PH == G2) {
      wait_count(smem, l * 8 + 3, 4);
      const f32x2 q = *(const LAS f32x2*)(smem + PS_OFF + row * 8) + *(const LAS f32x2*)(smem + PS_OFF + (128 + row) * 8);
      __hip_atomic_store(stat_slot(wl + O_ST1, t.bn * 2 + t.kh, t.bm * 128 + row), __builtin_bit_cast(u64, q), RLX_AGENT);
      const u64 tag = tag_of(smem);
      WAIT_V(0);
      if (lane == 0) __hip_atomic_store(flags(wl, F_S1 + w, t.bm)[t.bn * 2 + t.kh].v, tag, RLX_AGENT);
    }
  }
  wpos = st.slot;
}

__device__ __forceinline__ void touch(LAS char* smem, const Params& p, int wid, int lane) {
  const int u = ((blockIdx.x >> 3) - 28 + (blockIdx.x & 7) * 4) * 8 + wid;
  BARRIER();
#pragma unroll 1
  for (int i = 0; i < 46; i += 2) {
    const char* g = (i < 32 ? p.wsp + (size_t)(u * 32 + i) * 1024 : p.wout + (size_t)(u * 14 + i - 32) * 1024) + lane * 16;
    __builtin_amdgcn_global_load_lds((const unsigned*)g, (LAS unsigned*)(smem + A_RING + wid * 1024), 16, 0, 0);
    __builtin_amdgcn_global_load_lds((const unsigned*)(g + 1024), (LAS unsigned*)(smem + A_RING + wid * 1024), 16, 0, 0);
    WAIT_V(8);
  }
  __builtin_amdgcn_s_waitcnt(0x0f70);
  BARRIER();
}

__global__ __launch_bounds__(512, 2) void mega_k(Params p) {
  extern __shared__ __attribute__((aligned(16))) char smem_[];
  LAS char* smem = (LAS char*)smem_;
  const int lane = threadIdx.x & 63, wid = __builtin_amdgcn_readfirstlane(threadIdx.x >> 6);
  const bool mid = (blockIdx.x >> 3) < 28;
  unsigned wpos = 0;
  if (wid < 4) {
    __builtin_amdgcn_s_waitcnt(0x0f70);
#pragma unroll 1
    for (int l = 0; l < 2; ++l) {
      u32x2 zk[4][4];
      u32x4 rv[4];
      compute_phase<CGA>(smem, p, l, wid, lane, zk, rv, wpos);
      compute_phase<CG2>(smem, p, l, wid, lane, zk, rv, wpos);
      if (mid) { compute_phase<CZZ>(smem, p, l, wid, lane, zk, rv, wpos); compute_phase<CK4>(smem, p, l, wid, lane, zk, rv, wpos); }
      else if (l == 0) touch(smem, p, wid, lane);
      compute_phase<CK5>(smem, p, l, wid, lane, zk, rv, wpos);
    }
  } else if (wid < 6) {
#pragma unroll 1
    for (int l = 0; l < 2; ++l) {
      wt_phase<CGA, CG2>(smem, p, l, wid - 4, lane, wpos, mid);
      wt_phase<CG2, CZZ>(smem, p, l, wid - 4, lane, wpos, mid);
      if (mid) { wt_phase<CZZ, CK4>(smem, p, l, wid - 4, lane, wpos, mid); wt_phase<CK4, CK5>(smem, p, l, wid - 4, lane, wpos, mid); }
      else if (l == 0) touch(smem, p, wid, lane);
      wt_phase<CK5, CGA>(smem, p, l, wid - 4, lane, wpos, mid);
    }
  } else {
#pragma unroll 1
    for (int l = 0; l < 2; ++l) {
      u64 early = 0;
      act_phase<CGA>(smem, p, l, wid - 6, lane, early);
      act_phase<CG2>(smem, p, l, wid - 6, lane, early);
      if (mid) { act_phase<CZZ>(smem, p, l, wid - 6, lane, early); act_phase<CK4>(smem, p, l, wid - 6, lane, early); }
      else if (l == 0) touch(smem, p, wid, lane);
      act_phase<CK5>(smem, p, l, wid - 6, lane, early);
    }
  }
}

extern "C" void kernel_launch(void* const* d_in, const int* in_sizes, int n_in, void* d_out, int out_size, void* d_ws, size_t ws_size, hipStream_t stream) {
  Params p;
  p.x = (const char*)d_in[0];
  p.wsre = (const char*)d_in[1]; p.wsim = (const char*)d_in[2]; p.wdre = (const char*)d_in[3]; p.wdim = (const char*)d_in[4];
  p.win = (const char*)d_in[6]; p.wsp = (const char*)d_in[7]; p.wout = (const char*)d_in[9]; p.bsp = (const u16*)d_in[8];
  p.out = (char*)d_out; p.ws = (char*)d_ws;
  (void)hipFuncSetAttribute((const void*)mega_k, hipFuncAttributeMaxDynamicSharedMemorySize, LDS_BYTES);
  hipLaunchKernelGGL(mega_k, dim3(256), dim3(512), LDS_BYTES, stream, p);
}
```

```cpp
#include <hip/hip_runtime.h>
#include <hip/hip_bf16.h>
#include <stdint.h>

#define LAS __attribute__((address_space(3)))
typedef unsigned short u16;
typedef short s16x4 __attribute__((ext_vector_type(4)));
typedef short s16x8 __attribute__((ext_vector_type(8)));
typedef __bf16 bf16x8 __attribute__((ext_vector_type(8)));
typedef __bf16 bf16x2 __attribute__((ext_vector_type(2)));
typedef float f32x2 __attribute__((ext_vector_type(2)));
typedef float f32x4 __attribute__((ext_vector_type(4)));
typedef unsigned u32x2 __attribute__((ext_vector_type(2)));
typedef unsigned u32x4 __attribute__((ext_vector_type(4)));

constexpr int S_ = 2048, D_ = 1024, F_ = 3584, FH = 1792;
constexpr float LN_EPS = 1e-5f;

__device__ __forceinline__ unsigned pack_bf16(float lo, float hi) {
  f32x2 v = {lo, hi};
  return __builtin_bit_cast(unsigned, __builtin_convertvector(v, bf16x2));
}
__device__ __forceinline__ float bf_lo(unsigned u) { return __builtin_bit_cast(float, u << 16); }
__device__ __forceinline__ float bf_hi(unsigned u) { return __builtin_bit_cast(float, u & 0xffff0000u); }
__device__ __forceinline__ void swap16(unsigned& a, unsigned& b) { asm volatile("s_nop 1\n\tv_permlane16_swap_b32 %0, %1" : "+v"(a), "+v"(b)); }
__device__ __forceinline__ void swap32(unsigned& a, unsigned& b) { asm volatile("s_nop 2\n\tv_permlane32_swap_b32 %0, %1" : "+v"(a), "+v"(b)); }
__device__ __forceinline__ float sum32(float v) {
  unsigned a = __builtin_bit_cast(unsigned, v), b = a; swap32(a, b);
  return __builtin_bit_cast(float, a) + __builtin_bit_cast(float, b);
}
__device__ __forceinline__ float sum_groups(float v) {
  unsigned a = __builtin_bit_cast(unsigned, v), b = a; swap16(a, b);
  return sum32(__builtin_bit_cast(float, a) + __builtin_bit_cast(float, b));
}
#define CVT4 "v_cvt_pk_bf16_f32 %0, %4, %5\n\tv_cvt_pk_bf16_f32 %2, %8, %9\n\tv_cvt_pk_bf16_f32 %1, %6, %7\n\tv_cvt_pk_bf16_f32 %3, %10, %11\n\t"
#define CVT4_ARGS "=&v"(a0), "=&v"(a1), "=&v"(b0), "=&v"(b1) : "v"(a[0]), "v"(a[1]), "v"(a[2]), "v"(a[3]), "v"(b[0]), "v"(b[1]), "v"(b[2]), "v"(b[3])
__device__ __forceinline__ u32x4 pair_rows(f32x4 a, f32x4 b) {
  unsigned a0, a1, b0, b1;
  asm volatile(CVT4 "v_permlane16_swap_b32 %0, %2\n\ts_nop 0\n\tv_permlane16_swap_b32 %1, %3" : CVT4_ARGS);
  return (u32x4){a0, a1, b0, b1};
}
__device__ __forceinline__ u32x4 pair_cols(f32x4 a, f32x4 b) {
  unsigned a0, a1, b0, b1;
  asm volatile(CVT4 "v_permlane32_swap_b32 %0, %2\n\ts_nop 0\n\tv_permlane32_swap_b32 %1, %3\n\tv_permlane16_swap_b32 %0, %2\n\ts_nop 0\n\tv_permlane16_swap_b32 %1, %3" : CVT4_ARGS);
  return (u32x4){a0, a1, b0, b1};
}
__device__ __forceinline__ u32x2 pack4(f32x4 v) { return (u32x2){pack_bf16(v[0], v[1]), pack_bf16(v[2], v[3])}; }
__device__ __forceinline__ f32x4 unpack4(u32x2 y) { return (f32x4){bf_lo(y[0]), bf_hi(y[0]), bf_lo(y[1]), bf_hi(y[1])}; }
constexpr float GC1 = -2.0f * 0.7978845608028654f * 1.4426950408889634f, GC2 = GC1 * 0.044715f;
__device__ __forceinline__ f32x4 gelu_tanh4(f32x4 a, f32x4 k) {
  const f32x4 t = a * (a * a * k[2] + k[1]);
  f32x4 e;
#pragma unroll
  for (int i = 0; i < 4; ++i) e[i] = __builtin_amdgcn_exp2f(t[i]);
  e = e * k[3] + k[3];
#pragma unroll
  for (int i = 0; i < 4; ++i) e[i] = __builtin_amdgcn_rcpf(e[i]);
  return a * e;
}
#define STAUX 16
typedef __amdgpu_buffer_rsrc_t rsrc_t;
__device__ __forceinline__ rsrc_t mk_rsrc(const void* p, unsigned bytes) { return __builtin_amdgcn_make_buffer_rsrc((void*)p, 0, bytes, 0x00020000); }
__device__ __forceinline__ void st16(rsrc_t r, unsigned off, u32x4 v) { __builtin_amdgcn_raw_buffer_store_b128(v, r, off, 0, STAUX); }
template <int AUX> __device__ __forceinline__ u32x4 ld16(rsrc_t r, unsigned off) { return __builtin_amdgcn_raw_buffer_load_b128(r, off, 0, AUX); }
#define WAIT_V(n) asm volatile("s_waitcnt vmcnt(%0)" ::"n"(n) : "memory")
#define WAIT_L(n) __builtin_amdgcn_s_waitcnt(0xc07f | ((n) << 8))
#define WAIT_VL(n) asm volatile("s_waitcnt vmcnt(%0) lgkmcnt(0)" ::"n"(n) : "memory")
#define WAIT_L0() asm volatile("s_waitcnt lgkmcnt(0)" ::: "memory")
#define BARRIER() do { asm volatile("" ::: "memory"); __builtin_amdgcn_s_barrier(); asm volatile("" ::: "memory"); } while (0)
#define PIN_S(x) asm volatile("" : "+s"(x))

#define RLX_AGENT __ATOMIC_RELAXED, __HIP_MEMORY_SCOPE_AGENT
#define PF_THR 8
#define POLL_SLEEP 1
#define NORM_S 8
#define SIGA_S 4
#define LAT_S 1
#define ZLAG 1
#define WAUX 16
#define AAUX 16
#define ZLAT 10
#define ROT 0
#define G2_ACK 4
#define GS0 7
#define GS1 2
#define LATE_G 8
#define LATE_K 8
#define SEAM_G 2
#define SEAM_K 2
#define ZK 0
#define KEEP0 0
#define KEEPD 2
#define GL 0
#define PRE 1
#define DOTADD 1
#define ZORD 1
#define ZPOLL 0
#define TZ 10
#define TZN 32
#define TZD 8
#define PORD 1
#define PDEP 1
#define ZNAT 1
#define TA 6
#define TAM 0
static_assert(ZORD && GS0 >= 4 && GS0 >= TA + TAM && GS0 <= 7, "");
#define WTAB 2
#define W0 3
#define W1 5
#define FLAGL 0
#define POLL_K5 2
#define PULL 1
#define ZEARLY 1
#define ZA 1
#define ZB 3
constexpr unsigned SPIN_MAX = 1u << 18;
typedef unsigned long long u64;
template <int CTRL> __device__ __forceinline__ float dpp_add(float v) {
  return v + __builtin_bit_cast(float, __builtin_amdgcn_update_dpp(0, __builtin_bit_cast(int, v), CTRL, 0xf, 0xf, true));
}
__device__ __forceinline__ float sum8(float v) { return dpp_add<0x141>(dpp_add<0x4e>(dpp_add<0xb1>(v))); }

constexpr size_t MB = 1 << 20, LSTRIDE = 48 * MB;
constexpr size_t O_X = 0, O_X1 = 4 * MB, O_AB = 8 * MB, O_Z2 = 16 * MB, O_H = 24 * MB;
constexpr size_t O_XG = 32 * MB, O_XK = 40 * MB;
constexpr size_t O_STX = 36 * MB, O_ST1 = O_STX + 256 * 1024;
constexpr size_t O_XCH = O_STX + 512 * 1024;
constexpr size_t O_SF = O_STX + 768 * 1024;
constexpr size_t O_SD = 2 * LSTRIDE, O_SI = O_SD + 128 * 1024;
constexpr size_t O_EP = O_SD + 512 * 1024;
enum { F_X0, F_WD, F_GA, F_GB, F_G2, F_S1, F_S1B, F_Z, F_K4, F_KB, F_RD, F_K5 };
constexpr unsigned ZS_END = 160 * 1024, W_RING = 96 * 1024, A_RING = W_RING, A_BYTES = 48 * 1024, CS1_OFF = 144 * 1024, CS3_OFF = CS1_OFF + 512, ST_OFF = CS3_OFF + 1024, PS_OFF = ST_OFF + 2048,
                   BS_OFF = PS_OFF + 2048, CP_OFF = ZS_END - 1024, CNT_OFF = BS_OFF + 512, EP_OFF = CNT_OFF + 128, ZS_OFF = EP_OFF + 128;
constexpr unsigned LDS_BYTES = ZS_END;
static_assert(ZS_OFF + 8192 <= CP_OFF, "");

enum { GA = 0, G2 = 1, ZZ = 2, K4 = 3, K5 = 4 };

struct Params {
  const char *x, *wsre, *wsim, *wdre, *wdim, *win, *wsp, *wout;
  const u16* bsp;
  char* out;
  char* ws;
};

template <int PH_, int BM_, int BN_, int WM_, int WN_, int AM_, int BMODE_, int T_, int PARTS_, int TN_, int XN_, int PA_, int PB_, int WSIDE_>
struct Cfg {
  static constexpr int PH = PH_, BM = BM_, BN = BN_, WM = WM_, WN = WN_, AM = AM_, BMODE = BMODE_, T = T_, PARTS = PARTS_;
  static constexpr int TN = TN_, TJ = T_ - TN_, XM = 4, XN = XN_, PA = PA_, PB = PB_, WSIDE = WSIDE_;
  static constexpr int MT = BM / WM / 16, NT = BN / WN / 16;
  static constexpr int SEAM = PH_ == GA || PH_ == G2 ? SEAM_G : SEAM_K;
  static constexpr unsigned A0 = (SEAM != 1 ? 0 : (PH_ == G2 ? 1 : (PH_ == K5 ? 2 : 0))) * 16384;
  static constexpr bool SPLIT = PH_ == G2 || PH_ == K5;
};
#define GA_TN 6
#define G2_TN 6
#define K4_TN 6
#define K5_TN 6
#define GA_FS 2
#define GA_HK 6
using CGA = Cfg<GA, 128, 128, 2, 2, 1, 0, 16, 1, GA_TN, 8, D_ / 32, 2048,    1>;
using CG2 = Cfg<G2, 128, 128, 2, 2, 0, 2, 32, 1, G2_TN, 8, 4096,    D_ / 16, 0>;
using CZZ = Cfg<ZZ, 128, 128, 2, 2, 1, 1, 32, 2, 0,     7, D_ / 32, F_ * 2,  1>;
using CK4 = Cfg<K4, 128, 128, 2, 2, 0, 2, 32, 1, K4_TN, 7, 4096,    FH / 16, 0>;
using CK5 = Cfg<K5, 128, 128, 2, 2, 1, 1, 14, 1, K5_TN, 8, FH / 32, D_ * 2,  1>;
template <int V> struct IC { static constexpr int v = V; };
struct PairRing { static constexpr unsigned SLOT = 32768, D = 3; };

template <class C> struct Tile {
  int bm, bn, kh, cls, ln;
  __device__ __forceinline__ Tile() {
    int bid = blockIdx.x; PIN_S(bid);
    const int xcd = bid & 7, lm = bid >> 6;
    ln = bid >> 3 & 7;
    bm = (xcd >> 1) * C::XM + lm; bn = C::SPLIT ? ln : (xcd & 1) * C::XN + ln; kh = C::SPLIT ? xcd & 1 : 0; cls = (lm + ln) & 3;
  }
};
template <class C, int SIDE> struct Ring {
  static constexpr bool ISW = C::WSIDE == SIDE;
  static constexpr unsigned SLOT = (SIDE ? C::BN : C::BM) * 128, BASE = ISW ? 0 : A_RING, D = (ISW ? W_RING : A_BYTES) / SLOT;
};

#define FW 32
struct Flag { u64 v[FW]; };
__device__ __forceinline__ Flag* flags(char* wl, int id, int panel) { return (Flag*)(wl + O_SF) + (id * 16 + panel) * 16; }
__device__ __forceinline__ u64 tag_of(LAS char* smem) { return *(const volatile LAS u64*)(smem + EP_OFF); }
template <int N, bool LATE> __device__ __forceinline__ void signal_done(LAS char* smem, int slot, Flag* flag, Flag* late) {
  const u64 tag = tag_of(smem);
  unsigned old;
#define COUNT_IN(off) "s_mov_b64 exec, 1\n\tds_add_rtn_u32 %0, %1, %2 offset:" #off "\n\ts_waitcnt lgkmcnt(0)\n\tv_cmp_eq_u32 vcc, 3, %0\n\ts_and_b64 exec, exec, vcc\n\t"
#define FIRST_FLAG "s_waitcnt vmcnt(%7)\n\t" COUNT_IN(0) "s_cbranch_scc0 .Lsig_a%=\n\tglobal_store_dwordx2 %3, %4, %5 sc1\n\ts_waitcnt vmcnt(1)\n\ts_branch .Lsig_b%=\n" \
                   ".Lsig_a%=:\n\ts_waitcnt vmcnt(0)\n.Lsig_b%=:\n\t" COUNT_IN(4)
  if (LATE)
    asm volatile(FIRST_FLAG "s_cbranch_scc0 .Lsig_c%=\n\tglobal_store_dwordx2 %3, %4, %6 sc1\n.Lsig_c%=:\n\ts_mov_b64 exec, -1"
                 : "=&v"(old) : "v"((unsigned)(size_t)(smem + CNT_OFF) + slot * 4), "v"(1), "v"(0), "v"(tag), "s"(flag), "s"(late), "n"(N) : "vcc", "scc", "memory");
  else
    asm volatile("s_waitcnt vmcnt(0)\n\t" COUNT_IN(0) "s_cbranch_scc0 .Lsig_a%=\n\tglobal_store_dwordx2 %3, %4, %5 sc1\n.Lsig_a%=:\n\ts_mov_b64 exec, -1"
                 : "=&v"(old) : "v"((unsigned)(size_t)(smem + CNT_OFF) + slot * 4), "v"(1), "v"(0), "v"(tag), "s"(flag) : "vcc", "scc", "memory");
}
__device__ __forceinline__ u64 peek(u64* f) { return __hip_atomic_load(f, RLX_AGENT); }
__device__ __forceinline__ void ld8_async(u64& d, const void* sbase, unsigned voff) { asm volatile("s_nop 4\n\tglobal_load_dwordx2 %0, %1, %2 sc1" : "=v"(d) : "v"(voff), "s"(sbase) : "memory"); }
#define LCNT(n) (n)
#define LANDED1(n, a) asm volatile("s_waitcnt vmcnt(%1)" : "+v"(a) : "n"(LCNT(n)) : "memory")
__device__ __forceinline__ void ld16_async(f32x4& d, const void* sbase, unsigned voff) { asm volatile("s_nop 4\n\tglobal_load_dwordx4 %0, %1, %2 sc1" : "=v"(d) : "v"(voff), "s"(sbase) : "memory"); }
#define LANDED8(n, g) asm volatile("s_waitcnt vmcnt(%8)" : "+v"(g[0]), "+v"(g[1]), "+v"(g[2]), "+v"(g[3]), "+v"(g[4]), "+v"(g[5]), "+v"(g[6]), "+v"(g[7]) : "n"(LCNT(n)) : "memory")
__device__ __forceinline__ u64* stat_slot(char* base, int g, int row) { return (u64*)base + ((g >> 1) * S_ + row) * 2 + (g & 1); }
__device__ __forceinline__ void wait_flags(u64 tag, Flag* f, u64 first) {
  if (!__all(first == tag)) {
#pragma unroll 1
    for (unsigned spin = 0; spin < SPIN_MAX; ++spin) {
      if (__all(peek(f->v) == tag)) break;
      if (POLL_SLEEP) __builtin_amdgcn_s_sleep(POLL_SLEEP);
    }
  }
  asm volatile("" ::: "memory");
}

template <int GAP> __device__ __forceinline__ void poll_flags(u64 tag, const void* base, unsigned off, unsigned mask) {
  unsigned left = SPIN_MAX;
  u64 q;
  asm volatile("s_mov_b32 exec_lo, %5\n\ts_mov_b32 exec_hi, 0\n\ts_nop 4\n"
               ".Lpoll_next%=:\n\tglobal_load_dwordx2 %0, %2, %3 sc1\n\ts_waitcnt vmcnt(0)\n\tv_cmp_ne_u64 vcc, %4, %0\n\ts_and_b64 exec, exec, vcc\n\t"
               "s_cbranch_scc0 .Lpoll_done%=\n\ts_sleep %6\n\ts_sub_u32 %1, %1, 1\n\ts_cbranch_scc0 .Lpoll_next%=\n"
               ".Lpoll_done%=:\n\ts_mov_b64 exec, -1"
               : "=&v"(q), "+s"(left) : "v"(off), "s"(base), "v"(tag), "s"(__builtin_amdgcn_readfirstlane(mask)), "n"(GAP) : "vcc", "scc", "memory");
}
__device__ __forceinline__ int poll_past(u64 tag, const void* base, unsigned off, unsigned mask, int bars) {
  unsigned left = SPIN_MAX;
  u64 q;
  asm volatile("s_mov_b32 exec_lo, %6\n\ts_mov_b32 exec_hi, 0\n\ts_nop 4\n"
               ".Lpast_next%=:\n\tglobal_load_dwordx2 %0, %3, %4 sc1\n\ts_cmp_eq_u32 %2, 0\n\ts_cbranch_scc1 .Lpast_wait%=\n\ts_sub_u32 %2, %2, 1\n\ts_barrier\n"
               ".Lpast_wait%=:\n\ts_waitcnt vmcnt(0)\n\tv_cmp_ne_u64 vcc, %5, %0\n\ts_and_b64 exec, exec, vcc\n\ts_cbranch_scc0 .Lpast_done%=\n\t"
               "s_sub_u32 %1, %1, 1\n\ts_cbranch_scc0 .Lpast_next%=\n"
               ".Lpast_done%=:\n\ts_mov_b64 exec, -1"
               : "=&v"(q), "+s"(left), "+s"(bars) : "v"(off), "s"(base), "v"(tag), "s"(__builtin_amdgcn_readfirstlane(mask)) : "vcc", "scc", "memory");
  return bars;
}
__device__ __forceinline__ void wait_count(LAS char* smem, int slot, unsigned n) {
#pragma unroll 1
  for (unsigned spin = 0; spin < 64 * SPIN_MAX; ++spin) {
    if (*((const volatile LAS unsigned*)(smem + CNT_OFF) + slot) >= n) break;
    __builtin_amdgcn_s_sleep(1);
  }
  asm volatile("" ::: "memory");
}

struct Lanes { unsigned vo[4], tab, from; };
template <class C, int SIDE> struct Stream {
  using R = Ring<C, SIDE>;
  static constexpr int NP = 8, T = C::PH == ZZ && !R::ISW ? 16 : C::T, TJ = T - C::TN, D = R::D, MODE = SIDE ? C::BMODE : (C::AM ? 3 : 0), PITCH = SIDE ? C::PB : C::PA;
  static constexpr unsigned KS = MODE == 0 ? 128 : (MODE == 1 ? 64 * PITCH : (MODE == 2 ? 2 * PITCH * 1024 : 2048));
  static constexpr unsigned HALF = MODE == 0 ? 64 * PITCH : (MODE == 1 ? 128 : (MODE == 2 ? 2048 : 4 * PITCH * 1024));
  static constexpr unsigned P4 = MODE == 0 ? 32 * PITCH : (MODE == 1 ? 64 * PITCH : (MODE == 2 ? PITCH * 1024 : 2 * PITCH * 1024));
  static constexpr bool TWO = C::TN && !R::ISW;
  static constexpr int LAT = C::PH == ZZ ? ZLAT : LAT_S;
  static_assert(R::SLOT == 16384 && (C::TN == 0 || (C::TN == 6 && TJ % 2 == 0)), "");
#define AH_GA 235
#define AH_G2 246
#define AH_K5 246
#define AH_ZZ 234
  static constexpr int AHEAD = C::PH == GA ? AH_GA : (C::PH == G2 ? AH_G2 : (C::PH == ZZ ? AH_ZZ : (C::PH == K5 ? AH_K5 : 5)));
  rsrc_t rs; Lanes v; unsigned slot, g; int w, n, l32;
  __device__ __forceinline__ Stream(const char* src, unsigned bytes, const Lanes& v_, int l, int w_, unsigned slot0, int n0) {
    rs = mk_rsrc(src, bytes); v = v_; w = w_; l32 = l * 32;
    slot = slot0 + n0 * R::SLOT; if (slot >= D * R::SLOT) slot -= D * R::SLOT;
    n = n0; g = __builtin_amdgcn_readlane(v.tab, n + l32);
  }
  static __device__ __forceinline__ Lanes lanes(bool raw, int w, int lane) {
    Lanes v;
    const Tile<C> t;
#pragma unroll
    for (int i = 0; i < 4; ++i) {
      const unsigned row = i * 8 + (lane >> 3), sw = ((lane & 7) ^ (((i & 1) << 2) | (lane >> 4))) << 4;
      if (MODE == 0) v.vo[i] = row * PITCH + sw;
      else if (MODE == 1) v.vo[i] = row * PITCH + (((lane & 7) ^ ((((lane >> 4) & 1) << 2) | ((i & 1) << 1))) << 4);
      else if (MODE == 2) v.vo[i] = (i >> 1) * 4096 + (i & 1) * 1024 + lane * 16;
      else v.vo[i] = raw ? row * (PITCH * 64) + sw : (i >> 1) * PITCH * 1024 + (i & 1) * (C::PH == GA ? 1024 : 2048) + lane * 16;
      v.vo[i] -= i * 1024;
    }
    const int b = SIDE ? t.bn : t.bm, s = lane & 31;
    const unsigned base = MODE == 0 ? (C::PH == GA ? b & 7 : b) * 128 * PITCH : (MODE == 1 ? b * 256 : (MODE == 2 ? b * 8192 + t.kh * T * KS : b * 8 * PITCH * 1024));
    const unsigned first = C::SPLIT && TWO ? (1 - t.kh) * HALF : 0;
    const bool tail = TWO && s >= TJ, second = tail ? s >= TJ + 3 : w;
    const bool zw = C::PH == ZZ && R::ISW, z1 = s < 26 ? s & 1 : s >= 29;
    const int ch = chunk(t, lane >> 5, tail ? 2 * s - TJ + w - (second ? 6 : 0) : (zw ? (s < 26 ? s >> 1 : s - (z1 ? 16 : 13)) : s));
    v.tab = base + (C::BMODE == 1 ? (2 * ch - (ch & 1)) * (KS / 2) : ch * (raw ? 128 : KS)) + (second ? HALF - first : first) + (zw && !z1 ? FH * 2 : 0);
    v.from = R::ISW ? 0 : (C::PH == GA ? 3u << (ch & 14) : 1u << (ch >> (C::PH != ZZ) & 15));
    asm volatile("" : "+v"(v.vo[0]), "+v"(v.vo[1]), "+v"(v.vo[2]), "+v"(v.vo[3]), "+v"(v.tab));
    if (!R::ISW) asm volatile("" : "+v"(v.from));
    return v;
  }
  static __device__ __forceinline__ int chunk(const Tile<C>& t, int l, int ch) {
    constexpr int L = C::T / C::PARTS;
#define LEADE L
#define LEADM 2
#define LEADMASK 2
    static_assert(!(LEADMASK >> C::PH & 1) || LAT < 4, "");
    const int c0 = t.ln * 2 + (t.bn >= 7), zl = !ZORD ? t.bm + (t.bm < c0) : t.bm / 2 + ((t.bm ^ c0) & 1 ? 1 : 9 - (t.bm > c0));
    const int lat = l || (C::PH == ZZ && (t.bm == c0 || (ZNAT && zl < LAT))) ? LAT : (C::PH == GA ? (t.bm + t.bn) & 15 : zl);
#define LEAD8 3
    if (C::PH == G2 && (LEAD8 >> l & 1)) {
      const int d = (ch - t.ln - LEADM * t.bm) & 7;
      if (d == 0 && ch >= 8 && ch < LEADE) ch -= 4;
      else if (d == 4 && ch >= 4 && ch < LEADE - 4) ch += 4;
    } else
    if ((LEADMASK >> C::PH & 1) && (ch & 3) == t.cls && ch >= 4) { ch += 4; if (ch >= L) ch = t.cls + 4; }
    if (C::PH == GA || C::PH == ZZ) ch = ch == LAT ? lat : (ch == lat ? LAT : ch);
    if (C::PH == ZZ && !ZORD) ch = ch ? ch - (ch <= c0) : c0;
    if (C::PH == ZZ && ZORD) ch = ch ? (ch < 9 ? 2 * ch - 1 - (c0 & 1) : 2 * (ch - 9 + (ch - 9 >= t.ln)) + (c0 & 1)) : c0;
    if (C::PH == G2 && SEAM_G < 2) ch = (ch & 15) * 2 + (ch >> 4);
    if (C::PH == G2 && SEAM_G == 2) ch = ch < 2 ? ch : (ch < 17 ? 2 * ch - 2 : 2 * ch - 31);
    if ((C::PH == G2 && SEAM_G) || C::PH == K4) {
      if (ROT == 0) ch = ch < 2 ? ch + t.bm * 2 : ch - (ch < t.bm * 2 + 2 ? 2 : 0);
      if (ROT >= 2 && ch >= 2) ch += ch < 26 ? 6 : -24;
      if (ROT == 3) ch ^= t.bm * 2;
      else if (ROT) ch = (ch + t.bm * 2) & 31;
    }
    if (C::PH == K5) {
      if (t.ln == 7 || SEAM_K == 0) ch = ch < 7 ? 2 * ch : 2 * ch - 13;
      else if (SEAM_K == 1) { ch = (ch < 7 ? 2 * ch : 2 * ch - 13) + t.ln * 2; if (ch >= T) ch -= T; }
      else { ch = (ch < 2 ? ch : (ch < 8 ? 2 * ch - 2 : 2 * ch - 13)) + t.ln * 2; if (ch >= T) ch -= T; }
      ch += t.kh * T;
    }
    return ch;
  }
  __device__ __forceinline__ unsigned writers(int k) {
    unsigned m = 0;
#pragma unroll
    for (int s = 0; s < k; ++s) m |= __builtin_amdgcn_readlane(v.from, n + l32 + s);
    return m;
  }
  __device__ __forceinline__ void put(LAS char* smem) {
    {
#define PIECE(h, i) __builtin_amdgcn_raw_ptr_buffer_load_lds(rs, (LAS void*)(smem + R::BASE + slot + w * 8192 + h * 4096), 16, v.vo[i], g + h * P4, i * 1024, R::ISW ? WAUX : AAUX)
#ifdef __HIP_DEVICE_COMPILE__
#define PC
      PIECE(0, 0); PC; PIECE(0, 1); PC; PIECE(0, 2); PC; PIECE(0, 3); PC; PIECE(1, 0); PC; PIECE(1, 1); PC; PIECE(1, 2); PC; PIECE(1, 3);
#endif
    }
    slot += R::SLOT; if (slot == D * R::SLOT) slot = 0;
    ++n; g = __builtin_amdgcn_readlane(v.tab, n + l32);
    PIN_S(g);
  }
  __device__ __forceinline__ void fill(LAS char* smem, int n1) {
#pragma unroll 1
    while (n < n1) put(smem);
  }
  template <int X, class F> __device__ __forceinline__ void step(LAS char* smem, F pre) {
    BARRIER();
    pre();
    put(smem);
    WAIT_V(NP + X);
  }
  template <class F> __device__ __forceinline__ void finish(LAS char* smem, F job) {
#pragma unroll 1
    while (n < T) step<0>(smem, [] {});
    BARRIER();
    job(0);
    WAIT_V(0);
    BARRIER();
    job(1);
    if (C::SPLIT || !C::SEAM) BARRIER();
  }
  template <int X, int LAG> __device__ __forceinline__ void skip() const { WAIT_VL((4 - LAG) * NP + X); BARRIER(); }
  template <int X = 0, int LAG = 0> __device__ __forceinline__ void go(LAS char* smem, int b) {
#pragma unroll 1
    while (n < b + 5 - LAG) { put(smem); skip<X, LAG>(); }
  }
  __device__ __forceinline__ void close(LAS char* smem) {
    go(smem, TJ - 1);
#pragma unroll 1
    while (n < T) { put(smem); if (n < T) WAIT_V(3 * NP); else WAIT_V(2 * NP); BARRIER(); }
  }
  __device__ __forceinline__ void open(LAS char* smem) {
#pragma unroll 1
    for (int k = 8; k >= 0; k -= 4) {
      BARRIER();
      fill(smem, (AHEAD / 100 * 256 + AHEAD / 10 % 10 * 16 + AHEAD % 10) >> k & 15); }
  }
  template <class F> __device__ __forceinline__ void resume(LAS char* smem, F job) {
    WAIT_V((AHEAD % 10 - 1) * NP);
    BARRIER();
    job();
    fill(smem, 6);
    skip<0, 0>();
  }
};

#define LSOP "s_memrealtime"
#define LS(k)
#define TS(i)
#define TSR(i)
#define TS_DECL
#define TS_OUT(ph)

__device__ __forceinline__ unsigned tr_lane(int r, int j) { return ((r >> 2) & 1) * 512 + (j & 1) * 256 + (r >> 3) * 128 + (r & 3) * 32 + (j >> 1) * 16; }
__device__ __forceinline__ void unpack8(u32x4 v, float (&e)[8]) {
#pragma unroll
  for (int i = 0; i < 4; ++i) { e[2 * i] = bf_lo(v[i]); e[2 * i + 1] = bf_hi(v[i]); }
}

__device__ __forceinline__ float dot2(float a, float b, float c) { return __builtin_amdgcn_fdot2_f32_bf16(__builtin_bit_cast(bf16x2, a), __builtin_bit_cast(bf16x2, b), c, false); }
__device__ __forceinline__ void first_tag(LAS char* smem, const Params& p, int wid, int lane) {
  if (wid == 2 && lane < 32) *(LAS unsigned*)(smem + CNT_OFF + lane * 4) = 0u;
  if (wid == 3) {
    u64 e0;
    asm volatile("s_load_dwordx2 %0, %1, 0x0\n\ts_waitcnt lgkmcnt(0)" : "=s"(e0) : "s"(p.ws + O_EP) : "memory");
    u64 e = (e0 ^ (e0 >> 29)) * 0x9E3779B97F4A7C15ull + 0x632BE59BD9B4E019ull;
    if (e == e0) e = ~e0;
    if (lane == 0) *(LAS u64*)(smem + EP_OFF) = e;
  }
  WAIT_L0();
}
__device__ __forceinline__ float join2(float a, float b) {
  unsigned x = __builtin_bit_cast(unsigned, a), y = __builtin_bit_cast(unsigned, b); swap32(x, y);
  return __builtin_bit_cast(float, x) + __builtin_bit_cast(float, y);
}
__device__ __forceinline__ void add_stat(u32x4 y, float& s, float& q) {
#pragma unroll
  for (int k = 0; k < 4; ++k) { const unsigned u = y[k]; const float v = __builtin_bit_cast(float, u); s = dot2(v, __builtin_bit_cast(float, 0x3f803f80u), s); q = dot2(v, v, q); }
}
__device__ __forceinline__ void first_sums(LAS char* smem, int wid, int lane, const s16x8 (&ga)[2][4], const s16x8 (&gb)[2][4]) {
  const int wm = wid >> 1, wn = wid & 1;
  u32x4 ones = {0x3f803f80u, 0x3f803f80u, 0x3f803f80u, 0x3f803f80u};
  f32x4 d[6];
#define TWO_SUMS(d0, d1, x0, x1, x2, x3, f) asm volatile("v_mfma_f32_16x16x32_bf16 %0, %2, %6, 0\n\tv_mfma_f32_16x16x32_bf16 %0, %3, %7, %0\n\t" \
      "v_mfma_f32_16x16x32_bf16 %1, %4, %8, 0\n\tv_mfma_f32_16x16x32_bf16 %1, %5, %9, %1" : "=&v"(d0), "=&v"(d1) : "v"(x0), "v"(x1), "v"(x2), "v"(x3), "v"(f[0]), "v"(f[1]), "v"(f[2]), "v"(f[3]))
  auto rows = [&](const s16x8 (&f)[4]) { TWO_SUMS(d[0], d[1], f[0], f[1], f[2], f[3], f); TWO_SUMS(d[2], d[3], ones, ones, ones, ones, f); };
  asm volatile("" : "+v"(ones));
  if (wn) rows(ga[1]); else rows(ga[0]);
  if (wm) TWO_SUMS(d[4], d[5], ones, ones, ones, ones, gb[1]); else TWO_SUMS(d[4], d[5], ones, ones, ones, ones, gb[0]);
  LAS char* at = smem + (wn * 64 + wm * 32 + (lane & 15)) * 4;
  asm volatile("s_nop 7" : "+v"(d[0]), "+v"(d[1]), "+v"(d[2]), "+v"(d[3]), "+v"(d[4]), "+v"(d[5]), "+v"(at));
  if (lane >> 4 == (lane >> 2 & 3)) {
#pragma unroll
    for (int i = 0; i < 2; ++i) {
      *(LAS f32x4*)(smem + PS_OFF + (at - smem) * 4 + i * 256) = d[i];
      *(LAS float*)(at + ST_OFF + i * 64) = d[2 + i][0];
      *(LAS float*)(at + CP_OFF + i * 64) = d[4 + i][0];
    }
  }
  WAIT_L(0);
}

struct Spot { unsigned x, own, aux, res; };
template <class C> __device__ __forceinline__ Spot spot_of(LAS char* smem, int wid, int lane, bool raw) {
  constexpr int PH = C::PH;
  constexpr bool ROWS = PH == GA || PH == K5, TR = PH == GA || PH == ZZ;
  const Tile<C> t; const Tile<CG2> u;
  const int r = lane & 15, j = lane >> 4, wm = wid >> 1, wn = wid & 1, f = (PH == GA || PH == K4) && !u.kh;
  const int n0 = t.bn * 128 + wn * 64 + (PH == G2 ? t.kh * 32 : 0), m0 = t.bm * 128 + (ROWS ? wm * 32 + t.kh * 64 : wm * 64), hi = PH == GA && n0 >= D_;
  Spot s;
  s.x = TR ? tr_lane(r, j) : (j >> 1) * 256 + r * 16;
  s.own = (unsigned)(size_t)smem + A_RING + (TR ? f * 8192 + wm * (PH == GA ? 4096 : 16384) + wn * 2048 : (wm ^ f) * 8192 + (j & 1) * 2048 + wn * 1024) + s.x;
  if (TR) s.x += (PH == GA ? O_AB : O_Z2) + (unsigned)(((hi * S_ + m0) >> 5) * (PH == GA ? D_ / 16 : FH / 16) + ((n0 - hi * D_) >> 4)) * 1024u;
  else s.x += (PH == K4 ? O_H : (PH == G2 ? O_X1 : LSTRIDE + O_X)) + (unsigned)(((m0 >> 4) + (j & 1)) * (PH == K4 ? FH / 32 : D_ / 32)) * 1024u + (n0 >> 4) * 512;
  if (C::SPLIT) s.aux = (PH == G2 ? O_XG : O_XK) + (blockIdx.x * 4 + wid) * 4096 + lane * 16;
  else s.aux = lane != -wid ? 2 * LSTRIDE : O_SF + (PH == GA ? (F_GA * 16 + t.bn) * 16 + t.bm : (F_K4 * 16 + t.bm) * 16 + t.bn) * sizeof(Flag);
  const int um = u.bm * 128 + (PH == GA ? wm * 64 : wm * 32 + u.kh * 64), un = u.bn * 128 + wn * 64 + (PH == GA ? u.kh * 32 : 0);
  s.res = (unsigned)um * (D_ * 2) + (raw ? r * (D_ * 2) + un * 2 + j * 16 : un * 32 + lane * 16);
  asm volatile("" : "+v"(s.x));
  if (PH != K5) asm volatile("" : "+v"(s.own));
  if (PH != ZZ) asm volatile("" : "+v"(s.aux));
  if (PH == GA || PH == K4) asm volatile("" : "+v"(s.res));
  return s;
}

__device__ __forceinline__ void tables(LAS char* smem, int wid, int lane) {
  static_assert(Stream<CG2, 0>::HALF == Stream<CK4, 0>::HALF && Stream<CZZ, 1>::HALF == Stream<CK5, 1>::HALF, "");
  Lanes v;
  if (wid == 0) v = Stream<CG2, 0>::lanes(false, 0, lane);
  else if (wid == 1) v = Stream<CZZ, 1>::lanes(false, 0, lane);
  else if (wid == 2) v = Stream<CK4, 0>::lanes(false, 0, lane);
  else v = Stream<CK5, 1>::lanes(false, 0, lane);
  *(LAS u32x4*)(smem + ZS_OFF + wid * 1024 + lane * 16) = (u32x4){v.vo[0], v.vo[1], v.vo[2], v.vo[3]};
  *(LAS unsigned*)(smem + ZS_OFF + 4096 + wid * 256 + lane * 4) = v.tab;
}
template <class C>
__device__ __forceinline__ void compute_phase(LAS char* smem, const Params& p, int l, int wid, int lane, u32x4 (&zk)[4][2], u32x4 (&rv)[4], unsigned& wpos, Spot (&sp)[5], unsigned& lsv) {
  constexpr int BN = C::BN, MT = C::MT, NT = C::NT, PH = C::PH, TN = C::TN, TP = C::T - TN;
  using RX = Ring<C, 0>; using RY = Ring<C, 1>;
  TS_DECL; TSR(4); TS(0);
#define TR(i)
  asm volatile("" : "+v"(lane));
  Tile<C> t;
  char* wl = p.ws + l * LSTRIDE;
  constexpr bool ROWS = TN && C::WSIDE, COLS = TN && !C::WSIDE;
  const int wm = wid / C::WN, wn = wid % C::WN, ml = ROWS ? wm * 32 : wm * 64;
  unsigned fin_at;
  int n0, m0;
  constexpr int OW = PH == K4 ? FH : D_;
  f32x4 acc[MT][NT * C::PARTS];
  u32x4 pv[4];
  float ini[MT];
  constexpr int NA = C::AM == 0 || PH == GA ? 2 : 1, NB = C::BMODE == 0 ? 2 : (C::BMODE == 1 ? 4 : 1);
  unsigned ad[NA + NB];
  const unsigned xs = C::WSIDE ? C::A0 : wpos, ys = C::WSIDE ? wpos : C::A0;
  {
    const int r = lane & 15, j = lane >> 4;
    constexpr int MW = ROWS ? 2 : MT;
    if (C::AM == 0 || (PH == GA && l == 0)) { ad[0] = RX::BASE + xs + (wm * MW * 16 + r) * 128 + ((j ^ (r >> 1)) << 4); ad[NA - 1] = ad[0] ^ 64; }
    else { ad[0] = RX::BASE + xs + wm * MW * 2048 + lane * 16; if (NA == 2) ad[1] = ad[0] + 1024; }
    if (C::BMODE == 0) { ad[NA] = RY::BASE + ys + (wn * NT * 16 + r) * 128 + ((j ^ (r >> 1)) << 4); ad[NA + NB - 1] = ad[NA] ^ 64; }
    else if (C::BMODE == 1) {
      const int q = (lane >> 2) & 3, pp = lane & 3, f = ((q >> 1) << 1) | (j & 1);
#pragma unroll
      for (int c = 0; c < NB; ++c) ad[NA + c] = RY::BASE + ys + wn * 8192 + (j * 8 + q) * 128 + ((c ^ f) << 5) + pp * 8;
    } else ad[NA] = RY::BASE + ys + wn * 2048 + lane * 8;
#pragma unroll
    for (int u = 0; u < NA + NB; ++u) ad[u] += (unsigned)(size_t)smem;
#pragma unroll
    for (int mt = 0; mt < MT; ++mt) ini[mt] = PH == K4 ? *(const LAS float*)(smem + BS_OFF + (ml + mt * 16 + r) * 4) : 0.f;
  }
  unsigned cx = xs / RX::SLOT * (~0u / RX::D + 1), cy = ys / RY::SLOT * (~0u / RY::D + 1), dx, dy;
  wpos += (C::T + TN) * Ring<C, C::WSIDE>::SLOT % W_RING; if (wpos >= W_RING) wpos -= W_RING;
#define RING_NEXT(c, d, R) asm volatile("s_add_u32 %0, %0, %4\n\ts_cselect_b32 %1, %2, %3" : "+s"(c), "=&s"(d) : "n"(-(int)((R::D - 1) * R::SLOT)), "s"(R::SLOT), "n"(~0u / R::D + 1) : "scc")
  static_assert(NT == 4, "");
  constexpr int BR = C::BMODE == 0 ? 1 : 2, NM = MT * NT, MF = COLS ? 2 * MT : MT, NF = ROWS ? 2 * NT : NT;
  auto ra = [&](s16x8& f, int mt, int kk) {
    f = *(const LAS s16x8*)((LAS char*)(size_t)ad[NA == 2 ? kk : 0] + (NA == 2 ? 0 : kk * 1024) + (ROWS ? (mt >> 1) * 8192 + (mt & 1) * 2048 : mt / MT * RX::SLOT + mt % MT * 2048));
  };
  auto rb = [&](s16x8& f, int n, int kk, int h) {
    const int nt = n % NT;
    if (C::BMODE == 0) { f = *(const LAS s16x8*)((LAS char*)(size_t)ad[NA + kk] + nt * 2048 + n / NT * RY::SLOT); return; }
    const s16x4 v = __builtin_amdgcn_ds_read_tr16_b64_v4i16((LAS s16x4*)((LAS char*)(size_t)ad[NA + nt % NB] + kk * 4096 + (C::BMODE == 1 ? 0 : (nt >> 1) * 8192 + (nt & 1) * 1024) + h * 512 + n / NT * RY::SLOT));
    if (h == 0) f = __builtin_shufflevector(v, v, 0, 1, 2, 3, -1, -1, -1, -1);
    else { const s16x4 lo = __builtin_shufflevector(f, f, 0, 1, 2, 3); f = __builtin_shufflevector(lo, v, 0, 1, 2, 3, 4, 5, 6, 7); }
  };
  s16x8 ga[2][4], gb[2][4], fa[2][MF], fb[2][NF];
  auto rd = [&](int chunks, int kk, int u) {
    if (chunks == 1) {
      const int x = kk ? u : u - 8, y = kk ? u - 4 : u % 12, n = kk ? 2 : (u < 8 ? y / BR / 2 : 3);
      if (x >= 0 && x < 4) ra(ga[kk][x ^ x >> 1], 2 * kk + (x >> 1), (x ^ x >> 1) & 1);
      else if (y < (x < 0 ? 4 : 2) * BR) rb(gb[n >> 1][(n & 1) * 2 + (y / BR & 1)], n, y / BR & 1, y % BR);
    } else if (ROWS) {
      if (u < 4) ra(fa[kk][u], u, kk);
      else { const int y = (u - 4) / BR, n = (y & 1) * 4 + (y >> 1); rb(fb[kk][n % NF], n, kk, (u - 4) % BR); }
    } else {
      if (u < 4 * BR) rb(fb[kk][u / BR], u / BR, kk, u % BR);
      else { const int x = u - 4 * BR, m = (x & 1) * 4 + (x >> 1); ra(fa[kk][m % MF], m, kk); }
    }
  };
  auto half = [&](auto cm, auto nm, int kk, int hs, auto extra) {
    constexpr int NR = nm.v == 2 ? MF + NF * BR : MT + NT * BR;
    constexpr int GAPS = NR <= NM - 2 ? NR : NM - 2, FREE = NM - GAPS;
#pragma unroll
    for (int i = 0; i < NM; ++i) {
      const int k = (i ^ i >> 1) & 1, sh = (i >> 1 ^ i >> 2) & 1, lg = i >> 2, g = i >> 3;
      const int m = cm.v == 1 ? 2 * g + sh : (ROWS ? sh : lg), n = cm.v == 1 ? 2 - 2 * kk + ((lg ^ lg >> 1) & 1) : (ROWS ? lg : sh);
      f32x4& d = acc[m + (ROWS && hs > 0 ? 2 : 0)][n + (COLS && hs > 0 ? 2 : 0)];
      const s16x8& a = cm.v == 1 ? ga[g][2 * sh + k] : fa[1 - kk][k * (MF / 2) + m];
      const s16x8& b = cm.v == 1 ? gb[1 - kk][2 * (n & 1) + k] : fb[1 - kk][k * (NF / 2) + n];
      if (cm.v == 1 && kk == 1 && (i == 0 || i == 2)) WAIT_L(2 + 2 * BR);
      if (cm.v == 1 && kk == 1 && i == 8) WAIT_L(2 * BR);
      if (cm.v == 2 && kk == 1 && i == 0) WAIT_L(ROWS ? 6 * BR : 6);
      if (cm.v == 2 && kk == 1 && i == 4) WAIT_L((4 * NR + GAPS - 1) / GAPS);
      asm volatile("v_mfma_f32_16x16x32_bf16 %0, %1, %2, %0" : "+v"(d) : "v"(b), "v"(a));
      __builtin_amdgcn_sched_barrier(0);
      if (nm.v == 1) {
        rd(1, kk, i);
        if (kk == 1 && i == 9) RING_NEXT(cx, dx, RX);
        if (kk == 1 && i == 11) RING_NEXT(cy, dy, RY);
#pragma unroll
        for (int u = 0; u < NA + NB; ++u) if (kk == 1 && i == (0xdcdca >> 4 * u & 15)) asm volatile("v_add_u32 %0, %1, %0" : "+v"(ad[u]) : "s"(u < NA ? dx : dy));
      } else if (nm.v) {
#pragma unroll
        for (int u = (i * NR + GAPS - 1) / GAPS; u < ((i + 1) * NR + GAPS - 1) / GAPS; ++u) if (u < NR) rd(nm.v, kk, u);
        if (kk == 0 && i == (FREE > 2 ? 13 : 14)) { if (nm.v == 2 && COLS) RING_NEXT(cx, dx, PairRing); else RING_NEXT(cx, dx, RX); }
        if (kk == 0 && i == 15) { if (nm.v == 2 && ROWS) RING_NEXT(cy, dy, PairRing); else RING_NEXT(cy, dy, RY); }
        if (kk == 1 && i >= GAPS) {
#pragma unroll
          for (int u = NM - 1 - i; u < NA + NB; u += FREE) asm volatile("v_add_u32 %0, %1, %0" : "+v"(ad[u]) : "s"(u < NA ? dx : dy));
        }
      }
      extra(i);
      __builtin_amdgcn_sched_barrier(0);
    }
  };
  auto none = [](int) {};

  auto ht = [&](int u, int e, int h = 1) -> f32x4& { return COLS ? acc[(u & 2) + e][(u & 1) + 2 * h] : acc[e + 2 * h][u]; };
  auto pair_at = [&](int u) { return COLS ? (u & 2) * (OW / 32) * 1024 + (u & 1) * 512 : u * 512; };
#define HALF_NOP(h) do { asm volatile("s_nop 11" : "+v"(ht(0, 0, h)), "+v"(ht(1, 0, h)), "+v"(ht(2, 0, h)), "+v"(ht(3, 0, h))); \
                         asm volatile("s_nop 0" : "+v"(ht(0, 1, h)), "+v"(ht(1, 1, h)), "+v"(ht(2, 1, h)), "+v"(ht(3, 1, h))); } while (0)
  const rsrc_t rw = mk_rsrc(wl, 2 * LSTRIDE);
  if (PH != GA && PH != ZZ) asm volatile("" : "+v"(sp[PH].x), "+v"(sp[PH].aux), "+v"(sp[PH == K5 ? K4 : PH].own), "+v"(sp[K4].res));
  const Spot at = PH == GA || PH == ZZ ? spot_of<C>(smem, wid, lane, PH == GA && l == 0) : sp[PH];
  const unsigned x_at = at.x, own_at = at.own, out_at = C::SPLIT ? at.aux : at.x, flag_at = C::SPLIT ? at.aux ^ 16384 : at.aux, res_at = PH == K5 ? sp[K4].res : at.res;
  auto locate = [&] {
    t = Tile<C>();
    n0 = t.bn * BN + wn * (NT * 16); m0 = t.bm * 128 + ml + t.kh * 64;
    PIN_S(n0); PIN_S(m0);
  };
  if (PH == ZZ) locate();
  auto keep = [&](int h, int u, u32x4 y) {
    const unsigned to = PH == GA ? (own_at ^ (u >> 1) * 8192) + h * 16384 + (u & 1) * 1024 : own_at + (PH == ZZ ? (u >> 1) * 8192 + h * 4096 + (u & 1) * 1024 : h * 16384 + (u & 2) * 2048 + (u & 1) * 512);
    *(LAS u32x4*)(size_t)to = y;
  };
  auto residual = [&](int u) {
    const bool raw = PH == GA && l == 0;
    const rsrc_t rr = mk_rsrc(raw ? p.x : wl + (PH == GA ? O_X : O_X1), S_ * D_ * 2);
    rv[u] = __builtin_amdgcn_raw_buffer_load_b128(rr, res_at, PH == GA ? u * (D_ * 32) : (u & 1) * (D_ * 32) + (u >> 1) * 1024, 16);
  };
  if (PH == K5 && t.ln == 7) {
    wait_flags(tag_of(smem), flags(wl, F_G2, t.bm) + (lane & 15), ~tag_of(smem));
#pragma unroll
    for (int u = 0; u < 4; ++u) residual(u);
    __builtin_amdgcn_s_waitcnt(0x0f70);
  }
  if constexpr (PH == ZZ) {
    int left;
    auto quarter = [&](int c, int reads, int ring, auto R) {
#pragma unroll
      for (int i = 0; i < NM; ++i) {
        const int k = (i ^ i >> 1) & 1, sh = (i >> 1 ^ i >> 2) & 1, n = (i >> 2 ^ i >> 3) & 1, g = (i >> 3) ^ (reads == 2), at = reads < 2 ? 12 : 8;
        if (reads == 1 && (i == 0 || i == 2)) WAIT_L(2);
        if (reads == 1 ? i == 8 : reads == 2 && (i == 0 || i == 4)) WAIT_L(4);
        asm volatile("v_mfma_f32_16x16x32_bf16 %0, %1, %2, %0" : "+v"(acc[2 * g + sh][2 * c + n]) : "v"(gb[c & 1][2 * n + k]), "v"(ga[g][2 * sh + k]));
        __builtin_amdgcn_sched_barrier(0);
        const int x = reads == 1 ? i : i - 8, y = reads == 1 ? i - 4 : i;
        if (reads < 2 && x >= 0 && x < 4) ra(ga[reads][x], 2 * reads + (x >> 1), x & 1);
        else if (y < 8) rb(gb[reads & 1][y >> 1], 2 * reads + (y >> 2), y >> 1 & 1, y & 1);
        if (ring == 1 && i == at) RING_NEXT(cx, dx, RX);
        if (ring == 1 && i == at + 1) RING_NEXT(cy, dy, decltype(R));
        if (ring == 2 && i >= at && i < at + 4) {
#pragma unroll
          for (int u = at + 3 - i; u < NA + NB; u += 4) asm volatile("v_add_u32 %0, %1, %0" : "+v"(ad[u]) : "s"(u < NA ? dx : dy));
        }
        if (reads == 0 && i == 14) asm volatile("s_sub_u32 %0, %0, 1" : "+s"(left) :: "scc");
        if (reads >= 2 && i == 13) WAIT_L(0);
        if (reads == 3 && i == 13) BARRIER();
        __builtin_amdgcn_sched_barrier(0);
      }
    };
    auto first = [&] {
#pragma unroll
      for (int i = 0; i < 12; ++i) { if (i < 8) rb(gb[0][i >> 1], i >> 2, i >> 1 & 1, i & 1); else ra(ga[0][i - 8], (i - 8) >> 1, i & 1); }
      __builtin_amdgcn_sched_barrier(0);
    };
    const u32x4 ones = {0x3f803f80u, 0x3f803f80u, 0x3f803f80u, 0x3f803f80u};
#pragma unroll
    for (int i = 0; i < 4; ++i) {
#pragma unroll
      for (int nt = 0; nt < 8; ++nt) acc[i][nt] = (f32x4){};
      asm volatile("" : "+v"(acc[i][0]), "+v"(acc[i][1]), "+v"(acc[i][2]), "+v"(acc[i][3]), "+v"(acc[i][4]), "+v"(acc[i][5]), "+v"(acc[i][6]), "+v"(acc[i][7]));
    }
    if (ZEARLY) {
#pragma unroll
      for (int u = 0; u < 4; ++u) if (ZEARLY == 1) *(LAS u32x4*)(size_t)(sp[G2].own + (u & 2) * 2048 + (u & 1) * 512) = rv[u];
      signal_done<0, false>(smem, l * 8 + 2, flags(wl, F_G2, t.bm) + t.ln * 2 + (blockIdx.x & 1), nullptr);
    }
    WAIT_L(0);
    BARRIER();
    TS(1); TSR(7); TR(0); LS(1);
    first();
#pragma unroll 1
    for (int seg = l; seg < 2; ++seg) {
      const int c0 = t.ln * 2 + (blockIdx.x & 1), zl = t.bm / 2 + ((t.bm ^ c0) & 1 ? 1 : 9 - (t.bm > c0)), lat = t.bm == c0 ? 0 : (ZNAT && zl < ZLAT ? zl : ZLAT);
      left = l ? 13 : (seg ? 13 - lat : lat);
#pragma unroll 1
      while (left) {
        quarter(0, 1, 0, RY{});
        quarter(1, 2, 1, PairRing{});
        quarter(2, 3, 2, RY{});
        quarter(3, 0, 0, RY{});
        TR(seg ? 13 - left : ZLAT - left);
      }
      if (seg == 0) {
        s16x4 v[4][4];
        f32x4 d[4];
#pragma unroll
        for (int i = 0; i < 16; ++i) v[i >> 2][i & 3] = __builtin_amdgcn_ds_read_tr16_b64_v4i16((LAS s16x4*)((LAS char*)(size_t)(ad[NA + (i >> 2)] + wm * 16384) + (i & 2) * 2048 + (i & 1) * 512));
#pragma unroll
        for (int nt = 0; nt < 4; ++nt)
          asm volatile("s_nop 1\n\tv_mfma_f32_16x16x32_bf16 %0, %1, %2, 0\n\tv_mfma_f32_16x16x32_bf16 %0, %1, %3, %0" : "=&v"(d[nt])
                       : "v"(ones), "v"(__builtin_shufflevector(v[nt][0], v[nt][1], 0, 1, 2, 3, 4, 5, 6, 7)), "v"(__builtin_shufflevector(v[nt][2], v[nt][3], 0, 1, 2, 3, 4, 5, 6, 7)));
        asm volatile("s_nop 7" : "+v"(d[0]), "+v"(d[1]), "+v"(d[2]), "+v"(d[3]));
        const float d0 = d[0][0], d1 = d[1][0], d2 = d[2][0], d3 = d[3][0], v0 = lane & 16 ? d1 : d0, v1 = lane & 16 ? d3 : d2;
        __builtin_amdgcn_raw_buffer_store_b32(min(__builtin_bit_cast(unsigned, lane & 32 ? v1 : v0), 0xfffffffeu), mk_rsrc(p.ws + O_SI, 256 * 1024), lane * 16,
                                              ((t.bm >> 2) * F_ + wm * FH + n0) * 16 + (t.bm & 3) * 4, 16);
      }
    }
    RING_NEXT(cx, dx, RX); RING_NEXT(cy, dy, RY);
    f32x4 k[4], cs[4];
    LAS char* k_at = smem + ST_OFF + (ml + (lane & 15)) * 16;
    LAS char* cs_at = smem + CS3_OFF + wn * 256 + (lane & 48);
    asm volatile("" : "+v"(k_at), "+v"(cs_at));
    auto singles = [&](int c) {
      left = 3;
#pragma unroll 1
      do {
        quarter(c, 1, 2, RY{});
        WAIT_L(0);
        BARRIER();
        __builtin_amdgcn_sched_barrier(0);
        quarter(c + 1, 0, 1, RY{});
        TR(16 + c / 2 * 3 - left);
      } while (left);
#pragma unroll
      for (int i = 0; i < 4; ++i) { k[i] = *(const LAS f32x4*)(k_at + i * 256); cs[i] = *(const LAS f32x4*)(cs_at + c * 256 + i * 64); }
      __builtin_amdgcn_sched_barrier(0);
#pragma unroll
      for (int mt = 0; mt < MT; ++mt) asm volatile("s_nop %4" : "+v"(acc[mt][2 * c]), "+v"(acc[mt][2 * c + 1]), "+v"(acc[mt][2 * c + 2]), "+v"(acc[mt][2 * c + 3]) : "n"(mt ? 0 : 11));
    };
    auto gelu = [&](int part, int mt, auto between) {
#pragma unroll
      for (int nt = 0; nt < 4; ++nt) {
        f32x4& z = acc[mt][part * 4 + nt];
        z = gelu_tanh4(z + cs[nt] * k[mt][0], k[mt]);
        const u32x2 y = pack4(z);
        zk[mt][nt >> 1][2 * (nt & 1)] = y[0]; zk[mt][nt >> 1][2 * (nt & 1) + 1] = y[1];
        between(mt * 4 + nt, z);
      }
    };
    singles(0);
    TS(8);
    {
      f32x4 sm[4], q[4];
      u32x4 one = ones;
      asm volatile("" : "+v"(one));
      auto stat = [&](int i, f32x4& z) {
        if (i < 0) return;
        const u32x4 y = zk[i >> 2][i >> 1 & 1], x = i & 1 ? y : one;
        f32x4& d = i & 1 ? q[i >> 2] : sm[i >> 2];
        if (i & 2) asm volatile("v_mfma_f32_16x16x32_bf16 %0, %2, %3, %0" : "+v"(d), "+v"(z) : "v"(x), "v"(y));
        else asm volatile("v_mfma_f32_16x16x32_bf16 %0, %2, %3, 0" : "=&v"(d), "+v"(z) : "v"(x), "v"(y));
      };
#pragma unroll
      for (int mt = 0; mt < 4; ++mt) gelu(0, mt, [&](int i, f32x4& z) { stat(i - 2, z); });
      asm volatile("s_nop 1" : "+v"(zk[3][1]));
      stat(14, acc[3][3]); stat(15, acc[3][3]);
      LAS char* at = k_at + wn * 2048;
      asm volatile("s_nop 7" : "+v"(sm[0]), "+v"(sm[1]), "+v"(sm[2]), "+v"(sm[3]), "+v"(q[0]), "+v"(q[1]), "+v"(q[2]), "+v"(q[3]), "+v"(at));
      at += ZS_OFF - ST_OFF;
      if (lane >> 4 == (lane >> 2 & 3)) {
#pragma unroll
        for (int mt = 0; mt < 4; ++mt) { *(LAS f32x4*)(at + mt * 256) = q[mt]; *(LAS float*)(at + 4096 + mt * 256) = sm[mt][0]; }
      }
      asm volatile("" ::: "memory");
      first();
      WAIT_L(12);
      BARRIER();
    }
    TS(9);
    singles(2);
    TS(2); TSR(14); LS(2);
    u32x4 z2[2][4];
#pragma unroll
    for (int mt = 0; mt <= 4; ++mt) {
      if (mt == ZA) {
        TS(10);
        wait_count(smem, 16 + l * 2 + wm, 1);
        TS(11);
#pragma unroll
        for (int mp = 0; mp < 2; ++mp) {
          const f32x2 f0 = *(const LAS f32x2*)(k_at + PS_OFF - ST_OFF + mp * 512), f1 = *(const LAS f32x2*)(k_at + PS_OFF - ST_OFF + mp * 512 + 256);
#pragma unroll
          for (int nt = 0; nt < 4; ++nt) {
            z2[mp][nt] = pair_rows(acc[2 * mp][nt] * f0[0] + f0[1], acc[2 * mp + 1][nt] * f1[0] + f1[1]);
            st16(rw, out_at + mp * (FH / 16) * 1024 + nt * 1024, z2[mp][nt]);
            if (ZK == 0) keep(mp, nt, z2[mp][nt]);
          }
        }
        __builtin_amdgcn_sched_barrier(0);
        TS(12);
      }
      if (mt == ZB) { TS(13); signal_done<0, false>(smem, l * 8, flags(wl, F_Z, t.bn) + t.bm, nullptr); TS(15); }
      if (ZK && mt == ZK) {
#pragma unroll
        for (int e = 0; e < 8; ++e) keep(e >> 2, e & 3, z2[e >> 2][e & 3]);
        __builtin_amdgcn_sched_barrier(0);
      }
      if (mt < 4) { gelu(1, mt, [](int, f32x4&) {}); asm volatile("" : "+v"(zk[mt][0]), "+v"(zk[mt][1])); }
    }
  } else {
  unsigned bias;
  if (PH == GA && l == 0) {
    asm volatile("global_load_ushort %0, %1, %2" : "=v"(bias) : "v"((wn * 64 + lane) * 2), "s"(p.bsp + t.bm * 128) : "memory");
    first_tag(smem, p, wid, lane);
  }
  constexpr IC<1> ONE{}; constexpr IC<2> TWO{};
  auto stepm = [&](auto m, int hs, auto extra) {
    BARRIER();
    __builtin_amdgcn_sched_barrier(0);
    half(m, m, 0, hs, extra);
    half(m, m, 1, hs, [&](int i) { extra(i + 16); });
    WAIT_L(0);
  };
  {
    u32x4 pick[2];
    auto start = [&] {
#pragma unroll
      for (int u = 0; u < 4; ++u) {
#pragma unroll
        for (int e = 0; e < 2; ++e) {
          if (C::SPLIT) {
            asm volatile("v_mfma_f32_16x16x32_bf16 %0, %1, %2, 0" : "=&v"(ht(u, e)) : "v"(pick[u & 1]), "v"(rv[(u & 2) + e]));
            ht(u, e, 0) = (f32x4){};
            asm volatile("" : "+v"(ht(u, e, 0)));
          } else {
            acc[u][2 * e] = acc[u][2 * e + 1] = (f32x4){ini[u], ini[u], ini[u], ini[u]};
            asm volatile("" : "+v"(acc[u][2 * e]), "+v"(acc[u][2 * e + 1]));
          }
          __builtin_amdgcn_sched_barrier(0);
        }
      }
    };
    if (C::SPLIT) { pick[0] = *(const LAS u32x4*)(smem + CP_OFF + lane * 16); pick[1] = *(const LAS u32x4*)(smem + CP_OFF + (lane * 16 ^ 512)); }
    constexpr bool IDLE = PH == GA || (C::SPLIT && C::SEAM == 0);
    if (IDLE) start();
    WAIT_L(0);
    BARRIER();
    TS(1); TSR(7); TR(0); LS(1);
#pragma unroll
    for (int i = 0; i < 16; ++i) { rd(1, 0, i); __builtin_amdgcn_sched_barrier(0); }
    if (!IDLE) start();
    half(ONE, ONE, 1, 0, [&](int i) {
      if (WTAB == 3 && PH == GA && i == 13 && l == 0) { int ln = lane; asm volatile("" : "+v"(ln)); tables(smem, wid, ln); }
      if (i == 13) WAIT_L(0);
      if (i == 14 && (PH != GA || LAT_S)) BARRIER();
    });
    if (PH == GA && !LAT_S) {
      if (l == 0) first_sums(smem, wid, lane, ga, gb);
      BARRIER();
      __builtin_amdgcn_sched_barrier(0);
    }
#define PEEL 1
    int left = TP - 1;
    if (PH == GA && PEEL && l == 0) {
      TR(1);
      half(ONE, ONE, 0, 0, none);
      half(ONE, ONE, 1, 0, [&](int i) { if (i == 13) WAIT_L(0); });
      int ln = lane; asm volatile("s_waitcnt vmcnt(0)" : "+v"(ln), "+v"(bias));
      *(LAS unsigned*)(smem + BS_OFF + (wn * 64 + ln) * 4) = bias << 16;
      first_sums(smem, wid, ln, ga, gb);
      BARRIER();
      __builtin_amdgcn_sched_barrier(0);
      left = TP - 2;
    }
#pragma unroll 1
    do {
      TR(TP - left);
      half(ONE, ONE, 0, 0, none);
      half(ONE, ONE, 1, 0, [&](int i) {
        if (i == 8) asm volatile("s_sub_u32 %0, %0, 1" : "+s"(left) :: "scc");
        if (i == 13) __builtin_amdgcn_s_waitcnt(C::SPLIT && C::SEAM < 2 ? 0x0070 : 0xc07f);
        if (i == 14 && (PH != GA || !LAT_S || PEEL)) BARRIER();
      });
      if (PH == GA && LAT_S && !PEEL) {
        if (left + l * 16 == TP - 2) {
          int ln = lane; asm volatile("" : "+v"(ln));
          first_sums(smem, wid, ln, ga, gb);
        }
        BARRIER();
        __builtin_amdgcn_sched_barrier(0);
      }
    } while (left);
    TR(TP);
    if (PH == G2 || PH == K5) TS(12);
    half(ONE, TWO, 0, 0, none);
    {
      u64 tag;
      f32x2 st[2];
      f32x4 cs[4];
      unsigned y[4];
      f32x4 pre[4][2];
      auto operands = [&](int h, int i) {
        if (PH != GA) return;
        if (i < 2) st[i] = *(const LAS f32x2*)(smem + ST_OFF + (h * 64 + ml + i * 16) * 16 + (lane & 15) * 16);
        else if (i < 6 && !h) cs[i - 2] = *(const LAS f32x4*)(smem + CS1_OFF + (wn * 64 + (i - 2) * 16) * 4 + (lane & 48));
      };
      auto other = [&](int u, int e, int h) {
        const int mt = (u & 2) + e;
        return PH == GA ? cs[u] * st[e][1] : unpack4((u32x2){zk[mt][h][2 * (u & 1)], zk[mt][h][2 * (u & 1) + 1]});
      };
      auto finish = [&](int h) {
        if (h) { TS(2); TSR(14); LS(2); TR(TP + 7); } else TS(8);
#pragma unroll
        for (int k = 0; k < 4; ++k) {
          const int u = k ^ COLS;
          if (C::SPLIT) break;
          f32x4 v[2];
#pragma unroll
          for (int e = 0; e < 2; ++e) {
            const f32x4 o = h && PRE ? pre[u][e] : other(u, e, h);
            v[e] = PH == GA ? ht(u, e, h) * st[e][0] + o : ht(u, e, h) * o;
          }
          pv[u] = pair_rows(v[0], v[1]);
          if (h && C::SEAM == 2) keep(1, u, pv[u]);
          else st16(rw, out_at + (PH == GA ? h * (2 * D_ / 16 * 1024) + u * 1024 : h * 1024 + pair_at(u)), pv[u]);
          __builtin_amdgcn_sched_barrier(0);
        }
        if (!h) TS(9);
      };
      auto send = [&](int g) {
        const int k = g < 12 ? 0 : (g < 17 ? 1 : (g < 20 ? 2 : 3)), u = k ^ COLS, e = g - (0x14110c09 >> 8 * k & 255);
        if (!C::SPLIT || e < 0) return;
        if (e < 2) asm volatile("v_cvt_pk_bf16_f32 %0, %2, %3\n\tv_cvt_pk_bf16_f32 %1, %4, %5" : "=&v"(y[2 * e]), "=v"(y[2 * e + 1])
                                : "v"(ht(u, e, 0)[0]), "v"(ht(u, e, 0)[1]), "v"(ht(u, e, 0)[2]), "v"(ht(u, e, 0)[3]));
        if (e == 2) st16(rw, out_at + u * 1024, (u32x4){y[0], y[1], y[2], y[3]});
      };
      auto flag = [&] { __builtin_amdgcn_raw_buffer_store_b64(__builtin_bit_cast(u32x2, tag), rw, flag_at, 0, 16); };
      half(TWO, TWO, 1, 0, none);
      WAIT_L(0);
      if (PH == G2 || PH == K5) TS(15);
      TR(TP + 1);
      stepm(TWO, 0, [&](int i) {
        if (i == 15 && !C::SPLIT) tag = tag_of(smem);
        if (PH == GA && i >= 16 && i % 4 == 2) st16(mk_rsrc(p.ws + O_XG + ((blockIdx.x ^ 1) * 4 + wid) * 4096, l ? 0 : 4096), lane * 16 + (i / 4 - 4) * 1024, (u32x4){~0u, ~0u, ~0u, ~0u});
      });
      TR(TP + 2); stepm(TWO, 0, [&](int i) { if (i >= 16 && i % 4 == 2 && !C::SPLIT) residual(i / 4 - 4); });
      TR(TP + 3);
      BARRIER();
      __builtin_amdgcn_sched_barrier(0);
      half(TWO, TWO, 0, 0, [&](int i) { operands(0, i); send(i); });
      if (!C::SPLIT) finish(0);
      half(TWO, TWO, 1, 1, [&](int i) { send(i + 16); });
      WAIT_L(0);
      TR(TP + 4); stepm(TWO, 1, none);
      constexpr bool LATER = FLAGL >> PH & 1;
      if (!C::SPLIT && !LATER) __builtin_amdgcn_s_waitcnt(0x0f70);
#define PV_GAP 8
      TR(TP + 5);
      stepm(TWO, 1, [&](int i) {
        if (i == 0 && !C::SPLIT && !LATER) flag();
        if (i >= 28) operands(1, i - 28);
        if (C::SPLIT && i >= PV_GAP && i < PV_GAP + 8 && (i - PV_GAP) % 2 == 0) pv[(i - PV_GAP) / 2] = ld16<16>(rw, flag_at + (i - PV_GAP) * 512);
      });
      if (LATER) __builtin_amdgcn_s_waitcnt(0x0f70);
      TR(TP + 6);
      BARRIER();
      __builtin_amdgcn_sched_barrier(0);
      half(TWO, IC<0>{}, 0, 1, [&](int i) {
        if (i == 0 && LATER) flag();
        if (PRE && !C::SPLIT && i % 2) { pre[i >> 2][i >> 1 & 1] = other(i >> 2, i >> 1 & 1, 1); asm volatile("" : "+v"(pre[i >> 2][i >> 1 & 1])); }
        if (PH == K5 && i == 1) locate();
        if (PH == K5 && i == 5) { fin_at = ((m0 + (lane & 15)) * D_ + n0) * 2 + (lane & 48); asm volatile("" : "+v"(fin_at)); } if (!C::SPLIT && C::SEAM == 2 && i >= KEEP0 && i < KEEP0 + 4 * KEEPD && i % KEEPD == 0) keep(0, (i - KEEP0) / KEEPD, pv[(i - KEEP0) / KEEPD]); });
      finish(1);
    }
  }
  }

  asm volatile("" : "+v"(lane));
  const int r = lane & 15, j = lane >> 4;
  if (PH == G2 || PH == K5) {
    {
      TS(10);
      auto there = [&] {
        unsigned mx = 0;
#pragma unroll
        for (int u = 0; u < 4; ++u) mx = max(max(max(max(mx, pv[u][0]), pv[u][1]), pv[u][2]), pv[u][3]);
        return __all(mx != 0xffffffffu);
      };
      if (__builtin_expect(!there(), 0)) {
#pragma unroll 1
        for (unsigned spin = 0; spin < SPIN_MAX; ++spin) {
          asm volatile("" ::: "memory");
#pragma unroll
          for (int u = 0; u < 4; ++u) pv[u] = ld16<16>(rw, flag_at + u * 1024);
          if (there()) break;
        }
      }
      TS(11);
    }
    float low = __builtin_bit_cast(float, 0x3f80u);
    PIN_S(low);
    auto add = [&](int u) {
#pragma unroll
      for (int i = 0; i < 8; ++i) {
        const unsigned w = pv[u][i / 2];
        if (DOTADD) ht(u, i / 4)[i % 4] = dot2(__builtin_bit_cast(float, w), i % 2 ? 1.0f : low, ht(u, i / 4)[i % 4]);
        else ht(u, i / 4)[i % 4] += i % 2 ? bf_hi(w) : bf_lo(w);
      }
    };
    if (PH == K5) {
#pragma unroll
      for (int u = 0; u < 4; ++u) { add(u); asm volatile("" : "+v"(ht(u, 0)), "+v"(ht(u, 1))); }
    }
    if (PH == K5 && l == 1) {
      const rsrc_t ro = mk_rsrc(p.out, S_ * D_ * 2);
#pragma unroll
      for (int i = 0; i < 4; ++i) {
        __builtin_amdgcn_raw_buffer_store_b128(pair_cols(ht(i & 2, i & 1), ht((i & 2) + 1, i & 1)), ro, fin_at + (i >> 1) * 64 + (i & 1) * 16 * D_ * 2, 0, 0);
        __builtin_amdgcn_sched_barrier(0);
      }
      if (blockIdx.x == 0 && wid == 0 && lane == 0) __hip_atomic_store((u64*)(p.ws + O_EP), tag_of(smem), RLX_AGENT);
      TS(3);
    } else {
      u32x4 y[4];
#pragma unroll
      for (int u = 0; u < 4; ++u) { if (PH == G2) add(u); y[u] = pair_rows(ht(u, 0), ht(u, 1)); st16(rw, x_at + pair_at(u), y[u]); __builtin_amdgcn_sched_barrier(0); }
      asm volatile("" ::: "memory");
      float sm[2] = {}, q[2] = {};
#pragma unroll
      for (int u = 0; u < 4; ++u) add_stat(y[u], sm[u >> 1], q[u >> 1]);
      if (PH == G2) {
        *(LAS f32x2*)(smem + PS_OFF + (wn * 128 + ml + lane) * 8) = (f32x2){join2(sm[0], sm[1]), join2(q[0], q[1])};
        asm volatile("" ::: "memory");
        if (lane == 0) __hip_atomic_fetch_add((LAS unsigned*)(smem + CNT_OFF) + l * 8 + 4, 1u, __ATOMIC_RELAXED, __HIP_MEMORY_SCOPE_WORKGROUP);
        TS(3);
        t = Tile<C>();
        if (!ZEARLY || t.ln == 7) signal_done<0, false>(smem, l * 8 + 2, flags(wl, F_G2, t.bm) + t.bn * 2 + t.kh, nullptr);
#pragma unroll
        for (int u = 0; u < 4; ++u) { if (ZEARLY == 1) rv[u] = y[u]; else keep(0, u, y[u]); }
      } else {
        const f32x2 ps = {sum32(sm[0] + sm[1]), sum32(q[0] + q[1])};
        if (lane < 32) __hip_atomic_store(stat_slot(wl + LSTRIDE + O_STX, n0 >> 6, m0 + lane), __builtin_bit_cast(u64, ps), RLX_AGENT);
        TS(3);
        signal_done<1, true>(smem, l * 8 + 5, flags(wl, F_K5, t.bm) + t.bn * 2 + t.kh, flags(wl + LSTRIDE, F_X0, t.bm) + t.bn * 2 + t.kh);
      }
    }
  } else {
    TS(3);
  }
  TS(6); TSR(5); TS_OUT(PH); LS(3); TR(PH == ZZ ? 20 : TP + 8);
}

template <int N> __device__ __forceinline__ f32x4 sum_of(const f32x4* g) {
  f32x4 a = g[0];
#pragma unroll
  for (int i = 1; i < N; ++i) {
    a += g[i];
  }
  return a;
}
template <int N> __device__ __forceinline__ bool written(const f32x4* g) {
  unsigned mx = 0;
#pragma unroll
  for (int i = 0; i < N; ++i) { const u32x4 u = __builtin_bit_cast(u32x4, g[i]); mx = max(max(mx, max(u[0], u[1])), max(u[2], u[3])); }
  return __all(mx != 0xffffffffu);
}
template <int N, int W> __device__ __forceinline__ f32x2 row_stat(const f32x4 (&g)[8]) {
  const f32x4 a = sum_of<N>(g);
  const float mu = (a[0] + a[2]) * (1.0f / W), v = __builtin_fmaf(-mu, mu, (a[1] + a[3]) * (1.0f / W));
  return (f32x2){mu, (v < 0.f ? 0.f : v) + LN_EPS};
}

template <class C> __device__ __forceinline__ Flag* act_flag(char* wl) {
  const Tile<C> t;
  if (C::PH == GA) return flags(wl, F_K5, t.bm);
  if (C::PH == G2) return flags(wl, F_GA, t.kh * 8 + t.bn);
  if (C::PH == ZZ) return flags(wl, F_G2, t.bm);
  if (C::PH == K4) return flags(wl, F_Z, t.bn);
  return flags(wl, F_K4, t.bm);
}
template <class C>
__device__ __forceinline__ void act_phase(LAS char* smem, const Params& p, int l, int w, int lane, Lanes (&av)[5]) {
  constexpr int PH = C::PH;
  const int wid = 6 + w;
  TS_DECL; TS(0);
  asm volatile("" : "+v"(lane));
  char* wl = p.ws + l * LSTRIDE;
  using St = Stream<C, 1 - C::WSIDE>;
  const int own = PH == GA || (PH == K5 && (blockIdx.x & 56) == 56) ? 0 : (PH == ZZ ? 1 : (PH == K4 ? 2 : C::SEAM));
  St st(PH == GA && l == 0 ? p.x : wl + (PH == GA ? O_X : (PH == G2 ? O_AB : (PH == ZZ ? O_X1 : (PH == K4 ? O_Z2 : O_H)))),
                             S_ * 2 * (PH == G2 ? 2 * D_ : (PH == K4 || PH == K5 ? FH : D_)), av[PH], l, w, C::A0, C::SPLIT && C::SEAM == 1 ? 0 : own);
  const unsigned fo = (lane & 15) * sizeof(Flag), all = PH == K5 ? 0x7fu << (blockIdx.x & 1) * 7 : (PH == GA && l == 0 ? 0 : 0xffffu);
#define POLL_WAIT 6
#define POLL_ZZ 3
#define POLL_K4 2
#define POLL_G2 2
#define ZCNT 4
#define IDLE_GAP 8
  {
    const u64 tag = tag_of(smem);
    Flag* fl = act_flag<C>(PH == GA ? p.ws : wl);
    u64 seen;
    if (PH == K4 || (PH == GA && all)) wait_count(smem, PH == GA ? 5 : l * 8, 4);
    if (PH == K5 && (blockIdx.x & 56) == 56) poll_flags<IDLE_GAP>(tag, fl, fo, st.writers(1));
    else if (all) {
      if (PH == ZZ && !ZPOLL) wait_count(smem, l * 8 + 2, 4);
      if (C::SPLIT && C::SEAM == 1) st.put(smem);
      __builtin_amdgcn_s_sleep(PH == GA ? POLL_WAIT : (PH == ZZ ? POLL_ZZ : (PH == K4 ? POLL_K4 : (PH == K5 ? POLL_K5 : POLL_G2))));
    }
    int bars = C::SPLIT ? (own ? C::SEAM + 1 : 0) : own;
    unsigned need = all ? st.writers(4) : 0;
    if (PH == ZZ && ZPOLL) {
#pragma unroll 1
      while (need && *((const volatile LAS unsigned*)(smem + CNT_OFF) + l * 8 + ZCNT) < 4) need &= (unsigned)__builtin_amdgcn_ballot_w64(peek(fl[lane & 15].v) != tag);
    }
    if (need) bars = poll_past(tag, fl, fo, need, bars);
    TS(1); TSR(9);
#pragma unroll 1
    while (st.n < 3 + own / 2) {
      st.put(smem);
      if (st.n == 3) {
#pragma unroll 1
        for (; bars; --bars) BARRIER();
      }
    }
    TSR(11);
    auto ask = [&] { ld8_async(seen, fl, fo); };
    auto check = [&] {
      LANDED1(st.NP, seen);
      const unsigned miss = (unsigned)__builtin_amdgcn_ballot_w64(seen != tag) & all;
      if (miss) poll_flags<0>(tag, fl, fo, miss);
    };
    if (own) { WAIT_V(st.NP); st.template step<0>(smem, [] {}); }
    else {
      WAIT_V(2 * st.NP);
      BARRIER();
      WAIT_V(st.NP);
    }
#define PROG 8
    if (PROG >> PH & 1) {
      unsigned miss;
#pragma unroll 1
      do {
        st.template step<1>(smem, ask);
        st.template step<0>(smem, [&] {
          LANDED1(st.NP, seen);
          miss = (unsigned)__builtin_amdgcn_ballot_w64(seen != tag) & all;
          const unsigned now = st.n < st.T - 4 ? miss & st.writers(2) : miss;
          if (now) poll_flags<0>(tag, fl, fo, now);
          miss -= now;
        });
      } while (miss);
    } else {
      st.template step<1>(smem, ask);
      st.template step<0>(smem, check);
    }
    if (PH == K5) {
      fl += (F_KB - F_K4) * 256;
      if (!own) st.template step<0>(smem, [] {});
      st.template step<1>(smem, ask);
      st.template step<0>(smem, check);
    }
    TS(2);
    if (PH == ZZ) {
      const Tile<C> t;
      const int row = w * 64 + lane;
      const rsrc_t rw = mk_rsrc(p.ws, O_EP);
      f32x4 g[8];
      st.template step<1>(smem, [&] { ld8_async(seen, flags(wl, F_S1 + w, t.bm), fo); });
      st.template step<8>(smem, [&] {
        LANDED1(st.NP, seen);
        wait_flags(tag, flags(wl, F_S1 + w, t.bm) + (lane & 15), seen);
#pragma unroll
        for (int i = 0; i < 8; ++i) ld16_async(g[i], wl + O_ST1 + (i * S_ + t.bm * 128) * 16, row * 16);
      });
      st.template step<0>(smem, [] {});
      f32x4 rows[8];
      st.template step<0>(smem, [&] { LANDED8(63, g); rows[0] = sum_of<8>(g); asm volatile("" : "+v"(rows[0])); });
      auto sums = [&] {
#pragma unroll
        for (int i = 0; i < 8; ++i) ld16_async(g[i], p.ws + O_SI + (w * FH + t.bn * 128 + (i & 3) * F_) * 16 + (i >> 2) * 1024, lane * 16);
      };
#pragma unroll 1
      while (st.n < 15) st.template step<0>(smem, [] {});
      BARRIER();
      st.put(smem);
      sums();
      WAIT_V(16);
      BARRIER();
      {
        asm volatile("" : "+v"(rows[0]));
        const f32x2 m = row_stat<1, D_>(rows);
        const float rs = __builtin_amdgcn_rsqf(m[1]);
        *(LAS f32x4*)(smem + ST_OFF + row * 16) = (f32x4){-m[0], GC1 * rs, GC2 * rs * rs * rs, m[1] * rs};
      }
      WAIT_VL(8);
      BARRIER();
      if (l == 0) {
        f32x2 v;
#pragma unroll 1
        for (unsigned spin = 0; spin < SPIN_MAX; ++spin) {
          LANDED8(0, g);
          const f32x4 a = sum_of<4>(g), b = sum_of<4>(g + 4);
          v = (f32x2){(a[0] + a[1]) + (a[2] + a[3]), (b[0] + b[1]) + (b[2] + b[3])};
          if (__all(v[0] + v[1] == v[0] + v[1]) || written<8>(g)) break;
          sums();
        }
#pragma unroll
        for (int i = 0; i < 2; ++i) *(LAS float*)(smem + CS3_OFF + w * 512 + i * 256 + lane * 4) = v[i];
        WAIT_L0();
      }
      BARRIER();
      LANDED8(0, g);
      const LAS float* zs = (const LAS float*)(smem + ZS_OFF) + row * 4;
      const unsigned xo = l * LSTRIDE + O_XCH + ((t.bn >> 1) * S_ + t.bm * 128) * 16 + (t.bn & 1) * 8;
      __builtin_amdgcn_s_setprio(3);
      BARRIER();
      const u32x2 q = __builtin_bit_cast(u32x2, (f32x2){zs[1024] + zs[1536], zs[row & 3] + zs[512 + (row & 3)]});
      __builtin_amdgcn_raw_buffer_store_b64((u32x2){min(q[0], 0xfffffffeu), min(q[1], 0xfffffffeu)}, rw, row * 16, xo, 16);
      auto gather = [&] {
#pragma unroll
        for (int i = 0; i < 7; ++i) ld16_async(g[i], wl + O_XCH + (i * S_ + t.bm * 128) * 16, row * 16);
      };
#define ZASK 3
      BARRIER();
      if (ZASK == 1) gather();
      BARRIER();
      if (ZASK == 2) gather();
      BARRIER();
      if (ZASK == 3) gather();
      f32x2 m;
#pragma unroll 1
      for (unsigned spin = 0; spin < SPIN_MAX; ++spin) {
        LANDED8(0, g);
        m = row_stat<7, FH>(g);
        if (__all(m[1] == m[1]) || written<7>(g)) break;
        gather();
      }
      const float rs = __builtin_amdgcn_rsqf(m[1]);
      *(LAS f32x2*)(smem + PS_OFF + row * 16) = (f32x2){rs, -m[0] * rs};
      WAIT_L0();
      *((volatile LAS unsigned*)(smem + CNT_OFF) + 16 + l * 2 + w) = 1u;
      __builtin_amdgcn_s_setprio(0);
    } else st.finish(smem, [&](int k) {
      if (l) return;
      if (PH == GA && k == 0) av[G2] = Stream<CG2, 1>::lanes(false, w, lane);
      if (PH == G2 && k == 0) { av[ZZ] = Stream<CZZ, 0>::lanes(false, w, lane); av[K4] = Stream<CK4, 1>::lanes(false, w, lane); }
      if (PH == G2 && k == 1) { av[K5] = Stream<CK5, 0>::lanes(false, w, lane); av[GA] = Stream<CGA, 0>::lanes(false, w, lane); }
    });
  }
}

__device__ __forceinline__ const char* win_stage(const Params& p, int e) {
  const int x = blockIdx.x & 7, s = e / 14 * 4 + (x >> 1), q = s >> 1, c = e % 14, b = q ? (q < 9 ? q - 1 : q - 9 + (q - 9 >= c / 2)) : c / 2;
  return p.win + (b * 128 + ((x ^ (q && q < 9)) & 1) * 32) * (F_ * 2) + (s & 1) * (FH * 2) + ((x & 1) * 14 + c) * 128;
}
__device__ __forceinline__ unsigned win_lane(int lane) { return (lane + (lane & 32)) * (F_ * 2); }
__device__ __forceinline__ void touch(unsigned& junk, const char* base, unsigned off) { asm volatile("s_nop 4\n\tglobal_load_dword %0, %1, %2 sc1" : "+v"(junk) : "v"(off), "s"(base) : "memory"); }
template <class C> __device__ __forceinline__ Stream<C, C::WSIDE> wt_stream(const Params& p, const Lanes (&wv)[5], int l, int w, unsigned slot, int n) {
  constexpr int PH = C::PH;
  const Tile<C> t;
  return Stream<C, C::WSIDE>(PH == GA ? (t.bn >= 8 ? p.wdim : p.wdre) : (PH == G2 ? (t.kh ? p.wsim : p.wsre) : (PH == ZZ ? p.win : (PH == K4 ? p.wsp : p.wout))),
                             2 * (PH == GA ? D_ * D_ : (PH == ZZ ? D_ * F_ : (PH == K5 ? FH * D_ : S_ * S_))), wv[PH], PH == GA || PH == ZZ || PH == G2 ? l : 0, w, slot, n);
}
template <class C, class NEXT>
__device__ __forceinline__ void wt_phase(LAS char* smem, const Params& p, int l, int w, int lane, unsigned& wpos, bool mid, Lanes (&wv)[5]) {
  constexpr int PH = C::PH, AH = Stream<C, C::WSIDE>::AHEAD % 10;
  asm volatile("" : "+v"(lane));
  const Tile<C> t;
  char* wl = p.ws + l * LSTRIDE;
  auto st = wt_stream<C>(p, wv, l, w, wpos, (PH == GA && l == 0) || (PH == K5 && !mid) ? 0 : AH);
  const int row = w * 64 + lane;
  const unsigned fo = (lane & 15) * sizeof(Flag);
  const rsrc_t rw = mk_rsrc(p.ws, O_EP);
  u64 seen;
  f32x4 g[8];
  using SA = Stream<C, 1 - C::WSIDE>;
  auto late = [&](int i0, int i1) {
    const unsigned at = (PH == G2 ? O_AB + t.bn * 8192 + (t.kh * 32 + t.bm * 2 + 1) * SA::KS : O_H + t.bm * 8 * SA::PITCH * 1024 + (t.kh * 7 + t.ln) * 4096 + 1024) + (w ^ t.kh ^ 1) * SA::HALF;
#pragma unroll
    for (int i = i0; i < i1 && i < 8; ++i)
      __builtin_amdgcn_raw_buffer_store_b128(__builtin_bit_cast(u32x4, g[i]), mk_rsrc(wl, PH == G2 || mid ? LSTRIDE : 0), lane * 16,
                                             at + (i & 1) * (PH == G2 ? 1024 : 2048) + (i >> 2) * SA::P4 + (i >> 1 & 1) * (PH == G2 ? 4096 : SA::PITCH * 1024), STAUX);
  };
  if (PH == GA && l == 0) {
    st.fill(smem, W0); WAIT_V(8 * W0 - 8); BARRIER();
    st.fill(smem, W1);
    auto fetch = [&] {
#pragma unroll
      for (int i = 0; i < 4; ++i) {
        const u32x4 o = *(const LAS u32x4*)(smem + ZS_OFF + i * 1024 + lane * 16);
        wv[i + 1] = {{o[0], o[1], o[2], o[3]}, *(const LAS unsigned*)(smem + ZS_OFF + 4096 + i * 256 + lane * 4) + w * (i & 1 ? Stream<CZZ, 1>::HALF : Stream<CG2, 0>::HALF), 0};
        asm volatile("" : "+v"(wv[i + 1].vo[0]), "+v"(wv[i + 1].vo[1]), "+v"(wv[i + 1].vo[2]), "+v"(wv[i + 1].vo[3]), "+v"(wv[i + 1].tab));
      }
    };
    if (WTAB == 2) fetch();
    if (WTAB < 2) wv[G2] = Stream<CG2, 0>::lanes(false, w, lane);
    if (!WTAB) { wv[ZZ] = Stream<CZZ, 1>::lanes(false, w, lane); wv[K4] = Stream<CK4, 0>::lanes(false, w, lane); wv[K5] = Stream<CK5, 1>::lanes(false, w, lane); }
    WAIT_V(8 * W1 - 16); BARRIER();
    if (LAT_S) { st.fill(smem, 6); if (WTAB == 3) fetch(); WAIT_V(24); BARRIER(); }
    const int c = (t.bm + t.bn) & 15;
    if (w) {
#pragma unroll
      for (int i = 0; i < 2; ++i) __builtin_amdgcn_raw_buffer_store_b32(*(const LAS unsigned*)(smem + CP_OFF + i * 256 + lane * 4), rw, lane * 16 + i * 1024, O_SD + ((c >> 2) * 2048 + t.bn * 128) * 16 + (c & 3) * 4, 16);
      const unsigned d = (lane >> 1 & 7) - (lane >> 4) * 4, one = 0x3f80u << (lane & 1) * 16;
      asm volatile("" ::: "memory");
      *(LAS u32x4*)(smem + CP_OFF + lane * 16) = (u32x4){d == 0 ? one : 0, d == 1 ? one : 0, d == 2 ? one : 0, d == 3 ? one : 0};
    } else {
#pragma unroll
      for (int i = 0; i < 2; ++i) __builtin_amdgcn_raw_buffer_store_b64((u32x2){*(const LAS unsigned*)(smem + ST_OFF + i * 256 + lane * 4), *(const LAS unsigned*)(smem + PS_OFF + i * 1024 + lane * 16 + (lane & 3) * 4)}, rw, lane * 16 + i * 1024, O_STX + ((c >> 1) * S_ + t.bm * 128) * 16 + (c & 1) * 8, 16);
    }
    if (!LAT_S) { st.fill(smem, 6); WAIT_V(26); BARRIER(); }
    st.fill(smem, 8); WAIT_V(34); BARRIER();
  } else {
    if (PH == K5) st.fill(smem, AH);
    if (PH != ZZ) st.resume(smem, [&] {
      if (!C::SPLIT || C::SEAM < 2) return;
      LAS char* from = smem + A_RING + 16384 + w * 8192 + lane * 16;
      asm volatile("" : "+v"(from));
#pragma unroll
      for (int i = 0; i < 8; ++i) g[i] = *(const LAS f32x4*)(from + i * 1024);
      late(0, PH == G2 ? LATE_G : LATE_K);
    });
  }
  u64 tag = tag_of(smem);
  asm volatile("" : "+v"(tag));
  unsigned junk = 0;
  if (PH == ZZ) {
    WAIT_VL((AH - 2) * st.NP + (ZEARLY ? 1 : 0));
    BARRIER();
#pragma unroll 1
    for (int k = 6; k <= 32; k += k < 30 ? 2 : 1) {
      st.fill(smem, k); WAIT_V(2 * st.NP);
      if (ZEARLY && k == 6 && lane == 0) __hip_atomic_store(flags(wl, F_S1 + w, t.bm)[t.ln * 2 + (blockIdx.x & 1)].v, tag, RLX_AGENT);
      BARRIER();
    }
    auto nx = wt_stream<NEXT>(p, wv, l, w, st.slot, 0);
#pragma unroll 1
    for (int k = 0x54331; k; k >>= 4) { nx.fill(smem, k & 15); WAIT_V(3 * st.NP); BARRIER(); }
  } else {
    f32x4 c[4];
    if (PH == G2) {
      Flag* fl = flags(wl, F_GB, 0);
      const int fi = lane == 16 ? ((t.kh ^ 1) * 8 + t.bn) * 16 + t.bm : (t.kh * 8 + t.bn) * 16 + (lane & 15);
      if (SEAM_G < 2) st.go(smem, 4);
      else {
#pragma unroll
        for (int k = 1; k * LATE_G < 8; ++k) { late(k * LATE_G, (k + 1) * LATE_G); st.go(smem, k + 1); }
        st.template go<LATE_G == 8 && AH == 6 ? -24 : -16>(smem, (7 + LATE_G) / LATE_G + 1);
      }
      const Tile<CGA> ta;
      if (w == 0 && lane == 0) __hip_atomic_store(flags(wl, F_GB, ta.bn)[ta.bm].v, tag, RLX_AGENT);
      constexpr int GB = (SEAM_G < 2 ? 7 : 8) + (LEADMASK & 2 ? 0 : 4);
      st.go(smem, GB);
      ld8_async(seen, fl, fi * sizeof(Flag));
      st.go(smem, GB + 2);
      LANDED1(16, seen);
      wait_flags(tag, fl + fi, seen);
      if (TZ && l == 0 && !mid) {
#pragma unroll 1
        for (int e = 0; e < TZN; e += 8) { st.go(smem, TZ + e / TZD); touch(junk, win_stage(p, e + (blockIdx.x >> 6) * 2 + w), win_lane(lane)); }
      }
    } else if (PH == K5) {
      if (SEAM_K < 2) st.go(smem, 2);
      else {
#pragma unroll
        for (int k = 1; k * LATE_K < 8; ++k) { late(k * LATE_K, (k + 1) * LATE_K); st.go(smem, k + 1); }
        st.template go<LATE_K == 8 && AH == 6 ? -24 : -16>(smem, (7 + LATE_K) / LATE_K + 1);
      }
      const Tile<CK4> tk;
      if (mid && w == 0 && lane == 0) __hip_atomic_store(flags(wl, F_KB, tk.bm)[tk.bn].v, tag, RLX_AGENT);
      ld8_async(seen, flags(wl, F_RD, t.bm), (lane & 1) * sizeof(Flag));
      st.go(smem, (7 + LATE_K) / LATE_K + 3);
      LANDED1(16, seen);
      wait_flags(tag, flags(wl, F_RD, t.bm) + (lane & 1), seen);
    } else if (PH == GA) {
      if (l == 0) {
        if (LAT_S) { st.go(smem, 4); WAIT_V(24); } else WAIT_V(32);
        const int own = (F_X0 * 16 + t.bm) * 16 + t.bn + w * ((F_WD - F_X0) * 256 + (t.bn - t.bm) * 15);
        if (lane == 0) __hip_atomic_store(flags(wl, 0, 0)[own].v, tag, RLX_AGENT);
        __builtin_amdgcn_raw_buffer_store_b128((u32x4){~0u, ~0u, ~0u, ~0u}, rw, lane * 16, w * LSTRIDE + O_XCH + blockIdx.x * 1024, 16);
        if (w) __builtin_amdgcn_raw_buffer_store_b128((u32x4){~0u, ~0u, ~0u, ~0u}, rw, lane * 16, O_SI + blockIdx.x * 1024, 16);
        if (TA) {
          const Tile<CG2> u;
          const int j = (lane & 7) - 2;
          if (TA > 4 || TAM) st.go(smem, TA + (t.cls & TAM));
          touch(junk, (u.kh ? p.wsim : p.wsre) + (u.bm * 128 + u.bn * 16 + w * 8) * (S_ * 2), (lane >> 3) * (S_ * 2) + (j < 0 ? u.bm * 256 + (lane & 1) * 128 : (j + (j >= u.bm)) * 256));
        }
      }
      const unsigned fa = ((lane & 16) && l == 0 ? F_WD * 16 + t.bn : F_X0 * 16 + t.bm) * 16 * sizeof(Flag) + fo;
      st.go(smem, l ? GS1 : GS0);
      ld8_async(seen, wl + O_SF, fa);
      st.go(smem, (l ? GS1 : GS0) + 2);
      LANDED1(16, seen);
      wait_flags(tag, (Flag*)(wl + O_SF + fa), seen);
      if (GL) { st.go(smem, 9); st.put(smem); WAIT_V(24); BARRIER(); st.put(smem); }
      if (l == 0) {
#pragma unroll
        for (int i = 0; i < 4; ++i) ld16_async(c[i], p.ws + O_SD + (i * 2048 + t.bn * 128) * 16, row * 16);
      }
#pragma unroll
      for (int i = 0; i < 8; ++i) ld16_async(g[i], wl + O_STX + (i * S_ + t.bm * 128) * 16, row * 16);
    }
    if (PH == GA && GL) { WAIT_V(24); BARRIER(); } else
    st.close(smem);
    if (WTAB == 1 && PH == G2 && l == 0) wv[ZZ] = Stream<CZZ, 1>::lanes(false, w, lane);
    if (PH == GA) {
      LANDED8(GL ? 0 : 16, g);
      if (l == 0) {
        asm volatile("" : "+v"(c[0]), "+v"(c[1]), "+v"(c[2]), "+v"(c[3]));
        const f32x4 a = sum_of<4>(c);
        *(LAS float*)(smem + CS1_OFF + row * 4) = (a[0] + a[1]) + (a[2] + a[3]);
      }
      g[0] = sum_of<8>(g);
      asm volatile("" : "+v"(g[0]));
    }
    st.template skip<0, 4>();
    if (WTAB == 1 && PH == G2 && l == 0) { wv[K4] = Stream<CK4, 0>::lanes(false, w, lane); wv[K5] = Stream<CK5, 1>::lanes(false, w, lane); }
    if (PH == GA) {
      asm volatile("" : "+v"(g[0]));
      const f32x2 m = row_stat<1, D_>(g);
      const float rs = __builtin_amdgcn_rsqf(m[1]);
      *(LAS f32x2*)(smem + ST_OFF + row * 16) = (f32x2){rs, -m[0] * rs} * (t.bn >= 8 ? -1.0f : 1.0f);
    }
    asm volatile("" : "+v"(junk));
    st.template skip<0, 4>();
    if (PH == G2 ? mid : PH != K5 || l == 0) wt_stream<NEXT>(p, wv, PH == K5 ? 1 : l, w, st.slot, 0).open(smem);
    else { BARRIER(); BARRIER(); BARRIER(); }
    if (PH == G2) {
      wait_count(smem, l * 8 + 4, 4);
      const f32x2 q = *(const LAS f32x2*)(smem + PS_OFF + row * 8) + *(const LAS f32x2*)(smem + PS_OFF + (128 + row) * 8);
      __hip_atomic_store(stat_slot(wl + O_ST1, t.bn * 2 + t.kh, t.bm * 128 + row), __builtin_bit_cast(u64, q), RLX_AGENT);
      if (!ZEARLY || !mid) {
        WAIT_V(0);
        if (lane == 0) __hip_atomic_store(flags(wl, F_S1 + w, t.bm)[t.bn * 2 + t.kh].v, tag, RLX_AGENT);
      }
    }
  }
  wpos = st.slot;
}

__device__ __forceinline__ void pull(const Params& p, int wid, int lane) {
  const int x = blockIdx.x & 7, u = (blockIdx.x >> 6) * 4 + wid;
  unsigned junk = 0;
#pragma unroll 1
  for (int e = TZN + u; e < 112 + 128 + 56; e += 16) {
    const int k = (e >> 4) - 7;
    const char* sp = PORD ? p.wsp + (x * 16 + (e & 15)) * (16 * S_ * 2) + (k ? k - (k <= x) : x) * 512 : p.wsp + (x * 128 + e - 112) * 8192;
    touch(junk, e < 112 ? win_stage(p, e) : (e < 240 ? sp : p.wout + (x * 56 + e - 240) * 8192), e < 112 ? win_lane(lane) : (PORD && e < 240 ? (lane >> 2) * (S_ * 2) + (lane & 3) * 128 : lane * 128));
    WAIT_V(PDEP);
  }
  asm volatile("s_waitcnt vmcnt(0)" : "+v"(junk) :: "memory");
}

__device__ __forceinline__ void wipe(LAS char* smem, const Params& p, int l, int wid, int lane) {
  const int x = blockIdx.x & 7, q = blockIdx.x >> 6;
  char* wl = p.ws + l * LSTRIDE;
#define WIPES 80
  __builtin_amdgcn_s_sleep(WIPES);
#pragma unroll 1
  for (int e = 0; e < (l ? 32 : 64); ++e) {
    __builtin_amdgcn_raw_buffer_store_b128((u32x4){~0u, ~0u, ~0u, ~0u}, mk_rsrc(wl, 2 * LSTRIDE), lane * 16,
                                           (e < 32 ? O_XK : LSTRIDE + O_XG) + (((q * 8 + (e >> 2 & 7)) * 8 + x) * 4 + wid) * 4096 + (e & 3) * 1024, 16);
  }
  signal_done<0, false>(smem, l * 8, flags(wl, F_RD, (x >> 1) * 4 + q) + (x & 1), nullptr);
}

__global__ __launch_bounds__(512, 2) void mega_k(Params) {
  extern __shared__ __attribute__((aligned(16))) char smem_[];
  LAS char* smem = (LAS char*)smem_;
  {
    const char* pc; unsigned t, off;
    asm volatile("s_getpc_b64 %0\n.Lcode_head:\n\ts_lshr_b32 %1, %5, 3\n\ts_sub_u32 %1, %1, 1\n\tv_lshl_add_u32 %2, %1, 8, %4\n\tv_readfirstlane_b32 %1, %4\n\ts_mov_b32 m0, %3\n\ts_cmp_lt_u32 %1, 256\n\t"
                 "s_cbranch_scc1 .Lcode_done\n\ts_mov_b32 %1, .Lcode_tail-.Lcode_head\n\tv_lshlrev_b32 %2, 4, %2\n\tv_min_u32 %2, %1, %2\n\tglobal_load_lds_dword %2, %0\n.Lcode_done:"
                 : "=&s"(pc), "=&s"(t), "=&v"(off) : "n"(PS_OFF), "v"(threadIdx.x), "s"(blockIdx.x) : "m0", "scc", "memory");
  }
  const int lane = threadIdx.x & 63, wid = __builtin_amdgcn_readfirstlane(threadIdx.x >> 6);
  const bool mid = (blockIdx.x & 56) != 56;
  Params p = {};
#ifdef __HIP_DEVICE_COMPILE__
  const auto args = __builtin_amdgcn_kernarg_segment_ptr();
#else
  const void* args = nullptr;
#endif
  __attribute__((address_space(1))) char* ws;
#define ARG_WS asm volatile("s_load_dwordx2 %0, %1, %2" : "=&s"(ws) : "s"(args), "n"(__builtin_offsetof(Params, ws)))
#define ARG(f) asm volatile("s_load_dwordx2 %0, %1, %2" : "=&s"(p.f) : "s"(args), "n"(__builtin_offsetof(Params, f)))
  unsigned wpos = 0;
  if (wid < 4) {
    __builtin_amdgcn_s_waitcnt(0x0f70);
    asm volatile("s_nop 4" :: "s"(args));
    ARG(x); ARG(win); ARG(wsp); ARG(wout); ARG(bsp); ARG(out); ARG_WS;
    Spot sp[5] = {{}, spot_of<CG2>(smem, wid, lane, false), {}, spot_of<CK4>(smem, wid, lane, false), spot_of<CK5>(smem, wid, lane, false)};
    if (WTAB == 2) tables(smem, wid, lane);
    asm volatile("s_waitcnt lgkmcnt(0)" : "+s"(p.x), "+s"(p.win), "+s"(p.wsp), "+s"(p.wout), "+s"(p.bsp), "+s"(p.out), "+s"(ws));
    p.ws = (char*)ws;
    unsigned lsv = 0;
    { constexpr int l = 0, PH = 0; LS(0); }
#pragma unroll 1
    for (int l = 0; l < 2; ++l) {
      u32x4 zk[4][2];
      u32x4 rv[4];
      compute_phase<CGA>(smem, p, l, wid, lane, zk, rv, wpos, sp, lsv);
      compute_phase<CG2>(smem, p, l, wid, lane, zk, rv, wpos, sp, lsv);
      if (mid) { compute_phase<CZZ>(smem, p, l, wid, lane, zk, rv, wpos, sp, lsv); compute_phase<CK4>(smem, p, l, wid, lane, zk, rv, wpos, sp, lsv); }
      else if (PULL && l == 0) pull(p, wid, lane);
      compute_phase<CK5>(smem, p, l, wid, lane, zk, rv, wpos, sp, lsv);
    }
    asm volatile(".Lcode_tail:\n\ts_nop 0\n\ts_nop 0");
  } else if (wid < 6) {
    Lanes wv[5] = {};
    asm volatile("s_nop 4" :: "s"(args));
    ARG(wsre); ARG(wsim); ARG(wdre); ARG(wdim); ARG(win); ARG(wsp); ARG(wout); ARG_WS;
    wv[GA] = Stream<CGA, 1>::lanes(false, wid - 4, lane);
    asm volatile("s_waitcnt lgkmcnt(0)" : "+s"(p.wsre), "+s"(p.wsim), "+s"(p.wdre), "+s"(p.wdim), "+s"(p.win), "+s"(p.wsp), "+s"(p.wout), "+s"(ws));
    p.ws = (char*)ws;
#pragma unroll 1
    for (int l = 0; l < 2; ++l) {
      wt_phase<CGA, CG2>(smem, p, l, wid - 4, lane, wpos, mid, wv);
      wt_phase<CG2, CZZ>(smem, p, l, wid - 4, lane, wpos, mid, wv);
      if (mid) { wt_phase<CZZ, CK4>(smem, p, l, wid - 4, lane, wpos, mid, wv); wt_phase<CK4, CK5>(smem, p, l, wid - 4, lane, wpos, mid, wv); }
      else wipe(smem, p, l, wid - 4, lane);
      wt_phase<CK5, CGA>(smem, p, l, wid - 4, lane, wpos, mid, wv);
    }
  } else {
    Lanes av[5] = {};
    asm volatile("s_nop 4" :: "s"(args));
    ARG(x); ARG_WS;
    av[GA] = Stream<CGA, 0>::lanes(true, wid - 6, lane);
    asm volatile("s_waitcnt lgkmcnt(0)" : "+s"(p.x), "+s"(ws));
    p.ws = (char*)ws;
#pragma unroll 1
    for (int l = 0; l < 2; ++l) {
      act_phase<CGA>(smem, p, l, wid - 6, lane, av);
      act_phase<CG2>(smem, p, l, wid - 6, lane, av);
      if (mid) { act_phase<CZZ>(smem, p, l, wid - 6, lane, av); act_phase<CK4>(smem, p, l, wid - 6, lane, av); }
      else wipe(smem, p, l, wid - 4, lane);
      act_phase<CK5>(smem, p, l, wid - 6, lane, av);
    }
  }
}

extern "C" void kernel_launch(void* const* d_in, const int* in_sizes, int n_in, void* d_out, int out_size, void* d_ws, size_t ws_size, hipStream_t stream) {
  Params p;
  p.x = (const char*)d_in[0];
  p.wsre = (const char*)d_in[1]; p.wsim = (const char*)d_in[2]; p.wdre = (const char*)d_in[3]; p.wdim = (const char*)d_in[4];
  p.win = (const char*)d_in[6]; p.wsp = (const char*)d_in[7]; p.wout = (const char*)d_in[9]; p.bsp = (const u16*)d_in[8];
  p.out = (char*)d_out; p.ws = (char*)d_ws;
  (void)hipFuncSetAttribute((const void*)mega_k, hipFuncAttributeMaxDynamicSharedMemorySize, LDS_BYTES);
  hipLaunchKernelGGL(mega_k, dim3(256), dim3(512), LDS_BYTES, stream, p);
}
```
